```python
import torch

def _softmax(x, axis=-1):
    return torch.softmax(x, dim=axis)
def _gelu(x):
    return torch.nn.functional.gelu(x, approximate="tanh")
def _silu(x):
    return torch.nn.functional.silu(x)
def _sigmoid(x):
    return torch.sigmoid(x)
def _layernorm(x, axis=-1, eps=1e-5):
    m = x.mean(dim=axis, keepdim=True)
    v = x.var(dim=axis, keepdim=True, unbiased=False)
    return (x - m) / torch.sqrt(v + eps)
def _scatter_add(tgt, idx, src, axis=0):
    return tgt.clone().index_add_(axis, idx, src)
def _segment_sum(src, seg, n):
    out = torch.zeros((n,) + src.shape[1:], dtype=src.dtype)
    return out.index_add_(0, seg, src)
def _pad(x, pw):
    flat = tuple(v for pair in reversed(pw) for v in pair)
    return torch.nn.functional.pad(x, flat)

kernel_name = 'interleave_layers__compressed_kv_cla__swiglu_ffn__decode'

def setup_inputs():

    _w = (lambda a: (lambda t: t.to(torch.bfloat16) if t.is_floating_point() else t)(torch.as_tensor(a)))
    def _A_setup():
        B, H, Sq, Sk, D, M = 4, 32, 1, 4096, 128, 512
        Dc = 4 * D
        return {
            "x": _w((lambda *s: torch.randn(*s, dtype=torch.bfloat16) * 0.05)(B, Sq, M)),
            "c_kv": _w((lambda *s: torch.randn(*s, dtype=torch.bfloat16) * 1.6)(B, Sk, Dc)),
            "w_q": _w((lambda *s: torch.randn(*s, dtype=torch.bfloat16) * 1.6)(M, H, D) * (M ** -0.5)),
            "w_uk": _w((lambda *s: torch.randn(*s, dtype=torch.bfloat16) * 1.6)(Dc, H, D) * (Dc ** -0.5)),
            "w_uv": _w((lambda *s: torch.randn(*s, dtype=torch.bfloat16) * 1.6)(Dc, H, D) * (Dc ** -0.5)),
            "w_o": _w((lambda *s: torch.randn(*s, dtype=torch.bfloat16) * 1.6)(H, D, M) * ((H * D) ** -0.5)),
        }
    _A_in = _A_setup()

    def _B_setup():
        B, S, D, F = 4, 1, 512, 1792
        return {
            "x": _w((lambda *s: torch.randn(*s, dtype=torch.bfloat16) * 0.05)(B, S, D)),
            "w_gate": _w((lambda *s: torch.randn(*s, dtype=torch.bfloat16) * 0.05)(D, F)),
            "w_up": _w((lambda *s: torch.randn(*s, dtype=torch.bfloat16) * 0.05)(D, F)),
            "w_down": _w((lambda *s: torch.randn(*s, dtype=torch.bfloat16) * 0.05)(F, D)),
        }
    _B_in = _B_setup()

    return {
        "A_x": _A_in["x"],
        "A_c_kv": _A_in["c_kv"],
        "A_w_q": _A_in["w_q"],
        "A_w_uk": _A_in["w_uk"],
        "A_w_uv": _A_in["w_uv"],
        "A_w_o": _A_in["w_o"],
        "B_x": _B_in["x"],
        "B_w_gate": _B_in["w_gate"],
        "B_w_up": _B_in["w_up"],
        "B_w_down": _B_in["w_down"],
    }

def reference(A_x, A_c_kv, A_w_q, A_w_uk, A_w_uv, A_w_o, B_x, B_w_gate, B_w_up, B_w_down):
    def _A_ref(x, c_kv, w_q, w_uk, w_uv, w_o):
        B, Sq, M = x.shape[0], x.shape[1], x.shape[2]
        Sk = c_kv.shape[1]
        H, D = w_q.shape[1], w_q.shape[2]
        q = torch.einsum("bqm,mhd->bhqd", x, w_q)
        k = torch.einsum("bkc,chd->bhkd", c_kv, w_uk)
        v = torch.einsum("bkc,chd->bhkd", c_kv, w_uv)
        scores = torch.einsum("bhqd,bhkd->bhqk", q, k) * (D ** -0.5)
        off = Sk - Sq
        qi = torch.arange(Sq)[:, None]
        kj = torch.arange(Sk)[None, :]
        cmask = kj <= (qi + off)
        scores = torch.where(cmask[None, None], scores, -1e9)
        p = _softmax(scores, axis=-1)
        ctx = torch.einsum("bhqk,bhkd->bhqd", p, v)
        return torch.einsum("bhqd,hdm->bqm", ctx, w_o)

    def _B_ref(x, w_gate, w_up, w_down):
        g = _silu(x @ w_gate)
        u = x @ w_up
        return (g * u) @ w_down

    x = A_x
    x = x + _A_ref(_layernorm(x, axis=-1), A_c_kv, A_w_q, A_w_uk, A_w_uv, A_w_o)
    x = x + _B_ref(_layernorm(x, axis=-1), B_w_gate, B_w_up, B_w_down)
    x = x + _A_ref(_layernorm(x, axis=-1), A_c_kv, A_w_q, A_w_uk, A_w_uv, A_w_o)
    x = x + _B_ref(_layernorm(x, axis=-1), B_w_gate, B_w_up, B_w_down)
    return x
```

```cpp
#include <hip/hip_runtime.h>
#include <hip/hip_bf16.h>
#include <stdint.h>
typedef unsigned short u16;
__device__ __forceinline__ float bf2f(u16 b){ return __uint_as_float(((unsigned)b)<<16); }
__device__ __forceinline__ u16 f2bf(float f){ unsigned u=__float_as_uint(f); u += 0x7FFFu + ((u>>16)&1u); return (u16)(u>>16); }
constexpr int B=4,H=32,SK=4096,D=128,M=512,DC=512,F=1792;

__device__ float block_sum(float v, float* red){
  for(int o=32;o>=1;o>>=1) v += __shfl_xor(v,o);
  int w=threadIdx.x>>6; int nw=blockDim.x>>6;
  __syncthreads();
  if((threadIdx.x&63)==0) red[w]=v;
  __syncthreads();
  float s=0; for(int i=0;i<nw;i++) s+=red[i];
  return s;
}
__global__ void k_init(const u16* x, float* xf){ int i=blockIdx.x*blockDim.x+threadIdx.x; if(i<B*M) xf[i]=bf2f(x[i]); }
__global__ void k_qabs(const float* xf, const u16* wq, const u16* wuk, float* qabs){
  __shared__ float xn[M]; __shared__ float q[D]; __shared__ float red[16];
  int b=blockIdx.x/H, h=blockIdx.x%H, t=threadIdx.x;
  float v=xf[b*M+t];
  float mean=block_sum(v,red)/M; float dv=v-mean; float var=block_sum(dv*dv,red)/M;
  xn[t]=dv*rsqrtf(var+1e-5f);
  __syncthreads();
  if(t<D){ float s=0; for(int m=0;m<M;m++) s+=xn[m]*bf2f(wq[(size_t)m*H*D+h*D+t]); q[t]=s; }
  __syncthreads();
  float s=0; for(int d=0;d<D;d++) s+=q[d]*bf2f(wuk[(size_t)t*H*D+h*D+d]);
  qabs[(b*H+h)*DC+t]=s*0.08838834764831845f;
}
__global__ void k_attn(const float* qabs, const u16* ckv, const u16* wuv, float* ctx){
  __shared__ float qa[DC]; __shared__ float p[SK]; __shared__ float red[16]; __shared__ float cc[DC];
  int b=blockIdx.x/H, h=blockIdx.x%H, t=threadIdx.x;
  qa[t]=qabs[(b*H+h)*DC+t];
  __syncthreads();
  int w=t>>6, l=t&63;
  for(int k=w;k<SK;k+=8){
    const u16* row=ckv+((size_t)b*SK+k)*DC;
    float s=0; for(int j=0;j<8;j++) s+=qa[l*8+j]*bf2f(row[l*8+j]);
    for(int o=32;o>=1;o>>=1) s+=__shfl_xor(s,o);
    if(l==0) p[k]=s;
  }
  __syncthreads();
  float mx=-1e30f; for(int k=t;k<SK;k+=512) mx=fmaxf(mx,p[k]);
  for(int o=32;o>=1;o>>=1) mx=fmaxf(mx,__shfl_xor(mx,o));
  __syncthreads(); if(l==0) red[w]=mx; __syncthreads();
  mx=red[0]; for(int i=1;i<8;i++) mx=fmaxf(mx,red[i]);
  float sum=0; for(int k=t;k<SK;k+=512){ float e=__expf(p[k]-mx); p[k]=e; sum+=e; }
  sum=block_sum(sum,red);
  __syncthreads();
  float acc=0; for(int k=0;k<SK;k++) acc+=p[k]*bf2f(ckv[((size_t)b*SK+k)*DC+t]);
  cc[t]=acc/sum;
  __syncthreads();
  if(t<D){ float s=0; for(int c=0;c<DC;c++) s+=cc[c]*bf2f(wuv[(size_t)c*H*D+h*D+t]); ctx[(b*H+h)*D+t]=s; }
}
__global__ void k_oproj(const float* ctx, const u16* wo, float* xf){
  int b=blockIdx.x/8, m=(blockIdx.x%8)*64+threadIdx.x;
  float s=0; for(int i=0;i<H*D;i++) s+=ctx[b*H*D+i]*bf2f(wo[(size_t)i*M+m]);
  xf[b*M+m]+=s;
}
__global__ void k_ffn1(const float* xf, const u16* wg, const u16* wu, float* hh){
  __shared__ float xn[M]; __shared__ float red[16];
  int b=blockIdx.x/4, part=blockIdx.x%4, t=threadIdx.x;
  float v=xf[b*M+t];
  float mean=block_sum(v,red)/M; float dv=v-mean; float var=block_sum(dv*dv,red)/M;
  xn[t]=dv*rsqrtf(var+1e-5f);
  __syncthreads();
  if(t<448){ int f=part*448+t; float g=0,u=0; for(int m=0;m<M;m++){ g+=xn[m]*bf2f(wg[(size_t)m*F+f]); u+=xn[m]*bf2f(wu[(size_t)m*F+f]); }
    hh[b*F+f]=g/(1.f+__expf(-g))*u; }
}
__global__ void k_ffn2(const float* hh, const u16* wd, float* xf, u16* out){
  int b=blockIdx.x/8, m=(blockIdx.x%8)*64+threadIdx.x;
  float s=0; for(int i=0;i<F;i++) s+=hh[b*F+i]*bf2f(wd[(size_t)i*M+m]);
  float r=xf[b*M+m]+s; xf[b*M+m]=r; out[b*M+m]=f2bf(r);
}
extern "C" void kernel_launch(void* const* d_in, const int* in_sizes, int n_in, void* d_out, int out_size, void* d_ws, size_t ws_size, hipStream_t stream){
  const u16* x=(const u16*)d_in[0]; const u16* ckv=(const u16*)d_in[1]; const u16* wq=(const u16*)d_in[2]; const u16* wuk=(const u16*)d_in[3];
  const u16* wuv=(const u16*)d_in[4]; const u16* wo=(const u16*)d_in[5]; const u16* wg=(const u16*)d_in[7]; const u16* wu=(const u16*)d_in[8]; const u16* wd=(const u16*)d_in[9];
  float* ws=(float*)d_ws; float* xf=ws; float* qabs=ws+4096; float* ctx=qabs+B*H*DC; float* hh=ctx+B*H*D;
  k_init<<<8,256,0,stream>>>(x,xf);
  for(int l=0;l<2;l++){
    k_qabs<<<B*H,512,0,stream>>>(xf,wq,wuk,qabs);
    k_attn<<<B*H,512,0,stream>>>(qabs,ckv,wuv,ctx);
    k_oproj<<<B*8,64,0,stream>>>(ctx,wo,xf);
    k_ffn1<<<B*4,512,0,stream>>>(xf,wg,wu,hh);
    k_ffn2<<<B*8,64,0,stream>>>(hh,wd,xf,(u16*)d_out);
  }
}
```

```cpp
#include <hip/hip_runtime.h>
#include <hip/hip_bf16.h>
#include <stdint.h>
typedef unsigned short u16;
__device__ __forceinline__ float bf2f(u16 b){ return __uint_as_float(((unsigned)b)<<16); }
__device__ __forceinline__ u16 f2bf(float f){ unsigned u=__float_as_uint(f); u += 0x7FFFu + ((u>>16)&1u); return (u16)(u>>16); }
constexpr int B=4,H=32,SK=4096,D=128,M=512,DC=512,F=1792;

__device__ float block_sum(float v, float* red){
  for(int o=32;o>=1;o>>=1) v += __shfl_xor(v,o);
  int w=threadIdx.x>>6; int nw=blockDim.x>>6;
  __syncthreads();
  if((threadIdx.x&63)==0) red[w]=v;
  __syncthreads();
  float s=0; for(int i=0;i<nw;i++) s+=red[i];
  return s;
}
__global__ void k_init(const u16* x, float* xf){ int i=blockIdx.x*blockDim.x+threadIdx.x; if(i<B*M) xf[i]=bf2f(x[i]); }
__global__ void k_qabs(const float* xf, const u16* wq, const u16* wuk, float* qabs){
  __shared__ float xn[M]; __shared__ float q[D]; __shared__ float red[16];
  int b=blockIdx.x/H, h=blockIdx.x%H, t=threadIdx.x;
  float v=xf[b*M+t];
  float mean=block_sum(v,red)/M; float dv=v-mean; float var=block_sum(dv*dv,red)/M;
  xn[t]=dv*rsqrtf(var+1e-5f);
  __syncthreads();
  if(t<D){ float s=0; for(int m=0;m<M;m++) s+=xn[m]*bf2f(wq[(size_t)m*H*D+h*D+t]); q[t]=s; }
  __syncthreads();
  float s=0; for(int d=0;d<D;d++) s+=q[d]*bf2f(wuk[(size_t)t*H*D+h*D+d]);
  qabs[(b*H+h)*DC+t]=s*0.08838834764831845f;
}
__global__ void k_attn(const float* qabs, const u16* ckv, const u16* wuv, float* ctx){
  __shared__ float qa[DC]; __shared__ float p[SK]; __shared__ float red[16]; __shared__ float cc[DC];
  int b=blockIdx.x/H, h=blockIdx.x%H, t=threadIdx.x;
  qa[t]=qabs[(b*H+h)*DC+t];
  __syncthreads();
  int w=t>>6, l=t&63;
  for(int k=w;k<SK;k+=8){
    const u16* row=ckv+((size_t)b*SK+k)*DC;
    float s=0; for(int j=0;j<8;j++) s+=qa[l*8+j]*bf2f(row[l*8+j]);
    for(int o=32;o>=1;o>>=1) s+=__shfl_xor(s,o);
    if(l==0) p[k]=s;
  }
  __syncthreads();
  float mx=-1e30f; for(int k=t;k<SK;k+=512) mx=fmaxf(mx,p[k]);
  for(int o=32;o>=1;o>>=1) mx=fmaxf(mx,__shfl_xor(mx,o));
  __syncthreads(); if(l==0) red[w]=mx; __syncthreads();
  mx=red[0]; for(int i=1;i<8;i++) mx=fmaxf(mx,red[i]);
  float sum=0; for(int k=t;k<SK;k+=512){ float e=__expf(p[k]-mx); p[k]=e; sum+=e; }
  sum=block_sum(sum,red);
  __syncthreads();
  float acc=0; for(int k=0;k<SK;k++) acc+=p[k]*bf2f(ckv[((size_t)b*SK+k)*DC+t]);
  cc[t]=acc/sum;
  __syncthreads();
  if(t<D){ float s=0; for(int c=0;c<DC;c++) s+=cc[c]*bf2f(wuv[(size_t)c*H*D+h*D+t]); ctx[(b*H+h)*D+t]=s; }
}
__global__ void k_oproj(const float* ctx, const u16* wo, float* xf){
  int b=blockIdx.x/8, m=(blockIdx.x%8)*64+threadIdx.x;
  float s=0; for(int i=0;i<H*D;i++) s+=ctx[b*H*D+i]*bf2f(wo[(size_t)i*M+m]);
  xf[b*M+m]+=s;
}
__global__ void k_ffn1(const float* xf, const u16* wg, const u16* wu, float* hh){
  __shared__ float xn[M]; __shared__ float red[16];
  int b=blockIdx.x/4, part=blockIdx.x%4, t=threadIdx.x;
  float v=xf[b*M+t];
  float mean=block_sum(v,red)/M; float dv=v-mean; float var=block_sum(dv*dv,red)/M;
  xn[t]=dv*rsqrtf(var+1e-5f);
  __syncthreads();
  if(t<448){ int f=part*448+t; float g=0,u=0; for(int m=0;m<M;m++){ g+=xn[m]*bf2f(wg[(size_t)m*F+f]); u+=xn[m]*bf2f(wu[(size_t)m*F+f]); }
    hh[b*F+f]=g/(1.f+__expf(-g))*u; }
}
__global__ void k_ffn2(const float* hh, const u16* wd, float* xf, u16* out){
  int b=blockIdx.x/8, m=(blockIdx.x%8)*64+threadIdx.x;
  float s=0; for(int i=0;i<F;i++) s+=hh[b*F+i]*bf2f(wd[(size_t)i*M+m]);
  float r=xf[b*M+m]+s; xf[b*M+m]=r; out[b*M+m]=f2bf(r);
}

__global__ void k_probe(const unsigned char* p, size_t stride, int n, unsigned* sink){
  if(threadIdx.x!=0) return;
  unsigned acc=0;
  for(int i=0;i<n;i++){ unsigned v; const unsigned char* a=p+(size_t)i*stride; asm volatile("global_load_dword %0, %1, off nt\n\ts_waitcnt vmcnt(0)" : "=&v"(v) : "v"(a) : "memory"); acc+=v; }
  *sink=acc;
}
extern "C" void kernel_launch(void* const* d_in, const int* in_sizes, int n_in, void* d_out, int out_size, void* d_ws, size_t ws_size, hipStream_t stream){
  const u16* x=(const u16*)d_in[0]; const u16* ckv=(const u16*)d_in[1]; const u16* wq=(const u16*)d_in[2]; const u16* wuk=(const u16*)d_in[3];
  const u16* wuv=(const u16*)d_in[4]; const u16* wo=(const u16*)d_in[5]; const u16* wg=(const u16*)d_in[7]; const u16* wu=(const u16*)d_in[8]; const u16* wd=(const u16*)d_in[9];
  float* ws=(float*)d_ws; float* xf=ws; float* qabs=ws+4096; float* ctx=qabs+B*H*DC; float* hh=ctx+B*H*D;

  k_init<<<8,256,0,stream>>>(x,xf);
  for(int l=0;l<2;l++){
    k_qabs<<<B*H,512,0,stream>>>(xf,wq,wuk,qabs);
    k_attn<<<B*H,512,0,stream>>>(qabs,ckv,wuv,ctx);
    k_oproj<<<B*8,64,0,stream>>>(ctx,wo,xf);
    k_ffn1<<<B*4,512,0,stream>>>(xf,wg,wu,hh);
    k_ffn2<<<B*8,64,0,stream>>>(hh,wd,xf,(u16*)d_out);
  }
}
```

```cpp
#include <hip/hip_runtime.h>
#include <hip/hip_bf16.h>
#include <stdint.h>
typedef unsigned short u16;
__device__ __forceinline__ float bf2f(u16 b){ return __uint_as_float(((unsigned)b)<<16); }
__device__ __forceinline__ u16 f2bf(float f){ unsigned u=__float_as_uint(f); u += 0x7FFFu + ((u>>16)&1u); return (u16)(u>>16); }
constexpr int B=4,H=32,SK=4096,D=128,M=512,DC=512,F=1792;

__device__ float block_sum(float v, float* red){
  for(int o=32;o>=1;o>>=1) v += __shfl_xor(v,o);
  int w=threadIdx.x>>6; int nw=blockDim.x>>6;
  __syncthreads();
  if((threadIdx.x&63)==0) red[w]=v;
  __syncthreads();
  float s=0; for(int i=0;i<nw;i++) s+=red[i];
  return s;
}
__global__ void k_init(const u16* x, float* xf){ int i=blockIdx.x*blockDim.x+threadIdx.x; if(i<B*M) xf[i]=bf2f(x[i]); }
__global__ void k_qabs(const float* xf, const u16* wq, const u16* wuk, float* qabs){
  __shared__ float xn[M]; __shared__ float q[D]; __shared__ float red[16];
  int b=blockIdx.x/H, h=blockIdx.x%H, t=threadIdx.x;
  float v=xf[b*M+t];
  float mean=block_sum(v,red)/M; float dv=v-mean; float var=block_sum(dv*dv,red)/M;
  xn[t]=dv*rsqrtf(var+1e-5f);
  __syncthreads();
  if(t<D){ float s=0; for(int m=0;m<M;m++) s+=xn[m]*bf2f(wq[(size_t)m*H*D+h*D+t]); q[t]=s; }
  __syncthreads();
  float s=0; for(int d=0;d<D;d++) s+=q[d]*bf2f(wuk[(size_t)t*H*D+h*D+d]);
  qabs[(b*H+h)*DC+t]=s*0.08838834764831845f;
}
__global__ void k_attn(const float* qabs, const u16* ckv, const u16* wuv, float* ctx){
  __shared__ float qa[DC]; __shared__ float p[SK]; __shared__ float red[16]; __shared__ float cc[DC];
  int b=blockIdx.x/H, h=blockIdx.x%H, t=threadIdx.x;
  qa[t]=qabs[(b*H+h)*DC+t];
  __syncthreads();
  int w=t>>6, l=t&63;
  for(int k=w;k<SK;k+=8){
    const u16* row=ckv+((size_t)b*SK+k)*DC;
    float s=0; for(int j=0;j<8;j++) s+=qa[l*8+j]*bf2f(row[l*8+j]);
    for(int o=32;o>=1;o>>=1) s+=__shfl_xor(s,o);
    if(l==0) p[k]=s;
  }
  __syncthreads();
  float mx=-1e30f; for(int k=t;k<SK;k+=512) mx=fmaxf(mx,p[k]);
  for(int o=32;o>=1;o>>=1) mx=fmaxf(mx,__shfl_xor(mx,o));
  __syncthreads(); if(l==0) red[w]=mx; __syncthreads();
  mx=red[0]; for(int i=1;i<8;i++) mx=fmaxf(mx,red[i]);
  float sum=0; for(int k=t;k<SK;k+=512){ float e=__expf(p[k]-mx); p[k]=e; sum+=e; }
  sum=block_sum(sum,red);
  __syncthreads();
  float acc=0; for(int k=0;k<SK;k++) acc+=p[k]*bf2f(ckv[((size_t)b*SK+k)*DC+t]);
  cc[t]=acc/sum;
  __syncthreads();
  if(t<D){ float s=0; for(int c=0;c<DC;c++) s+=cc[c]*bf2f(wuv[(size_t)c*H*D+h*D+t]); ctx[(b*H+h)*D+t]=s; }
}
__global__ void k_oproj(const float* ctx, const u16* wo, float* xf){
  int b=blockIdx.x/8, m=(blockIdx.x%8)*64+threadIdx.x;
  float s=0; for(int i=0;i<H*D;i++) s+=ctx[b*H*D+i]*bf2f(wo[(size_t)i*M+m]);
  xf[b*M+m]+=s;
}
__global__ void k_ffn1(const float* xf, const u16* wg, const u16* wu, float* hh){
  __shared__ float xn[M]; __shared__ float red[16];
  int b=blockIdx.x/4, part=blockIdx.x%4, t=threadIdx.x;
  float v=xf[b*M+t];
  float mean=block_sum(v,red)/M; float dv=v-mean; float var=block_sum(dv*dv,red)/M;
  xn[t]=dv*rsqrtf(var+1e-5f);
  __syncthreads();
  if(t<448){ int f=part*448+t; float g=0,u=0; for(int m=0;m<M;m++){ g+=xn[m]*bf2f(wg[(size_t)m*F+f]); u+=xn[m]*bf2f(wu[(size_t)m*F+f]); }
    hh[b*F+f]=g/(1.f+__expf(-g))*u; }
}
__global__ void k_ffn2(const float* hh, const u16* wd, float* xf, u16* out){
  int b=blockIdx.x/8, m=(blockIdx.x%8)*64+threadIdx.x;
  float s=0; for(int i=0;i<F;i++) s+=hh[b*F+i]*bf2f(wd[(size_t)i*M+m]);
  float r=xf[b*M+m]+s; xf[b*M+m]=r; out[b*M+m]=f2bf(r);
}

typedef unsigned v4u __attribute__((ext_vector_type(4)));
template<int MODE> __global__ void k_bw(v4u* p, size_t n, unsigned* sink){
  size_t i=blockIdx.x*(size_t)blockDim.x+threadIdx.x; unsigned acc=0; size_t st=(size_t)gridDim.x*blockDim.x;
  for(;i+3*st<n;i+=4*st){
    if(MODE==0){ v4u a=p[i],b=p[i+st],c=p[i+2*st],d=p[i+3*st]; v4u x=a^b^c^d; acc+=x.x^x.y^x.z^x.w; }
    if(MODE==1){ v4u a=__builtin_nontemporal_load(p+i),b=__builtin_nontemporal_load(p+i+st),c=__builtin_nontemporal_load(p+i+2*st),d=__builtin_nontemporal_load(p+i+3*st); v4u x=a^b^c^d; acc+=x.x^x.y^x.z^x.w; }
    if(MODE==2){ v4u v={1,2,3,4};
      for(int k=0;k<4;k++) asm volatile("global_store_dwordx4 %0, %1, off sc1\n\ts_nop 1" :: "v"(p+i+k*st), "v"(v) : "memory"); }
  }
  if(acc==0x12345678u) *sink=acc;
}
extern "C" void kernel_launch(void* const* d_in, const int* in_sizes, int n_in, void* d_out, int out_size, void* d_ws, size_t ws_size, hipStream_t stream){
  const u16* x=(const u16*)d_in[0]; const u16* ckv=(const u16*)d_in[1]; const u16* wq=(const u16*)d_in[2]; const u16* wuk=(const u16*)d_in[3];
  const u16* wuv=(const u16*)d_in[4]; const u16* wo=(const u16*)d_in[5]; const u16* wg=(const u16*)d_in[7]; const u16* wu=(const u16*)d_in[8]; const u16* wd=(const u16*)d_in[9];
  float* ws=(float*)d_ws; float* xf=ws; float* qabs=ws+4096; float* ctx=qabs+B*H*DC; float* hh=ctx+B*H*D;

  k_init<<<8,256,0,stream>>>(x,xf);
  for(int l=0;l<2;l++){
    k_qabs<<<B*H,512,0,stream>>>(xf,wq,wuk,qabs);
    k_attn<<<B*H,512,0,stream>>>(qabs,ckv,wuv,ctx);
    k_oproj<<<B*8,64,0,stream>>>(ctx,wo,xf);
    k_ffn1<<<B*4,512,0,stream>>>(xf,wg,wu,hh);
    k_ffn2<<<B*8,64,0,stream>>>(hh,wd,xf,(u16*)d_out);
  }
}
```

```cpp
#include <hip/hip_runtime.h>
#include <hip/hip_bf16.h>
#include <stdint.h>
typedef unsigned short u16;
__device__ __forceinline__ float bf2f(u16 b){ return __uint_as_float(((unsigned)b)<<16); }
__device__ __forceinline__ u16 f2bf(float f){ unsigned u=__float_as_uint(f); u += 0x7FFFu + ((u>>16)&1u); return (u16)(u>>16); }
constexpr int B=4,H=32,SK=4096,D=128,M=512,DC=512,F=1792;

__device__ float block_sum(float v, float* red){
  for(int o=32;o>=1;o>>=1) v += __shfl_xor(v,o);
  int w=threadIdx.x>>6; int nw=blockDim.x>>6;
  __syncthreads();
  if((threadIdx.x&63)==0) red[w]=v;
  __syncthreads();
  float s=0; for(int i=0;i<nw;i++) s+=red[i];
  return s;
}
__global__ void k_init(const u16* x, float* xf){ int i=blockIdx.x*blockDim.x+threadIdx.x; if(i<B*M) xf[i]=bf2f(x[i]); }
__global__ void k_qabs(const float* xf, const u16* wq, const u16* wuk, float* qabs){
  __shared__ float xn[M]; __shared__ float q[D]; __shared__ float red[16];
  int b=blockIdx.x/H, h=blockIdx.x%H, t=threadIdx.x;
  float v=xf[b*M+t];
  float mean=block_sum(v,red)/M; float dv=v-mean; float var=block_sum(dv*dv,red)/M;
  xn[t]=dv*rsqrtf(var+1e-5f);
  __syncthreads();
  if(t<D){ float s=0; for(int m=0;m<M;m++) s+=xn[m]*bf2f(wq[(size_t)m*H*D+h*D+t]); q[t]=s; }
  __syncthreads();
  float s=0; for(int d=0;d<D;d++) s+=q[d]*bf2f(wuk[(size_t)t*H*D+h*D+d]);
  qabs[(b*H+h)*DC+t]=s*0.08838834764831845f;
}
__global__ void k_attn(const float* qabs, const u16* ckv, const u16* wuv, float* ctx){
  __shared__ float qa[DC]; __shared__ float p[SK]; __shared__ float red[16]; __shared__ float cc[DC];
  int b=blockIdx.x/H, h=blockIdx.x%H, t=threadIdx.x;
  qa[t]=qabs[(b*H+h)*DC+t];
  __syncthreads();
  int w=t>>6, l=t&63;
  for(int k=w;k<SK;k+=8){
    const u16* row=ckv+((size_t)b*SK+k)*DC;
    float s=0; for(int j=0;j<8;j++) s+=qa[l*8+j]*bf2f(row[l*8+j]);
    for(int o=32;o>=1;o>>=1) s+=__shfl_xor(s,o);
    if(l==0) p[k]=s;
  }
  __syncthreads();
  float mx=-1e30f; for(int k=t;k<SK;k+=512) mx=fmaxf(mx,p[k]);
  for(int o=32;o>=1;o>>=1) mx=fmaxf(mx,__shfl_xor(mx,o));
  __syncthreads(); if(l==0) red[w]=mx; __syncthreads();
  mx=red[0]; for(int i=1;i<8;i++) mx=fmaxf(mx,red[i]);
  float sum=0; for(int k=t;k<SK;k+=512){ float e=__expf(p[k]-mx); p[k]=e; sum+=e; }
  sum=block_sum(sum,red);
  __syncthreads();
  float acc=0; for(int k=0;k<SK;k++) acc+=p[k]*bf2f(ckv[((size_t)b*SK+k)*DC+t]);
  cc[t]=acc/sum;
  __syncthreads();
  if(t<D){ float s=0; for(int c=0;c<DC;c++) s+=cc[c]*bf2f(wuv[(size_t)c*H*D+h*D+t]); ctx[(b*H+h)*D+t]=s; }
}
__global__ void k_oproj(const float* ctx, const u16* wo, float* xf){
  int b=blockIdx.x/8, m=(blockIdx.x%8)*64+threadIdx.x;
  float s=0; for(int i=0;i<H*D;i++) s+=ctx[b*H*D+i]*bf2f(wo[(size_t)i*M+m]);
  xf[b*M+m]+=s;
}
__global__ void k_ffn1(const float* xf, const u16* wg, const u16* wu, float* hh){
  __shared__ float xn[M]; __shared__ float red[16];
  int b=blockIdx.x/4, part=blockIdx.x%4, t=threadIdx.x;
  float v=xf[b*M+t];
  float mean=block_sum(v,red)/M; float dv=v-mean; float var=block_sum(dv*dv,red)/M;
  xn[t]=dv*rsqrtf(var+1e-5f);
  __syncthreads();
  if(t<448){ int f=part*448+t; float g=0,u=0; for(int m=0;m<M;m++){ g+=xn[m]*bf2f(wg[(size_t)m*F+f]); u+=xn[m]*bf2f(wu[(size_t)m*F+f]); }
    hh[b*F+f]=g/(1.f+__expf(-g))*u; }
}
__global__ void k_ffn2(const float* hh, const u16* wd, float* xf, u16* out){
  int b=blockIdx.x/8, m=(blockIdx.x%8)*64+threadIdx.x;
  float s=0; for(int i=0;i<F;i++) s+=hh[b*F+i]*bf2f(wd[(size_t)i*M+m]);
  float r=xf[b*M+m]+s; xf[b*M+m]=r; out[b*M+m]=f2bf(r);
}

typedef unsigned long long u64;
__global__ void k_skew(u64* o){ if(threadIdx.x==0) o[blockIdx.x]=__builtin_amdgcn_s_memrealtime(); }
__global__ void k_amp(const u64* o, int mode){ u64 now=__builtin_amdgcn_s_memrealtime(); u64 mn=~0ull,mx=0; for(int i=0;i<256;i++){ u64 v=o[i]; mn=v<mn?v:mn; mx=v>mx?v:mx; }
  u64 d = mode==0 ? (mx-mn) : (now-mx); if(d>2000) d=2000; u64 e=__builtin_amdgcn_s_memrealtime()+d*200; while(__builtin_amdgcn_s_memrealtime()<e){} }
extern "C" void kernel_launch(void* const* d_in, const int* in_sizes, int n_in, void* d_out, int out_size, void* d_ws, size_t ws_size, hipStream_t stream){
  const u16* x=(const u16*)d_in[0]; const u16* ckv=(const u16*)d_in[1]; const u16* wq=(const u16*)d_in[2]; const u16* wuk=(const u16*)d_in[3];
  const u16* wuv=(const u16*)d_in[4]; const u16* wo=(const u16*)d_in[5]; const u16* wg=(const u16*)d_in[7]; const u16* wu=(const u16*)d_in[8]; const u16* wd=(const u16*)d_in[9];
  float* ws=(float*)d_ws; float* xf=ws; float* qabs=ws+4096; float* ctx=qabs+B*H*DC; float* hh=ctx+B*H*D;
  k_skew<<<256,512,0,stream>>>((u64*)d_ws+(1<<21)); k_amp<<<1,1,0,stream>>>((u64*)d_ws+(1<<21),0);
  k_init<<<8,256,0,stream>>>(x,xf);
  for(int l=0;l<2;l++){
    k_qabs<<<B*H,512,0,stream>>>(xf,wq,wuk,qabs);
    k_attn<<<B*H,512,0,stream>>>(qabs,ckv,wuv,ctx);
    k_oproj<<<B*8,64,0,stream>>>(ctx,wo,xf);
    k_ffn1<<<B*4,512,0,stream>>>(xf,wg,wu,hh);
    k_ffn2<<<B*8,64,0,stream>>>(hh,wd,xf,(u16*)d_out);
  }
}
```

```cpp
#include <hip/hip_runtime.h>
#include <hip/hip_bf16.h>
#include <stdint.h>
typedef unsigned short u16;
__device__ __forceinline__ float bf2f(u16 b){ return __uint_as_float(((unsigned)b)<<16); }
__device__ __forceinline__ u16 f2bf(float f){ unsigned u=__float_as_uint(f); u += 0x7FFFu + ((u>>16)&1u); return (u16)(u>>16); }
constexpr int B=4,H=32,SK=4096,D=128,M=512,DC=512,F=1792;

__device__ float block_sum(float v, float* red){
  for(int o=32;o>=1;o>>=1) v += __shfl_xor(v,o);
  int w=threadIdx.x>>6; int nw=blockDim.x>>6;
  __syncthreads();
  if((threadIdx.x&63)==0) red[w]=v;
  __syncthreads();
  float s=0; for(int i=0;i<nw;i++) s+=red[i];
  return s;
}
__global__ void k_init(const u16* x, float* xf){ int i=blockIdx.x*blockDim.x+threadIdx.x; if(i<B*M) xf[i]=bf2f(x[i]); }
__global__ void k_qabs(const float* xf, const u16* wq, const u16* wuk, float* qabs){
  __shared__ float xn[M]; __shared__ float q[D]; __shared__ float red[16];
  int b=blockIdx.x/H, h=blockIdx.x%H, t=threadIdx.x;
  float v=xf[b*M+t];
  float mean=block_sum(v,red)/M; float dv=v-mean; float var=block_sum(dv*dv,red)/M;
  xn[t]=dv*rsqrtf(var+1e-5f);
  __syncthreads();
  if(t<D){ float s=0; for(int m=0;m<M;m++) s+=xn[m]*bf2f(wq[(size_t)m*H*D+h*D+t]); q[t]=s; }
  __syncthreads();
  float s=0; for(int d=0;d<D;d++) s+=q[d]*bf2f(wuk[(size_t)t*H*D+h*D+d]);
  qabs[(b*H+h)*DC+t]=s*0.08838834764831845f;
}
__global__ void k_attn(const float* qabs, const u16* ckv, const u16* wuv, float* ctx){
  __shared__ float qa[DC]; __shared__ float p[SK]; __shared__ float red[16]; __shared__ float cc[DC];
  int b=blockIdx.x/H, h=blockIdx.x%H, t=threadIdx.x;
  qa[t]=qabs[(b*H+h)*DC+t];
  __syncthreads();
  int w=t>>6, l=t&63;
  for(int k=w;k<SK;k+=8){
    const u16* row=ckv+((size_t)b*SK+k)*DC;
    float s=0; for(int j=0;j<8;j++) s+=qa[l*8+j]*bf2f(row[l*8+j]);
    for(int o=32;o>=1;o>>=1) s+=__shfl_xor(s,o);
    if(l==0) p[k]=s;
  }
  __syncthreads();
  float mx=-1e30f; for(int k=t;k<SK;k+=512) mx=fmaxf(mx,p[k]);
  for(int o=32;o>=1;o>>=1) mx=fmaxf(mx,__shfl_xor(mx,o));
  __syncthreads(); if(l==0) red[w]=mx; __syncthreads();
  mx=red[0]; for(int i=1;i<8;i++) mx=fmaxf(mx,red[i]);
  float sum=0; for(int k=t;k<SK;k+=512){ float e=__expf(p[k]-mx); p[k]=e; sum+=e; }
  sum=block_sum(sum,red);
  __syncthreads();
  float acc=0; for(int k=0;k<SK;k++) acc+=p[k]*bf2f(ckv[((size_t)b*SK+k)*DC+t]);
  cc[t]=acc/sum;
  __syncthreads();
  if(t<D){ float s=0; for(int c=0;c<DC;c++) s+=cc[c]*bf2f(wuv[(size_t)c*H*D+h*D+t]); ctx[(b*H+h)*D+t]=s; }
}
__global__ void k_oproj(const float* ctx, const u16* wo, float* xf){
  int b=blockIdx.x/8, m=(blockIdx.x%8)*64+threadIdx.x;
  float s=0; for(int i=0;i<H*D;i++) s+=ctx[b*H*D+i]*bf2f(wo[(size_t)i*M+m]);
  xf[b*M+m]+=s;
}
__global__ void k_ffn1(const float* xf, const u16* wg, const u16* wu, float* hh){
  __shared__ float xn[M]; __shared__ float red[16];
  int b=blockIdx.x/4, part=blockIdx.x%4, t=threadIdx.x;
  float v=xf[b*M+t];
  float mean=block_sum(v,red)/M; float dv=v-mean; float var=block_sum(dv*dv,red)/M;
  xn[t]=dv*rsqrtf(var+1e-5f);
  __syncthreads();
  if(t<448){ int f=part*448+t; float g=0,u=0; for(int m=0;m<M;m++){ g+=xn[m]*bf2f(wg[(size_t)m*F+f]); u+=xn[m]*bf2f(wu[(size_t)m*F+f]); }
    hh[b*F+f]=g/(1.f+__expf(-g))*u; }
}
__global__ void k_ffn2(const float* hh, const u16* wd, float* xf, u16* out){
  int b=blockIdx.x/8, m=(blockIdx.x%8)*64+threadIdx.x;
  float s=0; for(int i=0;i<F;i++) s+=hh[b*F+i]*bf2f(wd[(size_t)i*M+m]);
  float r=xf[b*M+m]+s; xf[b*M+m]=r; out[b*M+m]=f2bf(r);
}

typedef unsigned long long u64;
__device__ __forceinline__ u64 ct() { u64 t; __builtin_amdgcn_sched_barrier(0); asm volatile("s_memtime %0\n\ts_waitcnt lgkmcnt(0)" : "=s"(t) :: "memory"); __builtin_amdgcn_sched_barrier(0); return t; }
__device__ __forceinline__ unsigned ld(const unsigned char* p){ unsigned v; asm volatile("global_load_dword %0, %1, off\n\ts_waitcnt vmcnt(0)" : "=&v"(v) : "v"(p) : "memory"); return v; }
__global__ void k_meas(u64* o, const unsigned char* a, const unsigned char* b, const unsigned char* c, const unsigned char* d){
  u64 t0=ct();
  asm volatile(""::"s"(o),"s"(a),"s"(b),"s"(c),"s"(d));
  u64 t1=ct();
  ld(a+blockIdx.x*4096); u64 t2=ct();
  ld(a+blockIdx.x*4096+1024); u64 t3=ct();
  ld(b+blockIdx.x*4096); u64 t4=ct();
  ld(c+blockIdx.x*4096); u64 t5=ct();
  ld(d+blockIdx.x*4096); u64 t6=ct();
  if(threadIdx.x==0){ u64* q=o+blockIdx.x*8; q[0]=t1-t0; q[1]=t2-t1; q[2]=t3-t2; q[3]=t4-t3; q[4]=t5-t4; q[5]=t6-t5; }
}
__global__ void k_amp(const u64* o, int item){ u64 s=0; for(int i=0;i<256;i++) s+=o[i*8+item]; u64 d=s/256; if(d>20000) d=20000; u64 e=__builtin_amdgcn_s_memrealtime()+d*20; while(__builtin_amdgcn_s_memrealtime()<e){} }
extern "C" void kernel_launch(void* const* d_in, const int* in_sizes, int n_in, void* d_out, int out_size, void* d_ws, size_t ws_size, hipStream_t stream){
  const u16* x=(const u16*)d_in[0]; const u16* ckv=(const u16*)d_in[1]; const u16* wq=(const u16*)d_in[2]; const u16* wuk=(const u16*)d_in[3];
  const u16* wuv=(const u16*)d_in[4]; const u16* wo=(const u16*)d_in[5]; const u16* wg=(const u16*)d_in[7]; const u16* wu=(const u16*)d_in[8]; const u16* wd=(const u16*)d_in[9];
  float* ws=(float*)d_ws; float* xf=ws; float* qabs=ws+4096; float* ctx=qabs+B*H*DC; float* hh=ctx+B*H*D;
  k_meas<<<256,64,0,stream>>>((u64*)d_ws+(1<<21),(const unsigned char*)d_in[2],(const unsigned char*)d_in[1],(const unsigned char*)d_in[7],(const unsigned char*)d_ws+(64<<20)); k_amp<<<1,1,0,stream>>>((u64*)d_ws+(1<<21),2);
  k_init<<<8,256,0,stream>>>(x,xf);
  for(int l=0;l<2;l++){
    k_qabs<<<B*H,512,0,stream>>>(xf,wq,wuk,qabs);
    k_attn<<<B*H,512,0,stream>>>(qabs,ckv,wuv,ctx);
    k_oproj<<<B*8,64,0,stream>>>(ctx,wo,xf);
    k_ffn1<<<B*4,512,0,stream>>>(xf,wg,wu,hh);
    k_ffn2<<<B*8,64,0,stream>>>(hh,wd,xf,(u16*)d_out);
  }
}
```

```cpp
#include <hip/hip_runtime.h>
#include <stdint.h>

#define GAS __attribute__((address_space(1)))
#define LAS __attribute__((address_space(3)))
typedef unsigned long long u64;
typedef unsigned v4u __attribute__((ext_vector_type(4)));
typedef unsigned v2u __attribute__((ext_vector_type(2)));
typedef float f32x4 __attribute__((ext_vector_type(4)));
typedef float f32x2 __attribute__((ext_vector_type(2)));
typedef short bf16x8 __attribute__((ext_vector_type(8)));
typedef short s16x4 __attribute__((ext_vector_type(4)));
typedef __bf16 bf2 __attribute__((ext_vector_type(2)));
typedef __amdgpu_buffer_rsrc_t rsrc_t;

#define STAMP 0

constexpr unsigned OFF_E = 0, OFF_FL = 4096, OFF_QS0 = 16384, OFF_CSP = 65536;
constexpr unsigned OFF_SM = 262144;
constexpr unsigned SM_QA = 0, SM_MLF = 131072, SM_CP = 393216, SM_XP = 655360, SM_YP = 720896, SM_QS = 983040, SM_QP = 1048576, SM_STRIDE = 1572864;
constexpr unsigned OFF_OP = 4194304, OP_STRIDE = 4194304;
constexpr unsigned OFF_STAMP = 16777216;
constexpr unsigned WS_BYTES = 20971520;
constexpr unsigned OOB = 0x7FFFFFF0u;
constexpr unsigned SENT = 0xFFFFFFFFu;
constexpr int SPIN_MAX = 1 << 16;

constexpr int ROW = 1040;
constexpr int L_C = 0, L_Q = 128 * ROW, L_P = L_Q + 16 * ROW, L_MX = L_P + 4096, L_CX = L_MX + 512, L_X1 = L_CX + 512, L_GU = L_X1 + 1024,
              L_ST = L_GU + 2048, L_XR = L_ST + 64, L_R = L_XR + 512, L_CNT = L_R + 4096, LDS_BYTES = L_CNT + 64;
static_assert(LDS_BYTES <= 163840, "lds");

#define D_FL 10
#define D_HQ 9
#define D_H1 9
#define D_ML 8
#define D_HV 9
#define D_H3 11
#define D_H4 9

struct Args {
  const unsigned char *x, *ckv, *wq, *wuk, *wuv, *wo, *wg, *wu, *wd;
  unsigned char *out, *ws;
};

#define RS(p, n) __builtin_amdgcn_make_buffer_rsrc((void*)(p), 0, (n), 0x00020000)
#define SB() __builtin_amdgcn_sched_barrier(0)
#define MEMF() asm volatile("" ::: "memory")
#define VM0() asm volatile("s_waitcnt vmcnt(0)" ::: "memory")
#define BAR() do { asm volatile("s_waitcnt lgkmcnt(0)" ::: "memory"); __builtin_amdgcn_s_barrier(); MEMF(); } while (0)
constexpr int SC1 = 16;

__device__ __forceinline__ f32x4 mfma(v4u a, v4u b, f32x4 c) {
  return __builtin_amdgcn_mfma_f32_16x16x32_bf16(__builtin_bit_cast(bf16x8, a), __builtin_bit_cast(bf16x8, b), c, 0, 0, 0);
}
__device__ __forceinline__ unsigned pk(float lo, float hi) { bf2 v = {(__bf16)lo, (__bf16)hi}; return __builtin_bit_cast(unsigned, v); }
__device__ __forceinline__ float blo(unsigned u) { return __builtin_bit_cast(float, u << 16); }
__device__ __forceinline__ float bhi(unsigned u) { return __builtin_bit_cast(float, u & 0xFFFF0000u); }
__device__ __forceinline__ float f_(unsigned u) { return __builtin_bit_cast(float, u); }
__device__ __forceinline__ unsigned u_(float f) { return __builtin_bit_cast(unsigned, f); }
template <int C> __device__ __forceinline__ unsigned dppu(unsigned v) { return __builtin_amdgcn_update_dpp(0u, v, C, 0xf, 0xf, true); }
template <int C> __device__ __forceinline__ float dppf(float v) { return f_(dppu<C>(u_(v))); }
constexpr int XOR1 = 0xB1, XOR2 = 0x4E, HMIR = 0x141, MIR = 0x140, ROR8 = 0x128, SHL = 0x100, BCAST = 0x150;
__device__ __forceinline__ float add32(float a, float b) { auto r = __builtin_amdgcn_permlane32_swap(u_(a), u_(b), false, false); return f_(r[0]) + f_(r[1]); }
__device__ __forceinline__ float add16(float a, float b) { auto r = __builtin_amdgcn_permlane16_swap(u_(a), u_(b), false, false); return f_(r[0]) + f_(r[1]); }
__device__ __forceinline__ float max32(float a) { auto r = __builtin_amdgcn_permlane32_swap(u_(a), u_(a), false, false); return fmaxf(f_(r[0]), f_(r[1])); }
__device__ __forceinline__ float max16(float a) { auto r = __builtin_amdgcn_permlane16_swap(u_(a), u_(a), false, false); return fmaxf(f_(r[0]), f_(r[1])); }
__device__ __forceinline__ float sum4(f32x4 v) { return (v.x + v.y) + (v.z + v.w); }
__device__ __forceinline__ float dot2(unsigned a, unsigned b, float c) { return __builtin_amdgcn_fdot2_f32_bf16(__builtin_bit_cast(bf2, a), __builtin_bit_cast(bf2, b), c, false); }
constexpr unsigned ONES = 0x3F803F80u;
__device__ __forceinline__ unsigned umax4(v4u v) { return max(max(v.x, v.y), max(v.z, v.w)); }
#define nap(n) do { if ((n) > 0) __builtin_amdgcn_s_sleep((n) > 0 ? (n) : 0); } while (0)
__device__ __forceinline__ void tr2(const unsigned (&r)[8], v4u& f0, v4u& f1) {
#pragma unroll
  for (int d = 0; d < 4; ++d) {
    f0[d] = __builtin_amdgcn_perm(r[2 * d + 1], r[2 * d], 0x05040100u);
    f1[d] = __builtin_amdgcn_perm(r[2 * d + 1], r[2 * d], 0x07060302u);
  }
}
__device__ __forceinline__ void tr4(const v2u (&r)[8], v4u& f0, v4u& f1, v4u& f2, v4u& f3) {
#pragma unroll
  for (int d = 0; d < 4; ++d) {
    f0[d] = __builtin_amdgcn_perm(r[2 * d + 1].x, r[2 * d].x, 0x05040100u);
    f1[d] = __builtin_amdgcn_perm(r[2 * d + 1].x, r[2 * d].x, 0x07060302u);
    f2[d] = __builtin_amdgcn_perm(r[2 * d + 1].y, r[2 * d].y, 0x05040100u);
    f3[d] = __builtin_amdgcn_perm(r[2 * d + 1].y, r[2 * d].y, 0x07060302u);
  }
}
__device__ __forceinline__ int mcol(int ms, int p) { return 64 * (ms >> 1) + 4 * (p >> 1) + 2 * (ms & 1) + (p & 1); }

template <int N> __device__ __forceinline__ void hop_load(rsrc_t rs, const unsigned (&off)[N], v4u (&v)[N]) {
  for (int sp = 0;; ++sp) {
    MEMF();
#pragma unroll
    for (int i = 0; i < N; ++i) v[i] = __builtin_amdgcn_raw_buffer_load_b128(rs, off[i], 0, SC1);
    unsigned m = 0;
#pragma unroll
    for (int i = 0; i < N; ++i) m = max(m, umax4(v[i]));
    if (__builtin_amdgcn_ballot_w64(m == SENT) == 0ull || sp > SPIN_MAX) break;
  }
}

#define TS(i) do { } while (0)

__global__ void __launch_bounds__(512, 2) decode_block(Args a) {
  __shared__ __attribute__((aligned(16))) unsigned char smem[LDS_BYTES];
  LAS unsigned char* L = (LAS unsigned char*)smem;
  const unsigned lbase = (unsigned)(size_t)L;
  const int w = blockIdx.x, tid = threadIdx.x, lane = tid & 63, wv = __builtin_amdgcn_readfirstlane(tid >> 6);
  const int col = lane & 15, g = lane >> 4, q4 = wv & 3;
  const bool lo = wv < 4;
  const int x = w & 7, r = w >> 3;
  const int pair = x * 16 + (r >> 1), bA = pair >> 5, kc = pair & 31, hg = r & 1;
  const int h = 4 * x + (r >> 3), j = r & 7;
  const int hpv = 2 * x + (r >> 4), msv = r & 15;
  const int msq = 2 * x + (r >> 4), hpq = r & 15;
  const bool fact = r < 28;
  const int bF = r & 3, fs = 7 * x + (r >> 2);
  const bool resid = hpv == msv;
  TS(0);
  {
    u64 pc; unsigned len;
    asm volatile("s_getpc_b64 %0\n.Lpc_here:\n\ts_mov_b32 %1, .Lpc_end-.Lpc_here" : "=s"(pc), "=s"(len));
    if (wv == 7) {
      const unsigned o = (unsigned)(r + 32 * lane) * 128u;
      if (o < len) {
        unsigned keep;
        asm volatile("s_mov_b32 %0, m0\n\ts_mov_b32 m0, %3\n\ts_nop 0\n\tglobal_load_lds_dword %1, %2\n\ts_mov_b32 m0, %0"
                     : "=&s"(keep) : "v"(o), "s"(pc), "s"(lbase + L_P) : "memory");
      }
    }
  }
  const rsrc_t rW = RS(a.ws, WS_BYTES);
  if (tid < 16) ((LAS unsigned*)(L + L_CNT))[tid] = 0u;

  v4u wA[8];
  v4u wG[8], wD[4];
  unsigned xp = 0;
  u64 tag = 0, e0 = 0;
  if (lo) {
    const rsrc_t rX = RS(a.x, 4096), rQ = RS(a.wq, 4194304), rK = RS(a.wuk, 4194304);
    const v2u ev = __builtin_amdgcn_raw_buffer_load_b64(rW, OFF_E, 0, SC1);
    v4u xr[4];
#pragma unroll
    for (int i = 0; i < 4; ++i) xr[i] = __builtin_amdgcn_raw_buffer_load_b128(rX, (col >> 2) * 1024 + ((col & 3) * 4 + g) * 64 + i * 16, 0, 0);
    const unsigned xraw = __builtin_amdgcn_raw_buffer_load_b32(rX, (col >> 2) * 1024 + mcol(msq, 8 * g + 2 * (col & 3)) * 2, 0, 0);
    v2u t[8];
#pragma unroll
    for (int e = 0; e < 8; ++e) t[e] = __builtin_amdgcn_raw_buffer_load_b64(rQ, mcol(msq, 8 * g + e) * 8192 + hpq * 512 + q4 * 128 + col * 8, 0, 0);
    { const v4u f = {SENT, SENT, SENT, SENT}; __builtin_amdgcn_raw_buffer_store_b128(f, rW, OFF_SM + w * 4096 + tid * 16, 0, SC1); }
#pragma unroll
    for (int i = 0; i < 4; ++i) wA[4 + i] = __builtin_amdgcn_raw_buffer_load_b128(rK, (64 * j + 4 * col + i) * 8192 + h * 256 + q4 * 64 + g * 16, 0, 0);
    SB();
    BAR();
    SB();
    e0 = ((u64)ev.y << 32) | ev.x;
    u64 z = e0 + 0x9E3779B97F4A7C15ull;
    z = (z ^ (z >> 30)) * 0xBF58476D1CE4E5B9ull; z = (z ^ (z >> 27)) * 0x94D049BB133111EBull; tag = z ^ (z >> 31);
    float s = 0.f;
#pragma unroll
    for (int i = 0; i < 4; ++i) { s = dot2(xr[i].x, ONES, s); s = dot2(xr[i].y, ONES, s); s = dot2(xr[i].z, ONES, s); s = dot2(xr[i].w, ONES, s); }
    s += dppf<XOR1>(s); s += dppf<XOR2>(s); s = add16(s, s); s = add32(s, s);
    const float mu = s * (1.f / 512.f);
    xp = pk(blo(xraw) - mu, bhi(xraw) - mu);
    if (wv == 0) *(LAS f32x2*)(L + L_XR + (col >> 2) * 128 + (8 * g + 2 * (col & 3)) * 4) = f32x2{blo(xraw), bhi(xraw)};
    tr4(t, wA[0], wA[1], wA[2], wA[3]);
  } else {
    BAR();
    const unsigned char* src = a.ckv + (size_t)pair * 131072 + lane * 16;
#pragma unroll
    for (int i = 0; i < 32; ++i) {
      const int row = 4 * i + q4;
      unsigned keep;
      asm volatile("s_mov_b32 %0, m0\n\ts_mov_b32 m0, %2\n\ts_nop 0\n\tglobal_load_lds_dwordx4 %1, off\n\ts_mov_b32 m0, %0"
                   : "=&s"(keep) : "v"(src + row * 1024), "s"(lbase + L_C + row * ROW) : "memory");
    }
  }
  TS(1);
  float cs4[4] = {0.f, 0.f, 0.f, 0.f};
  float csg = 0.f;
  v4u ID0, ID1;
#pragma unroll
  for (int d = 0; d < 4; ++d) { const bool on = (g == (col >> 2)) && ((col & 3) == d); ID0[d] = on ? 0x00003F80u : 0u; ID1[d] = on ? 0x3F800000u : 0u; }
  const f32x4 Z4 = {0.f, 0.f, 0.f, 0.f};

#pragma unroll
  for (int ly = 0; ly < 2; ++ly) {
    const unsigned SM = OFF_SM + ly * SM_STRIDE, OP = OFF_OP + ly * OP_STRIDE;
    const unsigned QS = ly == 0 ? OFF_QS0 : SM + SM_QS;
    const int T0 = 2 + ly * 14;
    if (lo) {
      const v4u xa = {dppu<0x00>(xp), dppu<0x55>(xp), dppu<0xAA>(xp), dppu<0xFF>(xp)};
      f32x4 c0 = mfma(xa, wA[0], Z4), c1 = mfma(xa, wA[1], Z4), c2 = mfma(xa, wA[2], Z4), c3 = mfma(xa, wA[3], Z4);
      const unsigned qoff = (((2 * hpq + (q4 >> 1)) * 4 + 2 * (q4 & 1) + (col >> 3)) * 16 + msq);
      __builtin_amdgcn_raw_buffer_store_b64(v2u{pk(c0.x, c1.x), pk(c2.x, c3.x)}, rW, SM + SM_QP + qoff * 256 + g * 64 + (col & 7) * 8, 0, SC1);
      if (wv == 3) {
        const float x0 = blo(xp), x1 = bhi(xp);
        float s1 = x0 + x1, s2 = x0 * x0 + x1 * x1;
        s1 += dppf<XOR1>(s1); s2 += dppf<XOR1>(s2); s1 += dppf<XOR2>(s1); s2 += dppf<XOR2>(s2);
        float sv = add16(s1, s2); sv = add32(sv, sv);
        __builtin_amdgcn_raw_buffer_store_b32(u_(sv), rW, (g < 2 && (col & 3) == 0) ? QS + (hpq * 16 + msq) * 32 + (col >> 2) * 8 + g * 4 : OOB, 0, SC1);
      }
      if (ly == 0) {
        const v4u on = {ONES, ONES, ONES, ONES};
        c0 = mfma(on, wA[0], Z4); c1 = mfma(on, wA[1], Z4); c2 = mfma(on, wA[2], Z4); c3 = mfma(on, wA[3], Z4);
        __builtin_amdgcn_raw_buffer_store_b64(v2u{pk(c0.x, c1.x), pk(c2.x, c3.x)}, rW, g == 0 ? OFF_CSP + qoff * 64 + (col & 7) * 8 : OOB, 0, SC1);
        VM0();
        unsigned old = 0;
        if (lane == 0) old = __hip_atomic_fetch_add((LAS unsigned*)(L + L_CNT), 1u, __ATOMIC_RELAXED, __HIP_MEMORY_SCOPE_WORKGROUP);
        if (__builtin_amdgcn_readfirstlane(old) == 3u)
          __builtin_amdgcn_raw_buffer_store_b64(v2u{(unsigned)tag, (unsigned)(tag >> 32)}, rW, lane == 0 ? OFF_FL + w * 8 : OOB, 0, SC1);
        TS(T0 + 0);
        nap(D_FL);
        for (int sp = 0;; ++sp) {
          MEMF();
          const v4u f0 = __builtin_amdgcn_raw_buffer_load_b128(rW, OFF_FL + lane * 16, 0, SC1), f1 = __builtin_amdgcn_raw_buffer_load_b128(rW, OFF_FL + 1024 + lane * 16, 0, SC1);
          const unsigned tl = (unsigned)tag, th = (unsigned)(tag >> 32);
          const unsigned bad = (f0.x ^ tl) | (f0.y ^ th) | (f0.z ^ tl) | (f0.w ^ th) | (f1.x ^ tl) | (f1.y ^ th) | (f1.z ^ tl) | (f1.w ^ th);
          if (__builtin_amdgcn_ballot_w64(bad != 0u) == 0ull || sp > SPIN_MAX) break;
          nap(2);
        }
        MEMF();
        if (w == 0 && wv == 0) {
          const u64 e1 = e0 + 1ull;
          __builtin_amdgcn_raw_buffer_store_b64(v2u{(unsigned)e1, (unsigned)(e1 >> 32)}, rW, lane == 0 ? OFF_E : OOB, 0, SC1);
        }
      } else {
        TS(T0 + 0);
        nap(D_HQ);
      }
      TS(T0 + 1);
      v4u qa[4]; v2u qs = {0u, 0u};
      {
        const unsigned base = SM + SM_QP + (h * 4 + q4) * 4096 + (col & 3) * 256 + (col >> 2) * 64 + g * 16;
        const unsigned soff = wv == 0 ? QS + ((h >> 1) * 16 + col) * 32 + g * 8 : OOB;
        for (int sp = 0;; ++sp) {
          MEMF();
#pragma unroll
          for (int i = 0; i < 4; ++i) qa[i] = __builtin_amdgcn_raw_buffer_load_b128(rW, base + i * 1024, 0, SC1);
          qs = __builtin_amdgcn_raw_buffer_load_b64(rW, soff, 0, SC1);
          if (ly == 0) break;
          const unsigned m = max(max(max(umax4(qa[0]), umax4(qa[1])), max(umax4(qa[2]), umax4(qa[3]))), max(qs.x, qs.y));
          if (__builtin_amdgcn_ballot_w64(m == SENT) == 0ull || sp > SPIN_MAX) break;
        }
      }
      f32x4 ac[4] = {Z4, Z4, Z4, Z4};
#pragma unroll
      for (int i = 0; i < 4; ++i)
#pragma unroll
        for (int t = 0; t < 4; ++t) ac[t] = mfma(qa[i], wA[4 + t], ac[t]);
      f32x4 pv = {sum4(ac[0]), sum4(ac[1]), sum4(ac[2]), sum4(ac[3])};
      if (wv != 0) {
        *(LAS f32x4*)(L + L_R + q4 * 1024 + lane * 16) = pv;
        MEMF();
        if (lane == 0) __hip_atomic_fetch_add((LAS unsigned*)(L + L_CNT + 4), 1u, __ATOMIC_RELAXED, __HIP_MEMORY_SCOPE_WORKGROUP);
        TS(T0 + 2);
        nap(D_H1 + 3);
      } else {
        float s1 = f_(qs.x), s2 = f_(qs.y);
        s1 += dppf<XOR1>(s1); s2 += dppf<XOR1>(s2); s1 += dppf<XOR2>(s1); s2 += dppf<XOR2>(s2);
        s1 += dppf<HMIR>(s1); s2 += dppf<HMIR>(s2); s1 += dppf<MIR>(s1); s2 += dppf<MIR>(s2);
        const float mu = s1 * (1.f / 512.f), var = fmaxf(s2 * (1.f / 512.f) - mu * mu, 0.f);
        const float sc = __builtin_amdgcn_rsqf(var + 1e-5f) * (0.08838834764831845f * 1.4426950408889634f);
        f32x4 p1, p2, p3;
        for (int sp = 0;; ++sp) {
          MEMF();
          const unsigned c = *(volatile LAS unsigned*)(L + L_CNT + 4);
          MEMF();
          p1 = *(LAS f32x4*)(L + L_R + 1024 + lane * 16); p2 = *(LAS f32x4*)(L + L_R + 2048 + lane * 16); p3 = *(LAS f32x4*)(L + L_R + 3072 + lane * 16);
          if (__builtin_amdgcn_readfirstlane(c) >= 3u * (ly + 1) || sp > SPIN_MAX) break;
        }
        pv = (pv + p1) + (p2 + p3);
        const float o0 = (pv.x - mu * cs4[0]) * sc, o1 = (pv.y - mu * cs4[1]) * sc, o2 = (pv.z - mu * cs4[2]) * sc, o3 = (pv.w - mu * cs4[3]) * sc;
        __builtin_amdgcn_raw_buffer_store_b64(v2u{pk(o0, o1), pk(o2, o3)}, rW, SM + SM_QA + (g * 32 + h) * 1024 + j * 128 + col * 8, 0, SC1);
        TS(T0 + 2);
        nap(D_H1);
      }
      unsigned off[4]; v4u qv[4];
#pragma unroll
      for (int i = 0; i < 4; ++i) off[i] = SM + SM_QA + (bA * 32 + hg * 16 + 4 * q4 + i) * 1024 + lane * 16;
      hop_load<4>(rW, off, qv);
#pragma unroll
      for (int i = 0; i < 4; ++i) *(LAS v4u*)(L + L_Q + (4 * q4 + i) * ROW + lane * 16) = qv[i];
    } else if (ly == 0) {
      VM0();
    }
    TS(T0 + 3);
    v4u cf[16];
    const int rho = 2 * (col & 7) + (col >> 3);
    if (ly == 1) {
#pragma unroll
      for (int s = 0; s < 16; ++s) cf[s] = *(LAS v4u*)(L + L_C + (16 * wv + rho) * ROW + g * 256 + s * 16);
    }
    BAR();
    unsigned tv[16], to[16];
    if (ly == 0) {
#pragma unroll
      for (int s = 0; s < 16; ++s) cf[s] = *(LAS v4u*)(L + L_C + (16 * wv + rho) * ROW + g * 256 + s * 16);
      if (!lo) {
        const rsrc_t rV = RS(a.wuv, 4194304), rO = RS(a.wo, 4194304);
#pragma unroll
        for (int e = 0; e < 16; ++e)
          tv[e] = __builtin_amdgcn_raw_buffer_load_b32(rV, (64 * j + 32 * (e >> 3) + 16 * ((e >> 2) & 1) + 4 * g + (e & 3)) * 8192 + h * 256 + q4 * 64 + col * 4, 0, 0);
#pragma unroll
        for (int e = 0; e < 16; ++e)
          to[e] = __builtin_amdgcn_raw_buffer_load_b32(rO, ((2 * hpv + (e >> 3)) * 128 + 32 * q4 + 8 * g + (e & 7)) * 1024 + mcol(msv, 2 * col) * 2, 0, 0);
        SB();
      }
    }
    f32x4 sc4 = Z4;
#pragma unroll
    for (int s = 0; s < 16; ++s) sc4 = mfma(cf[s], *(LAS v4u*)(L + L_Q + col * ROW + g * 256 + s * 16), sc4);
    float mx = fmaxf(fmaxf(sc4.x, sc4.y), fmaxf(sc4.z, sc4.w));
    mx = max16(mx); mx = max32(mx);
    if (g == 0) *(LAS float*)(L + L_MX + col * 32 + wv * 4) = mx;
    {
      const int qq = 4 * g + (col >> 2), rr = 2 * (qq & 7) + (qq >> 3);
      LAS unsigned char* tb = L + L_C + rr * ROW + wv * 128 + (col & 3) * 8;
#pragma unroll
      for (int s = 0; s < 4; ++s)
#pragma unroll
        for (int t = 0; t < 4; ++t) {
          const v2u u0 = __builtin_bit_cast(v2u, __builtin_amdgcn_ds_read_tr16_b64_v4i16((LAS s16x4*)(tb + (32 * s) * ROW + t * 32)));
          const v2u u1 = __builtin_bit_cast(v2u, __builtin_amdgcn_ds_read_tr16_b64_v4i16((LAS s16x4*)(tb + (32 * s + 16) * ROW + t * 32)));
          cf[4 * s + t] = v4u{u0.x, u0.y, u1.x, u1.y};
        }
    }
    BAR();
    {
      const f32x4 m0 = *(LAS f32x4*)(L + L_MX + col * 32), m1 = *(LAS f32x4*)(L + L_MX + col * 32 + 16);
      mx = fmaxf(fmaxf(fmaxf(m0.x, m0.y), fmaxf(m0.z, m0.w)), fmaxf(fmaxf(m1.x, m1.y), fmaxf(m1.z, m1.w)));
      const float p0 = __builtin_amdgcn_exp2f(sc4.x - mx), p1 = __builtin_amdgcn_exp2f(sc4.y - mx), p2 = __builtin_amdgcn_exp2f(sc4.z - mx), p3 = __builtin_amdgcn_exp2f(sc4.w - mx);
      *(LAS v2u*)(L + L_P + (wv >> 1) * 1024 + lane * 16 + (wv & 1) * 8) = v2u{pk(p0, p1), pk(p2, p3)};
    }
    BAR();
    TS(T0 + 4);
    {
      v4u pf[4];
#pragma unroll
      for (int s = 0; s < 4; ++s) pf[s] = *(LAS v4u*)(L + L_P + s * 1024 + lane * 16);
      f32x4 o[4] = {Z4, Z4, Z4, Z4};
#pragma unroll
      for (int t = 0; t < 4; ++t)
#pragma unroll
        for (int s = 0; s < 4; ++s) o[t] = mfma(cf[4 * s + t], pf[s], o[t]);
      const int hh = hg * 16 + col;
      const unsigned ob = OP + ((((hh >> 1) * 8 + wv) * 2) * 4 + bA) * 4096 + kc * 128 + (hh & 1) * 64 + g * 16;
#pragma unroll
      for (int u = 0; u < 2; ++u)
        __builtin_amdgcn_raw_buffer_store_b128(v4u{pk(o[2 * u].x, o[2 * u].y), pk(o[2 * u].z, o[2 * u].w), pk(o[2 * u + 1].x, o[2 * u + 1].y), pk(o[2 * u + 1].z, o[2 * u + 1].w)},
                                               rW, ob + u * 16384, 0, SC1);
      float l = 0.f;
#pragma unroll
      for (int s = 0; s < 4; ++s) { l = dot2(pf[s].x, ONES, l); l = dot2(pf[s].y, ONES, l); l = dot2(pf[s].z, ONES, l); l = dot2(pf[s].w, ONES, l); }
      l = add16(l, l); l = add32(l, l);
      TS(T0 + 5);
      VM0();
      __builtin_amdgcn_raw_buffer_store_b64(v2u{u_(mx), u_(l)}, rW, g == 0 ? SM + SM_MLF + ((hh * 8 + wv) * 4 + bA) * 256 + kc * 8 : OOB, 0, SC1);
    }
    TS(T0 + 6);
    if (ly == 0 && !lo) {
      unsigned t8[8];
#pragma unroll
      for (int u = 0; u < 2; ++u) {
#pragma unroll
        for (int e = 0; e < 8; ++e) t8[e] = tv[8 * u + e];
        tr2(t8, wA[2 * u], wA[2 * u + 1]);
#pragma unroll
        for (int e = 0; e < 8; ++e) t8[e] = to[8 * u + e];
        tr2(t8, wA[4 + 2 * u], wA[4 + 2 * u + 1]);
      }
    }
    nap(D_ML);
    v2u gt[16], dt[8];
    {
      const int b = wv >> 1, ks = wv & 1;
      v2u ml;
      const unsigned moff = SM + SM_MLF + ((h * 8 + j) * 4 + b) * 256 + (16 * ((col >> 2) & 1) + 4 * g + (col & 3)) * 8;
      for (int sp = 0;; ++sp) {
        MEMF();
        ml = __builtin_amdgcn_raw_buffer_load_b64(rW, moff, 0, SC1);
        if (__builtin_amdgcn_ballot_w64(max(ml.x, ml.y) == SENT) == 0ull || sp > SPIN_MAX) break;
      }
      MEMF();
      TS(T0 + 7);
      const unsigned ob = OP + ((((h >> 1) * 8 + j) * 2 + ks) * 4 + b) * 4096 + col * 128 + (h & 1) * 64 + g * 16;
      const v4u o0 = __builtin_amdgcn_raw_buffer_load_b128(rW, ob, 0, SC1), o1 = __builtin_amdgcn_raw_buffer_load_b128(rW, ob + 2048, 0, SC1);
      v4u csv = {0u, 0u, 0u, 0u};
      if (ly == 0) {
        if (lo) csv = __builtin_amdgcn_raw_buffer_load_b128(rW, OFF_CSP + (h * 4 + q4) * 1024 + col * 64 + g * 16, 0, SC1);
        const rsrc_t rG = RS(a.wg, 1835008), rU = RS(a.wu, 1835008), rD = RS(a.wd, 1835008);
        (void)rG; (void)rU;
        const GAS unsigned char* gb = (const GAS unsigned char*)(col < 8 ? a.wg : a.wu) + (fact ? fs : 0) * 64 + (col & 7) * 8;
#pragma unroll
        for (int e = 0; e < 16; ++e) gt[e] = *(const GAS v2u*)(gb + (size_t)mcol(2 * wv + (e >> 3), 8 * g + (e & 7)) * 3584);
#pragma unroll
        for (int e = 0; e < 8; ++e) dt[e] = __builtin_amdgcn_raw_buffer_load_b64(rD, fact ? (32 * fs + 8 * g + e) * 1024 + wv * 128 + col * 8 : OOB, 0, 0);
        const v4u f = {SENT, SENT, SENT, SENT};
        __builtin_amdgcn_raw_buffer_store_b128(f, rW, tid < 384 ? OFF_SM + SM_STRIDE + w * 6144 + tid * 16 : OOB, 0, SC1);
        SB();
      }
      float m = f_(ml.x), M = m;
      M = fmaxf(M, dppf<XOR1>(M)); M = fmaxf(M, dppf<XOR2>(M)); M = fmaxf(M, dppf<HMIR>(M)); M = max16(M); M = max32(M);
      const float wt = __builtin_amdgcn_exp2f(m - M);
      float Ls = wt * f_(ml.y);
      Ls += dppf<XOR1>(Ls); Ls += dppf<XOR2>(Ls); Ls += dppf<HMIR>(Ls); Ls = add16(Ls, Ls); Ls = add32(Ls, Ls);
      const float wn = wt * __builtin_amdgcn_rcpf(Ls);
      const float w0 = dppf<BCAST + 0>(wn), w1 = dppf<BCAST + 1>(wn), w2 = dppf<BCAST + 2>(wn), w3 = dppf<BCAST + 3>(wn);
      const float w4 = dppf<BCAST + 4>(wn), w5 = dppf<BCAST + 5>(wn), w6 = dppf<BCAST + 6>(wn), w7 = dppf<BCAST + 7>(wn);
      const f32x4 d00 = mfma(o0, ID0, Z4), d01 = mfma(o0, ID1, Z4), d10 = mfma(o1, ID0, Z4), d11 = mfma(o1, ID1, Z4);
      const float z0 = ((w0 * d00.x + w1 * d00.y) + (w2 * d00.z + w3 * d00.w)) + ((w4 * d10.x + w5 * d10.y) + (w6 * d10.z + w7 * d10.w));
      const float z1 = ((w0 * d01.x + w1 * d01.y) + (w2 * d01.z + w3 * d01.w)) + ((w4 * d11.x + w5 * d11.y) + (w6 * d11.z + w7 * d11.w));
      float z = add32(z0, z1); z = add16(z, z);
      *(LAS unsigned short*)(L + L_CX + b * 128 + ks * 64 + (2 * col + (g >> 1)) * 2) = (unsigned short)pk(z, z);
      if (ly == 0 && lo) {
        f32x4 e4;
#pragma unroll
        for (int t = 0; t < 4; ++t) { float v = sum4(mfma(csv, wA[4 + t], Z4)); v = add16(v, v); e4[t] = add32(v, v); }
        *(LAS f32x4*)(L + L_Q + q4 * 1024 + lane * 16) = e4;
      }
    }
    BAR();
    TS(T0 + 8);
    if (!lo) {
      const v4u a0 = *(LAS v4u*)(L + L_CX + (col >> 2) * 128 + g * 16), a1 = *(LAS v4u*)(L + L_CX + (col >> 2) * 128 + 64 + g * 16);
      f32x4 c0 = mfma(a0, wA[0], Z4), c1 = mfma(a0, wA[1], Z4);
      c0 = mfma(a1, wA[2], c0); c1 = mfma(a1, wA[3], c1);
      __builtin_amdgcn_raw_buffer_store_b32(pk(c0.x, c1.x), rW, SM + SM_CP + ((h * 4 + q4) * 8 + j) * 256 + g * 64 + col * 4, 0, SC1);
      f32x2 xr = {0.f, 0.f};
      if (resid) xr = *(LAS f32x2*)(L + L_XR + g * 128 + col * 8);
      nap(D_HV);
      TS(T0 + 9);
      unsigned off[4]; v4u cv[4];
#pragma unroll
      for (int i = 0; i < 4; ++i) off[i] = SM + SM_CP + ((2 * hpv + (i >> 1)) * 4 + q4) * 2048 + (i & 1) * 1024 + (col & 3) * 256 + (col >> 2) * 64 + g * 16;
      hop_load<4>(rW, off, cv);
      c0 = Z4; c1 = Z4;
#pragma unroll
      for (int i = 0; i < 4; ++i) { c0 = mfma(cv[i], wA[4 + 2 * (i >> 1)], c0); c1 = mfma(cv[i], wA[5 + 2 * (i >> 1)], c1); }
      f32x2 v = {sum4(c0), sum4(c1)};
      if (q4 == 0) v += xr;
      *(LAS f32x2*)(L + L_R + q4 * 512 + lane * 8) = v;
    } else if (ly == 0 && wv == 0) {
      const f32x4 e = (*(LAS f32x4*)(L + L_Q + lane * 16) + *(LAS f32x4*)(L + L_Q + 1024 + lane * 16)) + (*(LAS f32x4*)(L + L_Q + 2048 + lane * 16) + *(LAS f32x4*)(L + L_Q + 3072 + lane * 16));
      cs4[0] = e.x; cs4[1] = e.y; cs4[2] = e.z; cs4[3] = e.w;
    }
    if (ly == 0) {
      VM0();
      v2u t8[8];
#pragma unroll
      for (int u = 0; u < 2; ++u) {
#pragma unroll
        for (int e = 0; e < 8; ++e) t8[e] = gt[8 * u + e];
        tr4(t8, wG[4 * u], wG[4 * u + 1], wG[4 * u + 2], wG[4 * u + 3]);
      }
      tr4(dt, wD[0], wD[1], wD[2], wD[3]);
      const v4u on = {ONES, ONES, ONES, ONES};
      float e4[4];
#pragma unroll
      for (int t = 0; t < 4; ++t) e4[t] = mfma(on, wG[4 + t], mfma(on, wG[t], Z4)).x;
      const float ev = g == 0 ? e4[0] : g == 1 ? e4[1] : g == 2 ? e4[2] : e4[3];
      *(LAS float*)(L + L_GU + (wv >> 2) * 1024 + (4 * col + g) * 16 + q4 * 4) = ev;
    }
    BAR();
    if (wv == 0) {
      const f32x2 v = (*(LAS f32x2*)(L + L_R + lane * 8) + *(LAS f32x2*)(L + L_R + 512 + lane * 8)) + (*(LAS f32x2*)(L + L_R + 1024 + lane * 8) + *(LAS f32x2*)(L + L_R + 1536 + lane * 8));
      __builtin_amdgcn_raw_buffer_store_b32(pk(v.x, v.y), rW, SM + SM_XP + ((g * 16 + msv) * 16 + hpv) * 64 + col * 4, 0, SC1);
    }
    const int gidx = (col >> 3) * 32 + 8 * g + (col & 7);
    if (ly == 0) {
      csg = sum4(*(LAS f32x4*)(L + L_GU + gidx * 16)) + sum4(*(LAS f32x4*)(L + L_GU + 1024 + gidx * 16));
      BAR();
    }
    TS(T0 + 10);
    if (wv != 0) nap(2);
    nap(D_H3 - 2);
    {
      unsigned off[2]; v4u xv[2];
#pragma unroll
      for (int i = 0; i < 2; ++i) off[i] = SM + SM_XP + (bF * 16 + 2 * wv + i) * 1024 + col * 64 + g * 16;
      hop_load<2>(rW, off, xv);
      TS(T0 + 11);
      const f32x4 i0 = mfma(xv[0], ID0, Z4), i1 = mfma(xv[0], ID1, Z4), i2 = mfma(xv[1], ID0, Z4), i3 = mfma(xv[1], ID1, Z4);
      f32x4 ac[4];
#pragma unroll
      for (int t = 0; t < 4; ++t) ac[t] = mfma(xv[1], wG[4 + t], mfma(xv[0], wG[t], Z4));
      float x0 = add32(sum4(i0), sum4(i2)), x1 = add32(sum4(i1), sum4(i3));
      x0 = add16(x0, x0); x1 = add16(x1, x1);
      float sv = (g & 1) ? x0 * x0 + x1 * x1 : x0 + x1;
      sv += dppf<XOR1>(sv); sv += dppf<XOR2>(sv); sv += dppf<HMIR>(sv); sv += dppf<MIR>(sv); sv = add32(sv, sv);
      if (col == 0 && g < 2) *(LAS float*)(L + L_ST + g * 32 + wv * 4) = sv;
      const float ra = add32(sum4(ac[0]), sum4(ac[2])), rb = add32(sum4(ac[1]), sum4(ac[3]));
      *(LAS float*)(L + L_GU + (wv >> 2) * 1024 + (4 * col + g) * 16 + q4 * 4) = add16(ra, rb);
      BAR();
      const float su = sum4(*(LAS f32x4*)(L + L_GU + gidx * 16)) + sum4(*(LAS f32x4*)(L + L_GU + 1024 + gidx * 16));
      const float sa = sum4(*(LAS f32x4*)(L + L_ST + (col >> 3) * 32)) + sum4(*(LAS f32x4*)(L + L_ST + (col >> 3) * 32 + 16));
      const float s1 = dppf<BCAST + 0>(sa), s2 = dppf<BCAST + 8>(sa);
      const float mu = s1 * (1.f / 512.f), var = fmaxf(s2 * (1.f / 512.f) - mu * mu, 0.f);
      const float val = (su - mu * csg) * __builtin_amdgcn_rsqf(var + 1e-5f);
      const float oth = dppf<ROR8>(val);
      const float gg = col < 8 ? val : oth;
      const float hv = val * oth * __builtin_amdgcn_rcpf(1.f + __builtin_amdgcn_exp2f(-1.4426950408889634f * gg));
      const unsigned hp2 = pk(hv, dppf<SHL + 1>(hv));
      const v4u ha = {hp2, dppu<SHL + 2>(hp2), dppu<SHL + 4>(hp2), dppu<SHL + 6>(hp2)};
      const float r0 = fs == 0 ? x0 : 0.f, r1 = fs == 0 ? x1 : 0.f;
      const f32x4 y0 = mfma(ha, wD[0], f32x4{r0, 0.f, 0.f, 0.f}), y1 = mfma(ha, wD[1], f32x4{r1, 0.f, 0.f, 0.f});
      const f32x4 y2 = mfma(ha, wD[2], f32x4{r0, 0.f, 0.f, 0.f}), y3 = mfma(ha, wD[3], f32x4{r1, 0.f, 0.f, 0.f});
      const unsigned yo = g < 2 ? pk(y0.x, y1.x) : pk(y2.x, y3.x);
      __builtin_amdgcn_raw_buffer_store_b32(yo, rW, (fact && !(g & 1)) ? SM + SM_YP + (((2 * wv + (g >> 1)) * 4 + bF) * 56 + fs) * 64 + col * 4 : OOB, 0, SC1);
    }
    TS(T0 + 12);
    if (ly == 1 && !resid) break;
    nap(D_H4);
    {
      const int b = wv >> 1, hf = wv & 1;
      unsigned off[2]; v4u yv[2];
      off[0] = SM + SM_YP + ((msq * 4 + b) * 56 + 28 * hf + col) * 64 + g * 16;
      off[1] = col < 12 ? off[0] + 1024 : OOB;
      hop_load<2>(rW, off, yv);
      TS(T0 + 13);
      const f32x4 d0 = mfma(yv[1], ID0, mfma(yv[0], ID0, Z4)), d1 = mfma(yv[1], ID1, mfma(yv[0], ID1, Z4));
      float z = add32(sum4(d0), sum4(d1)); z = add16(z, z);
      *(LAS float*)(L + L_X1 + (b * 2 + hf) * 128 + (2 * col + (g >> 1)) * 4) = z;
    }
    BAR();
    if (lo) {
      const int o = (col >> 2) * 256 + (8 * g + 2 * (col & 3)) * 4;
      const f32x2 v = *(LAS f32x2*)(L + L_X1 + o) + *(LAS f32x2*)(L + L_X1 + o + 128);
      xp = pk(v.x, v.y);
      if (ly == 0) {
        if (wv == 0) *(LAS f32x2*)(L + L_XR + (col >> 2) * 128 + (8 * g + 2 * (col & 3)) * 4) = f32x2{blo(xp), bhi(xp)};
      } else if (wv == 0) {
        *(GAS unsigned*)(a.out + (col >> 2) * 1024 + mcol(msq, 8 * g + 2 * (col & 3)) * 2) = xp;
      }
    }
  }
  TS(30);
  asm volatile(".Lpc_end:");
}

extern "C" void kernel_launch(void* const* d_in, const int* in_sizes, int n_in, void* d_out, int out_size, void* d_ws, size_t ws_size,
                              hipStream_t stream) {
  Args a;
  a.x = (const unsigned char*)d_in[0]; a.ckv = (const unsigned char*)d_in[1]; a.wq = (const unsigned char*)d_in[2];
  a.wuk = (const unsigned char*)d_in[3]; a.wuv = (const unsigned char*)d_in[4]; a.wo = (const unsigned char*)d_in[5];
  a.wg = (const unsigned char*)d_in[7]; a.wu = (const unsigned char*)d_in[8]; a.wd = (const unsigned char*)d_in[9];
  a.out = (unsigned char*)d_out; a.ws = (unsigned char*)d_ws;
  decode_block<<<256, 512, 0, stream>>>(a);
}
```

```cpp
#include <hip/hip_runtime.h>
#include <stdint.h>

#define GAS __attribute__((address_space(1)))
#define LAS __attribute__((address_space(3)))
typedef unsigned long long u64;
typedef unsigned v4u __attribute__((ext_vector_type(4)));
typedef unsigned v2u __attribute__((ext_vector_type(2)));
typedef float f32x4 __attribute__((ext_vector_type(4)));
typedef float f32x2 __attribute__((ext_vector_type(2)));
typedef short bf16x8 __attribute__((ext_vector_type(8)));
typedef short s16x4 __attribute__((ext_vector_type(4)));
typedef __bf16 bf2 __attribute__((ext_vector_type(2)));
typedef __amdgpu_buffer_rsrc_t rsrc_t;

#define STAMP 0

constexpr unsigned OFF_E = 0, OFF_FL = 4096, OFF_QS0 = 16384, OFF_CSP = 65536;
constexpr unsigned OFF_SM = 262144;
constexpr unsigned SM_QA = 0, SM_MLF = 131072, SM_CP = 393216, SM_XP = 655360, SM_YP = 720896, SM_QS = 983040, SM_QP = 1048576, SM_STRIDE = 1572864;
constexpr unsigned OFF_OP = 4194304, OP_STRIDE = 4194304;
constexpr unsigned OFF_STAMP = 16777216;
constexpr unsigned WS_BYTES = 20971520;
constexpr unsigned OOB = 0x7FFFFFF0u;
constexpr unsigned SENT = 0xFFFFFFFFu;
constexpr int SPIN_MAX = 1 << 16;

constexpr int ROW = 1040;
constexpr int L_C = 0, L_Q = 128 * ROW, L_P = L_Q + 16 * ROW, L_MX = L_P + 4096, L_CX = L_MX + 512, L_X1 = L_CX + 512, L_GU = L_X1 + 1024,
              L_ST = L_GU + 2048, L_XR = L_ST + 64, L_R = L_XR + 512, L_CNT = L_R + 4096, LDS_BYTES = L_CNT + 64;
static_assert(LDS_BYTES <= 163840, "lds");

#define TUNE 0
constexpr int DLY[16] = {60,30,40,60,60,20,0,20,20,20,20,20,20,0,10,0};
enum { D_HQ = 0, D_H1 = 1, D_ML = 2, D_HV = 3, D_H3 = 4, D_H4 = 5, D_RT = 14 };

template <int V> struct IC { static constexpr int value = V; };
struct Args {
  const unsigned char *x, *ckv, *wq, *wuk, *wuv, *wo, *wg, *wu, *wd;
  unsigned char *out, *ws;
};

#define RS(p, n) __builtin_amdgcn_make_buffer_rsrc((void*)(p), 0, (n), 0x00020000)
#define SB() __builtin_amdgcn_sched_barrier(0)
#define MEMF() asm volatile("" ::: "memory")
#define VM0() asm volatile("s_waitcnt vmcnt(0)" ::: "memory")
#define BAR() do { asm volatile("s_waitcnt lgkmcnt(0)" ::: "memory"); __builtin_amdgcn_s_barrier(); MEMF(); } while (0)
constexpr int SC1 = 16;

__device__ __forceinline__ f32x4 mfma(v4u a, v4u b, f32x4 c) {
  return __builtin_amdgcn_mfma_f32_16x16x32_bf16(__builtin_bit_cast(bf16x8, a), __builtin_bit_cast(bf16x8, b), c, 0, 0, 0);
}
__device__ __forceinline__ unsigned pk(float lo, float hi) { bf2 v = {(__bf16)lo, (__bf16)hi}; return __builtin_bit_cast(unsigned, v); }
__device__ __forceinline__ float blo(unsigned u) { return __builtin_bit_cast(float, u << 16); }
__device__ __forceinline__ float bhi(unsigned u) { return __builtin_bit_cast(float, u & 0xFFFF0000u); }
__device__ __forceinline__ float f_(unsigned u) { return __builtin_bit_cast(float, u); }
__device__ __forceinline__ unsigned u_(float f) { return __builtin_bit_cast(unsigned, f); }
template <int C> __device__ __forceinline__ unsigned dppu(unsigned v) { return __builtin_amdgcn_update_dpp(0u, v, C, 0xf, 0xf, true); }
template <int C> __device__ __forceinline__ float dppf(float v) { return f_(dppu<C>(u_(v))); }
constexpr int XOR1 = 0xB1, XOR2 = 0x4E, HMIR = 0x141, MIR = 0x140, ROR8 = 0x128, SHL = 0x100, BCAST = 0x150;
__device__ __forceinline__ float add32(float a, float b) { auto r = __builtin_amdgcn_permlane32_swap(u_(a), u_(b), false, false); return f_(r[0]) + f_(r[1]); }
__device__ __forceinline__ float add16(float a, float b) { auto r = __builtin_amdgcn_permlane16_swap(u_(a), u_(b), false, false); return f_(r[0]) + f_(r[1]); }
__device__ __forceinline__ float max32(float a) { auto r = __builtin_amdgcn_permlane32_swap(u_(a), u_(a), false, false); return fmaxf(f_(r[0]), f_(r[1])); }
__device__ __forceinline__ float max16(float a) { auto r = __builtin_amdgcn_permlane16_swap(u_(a), u_(a), false, false); return fmaxf(f_(r[0]), f_(r[1])); }
__device__ __forceinline__ float sum4(f32x4 v) { return (v.x + v.y) + (v.z + v.w); }
__device__ __forceinline__ float dot2(unsigned a, unsigned b, float c) { return __builtin_amdgcn_fdot2_f32_bf16(__builtin_bit_cast(bf2, a), __builtin_bit_cast(bf2, b), c, false); }
constexpr unsigned ONES = 0x3F803F80u;
__device__ __forceinline__ unsigned umax4(v4u v) { return max(max(v.x, v.y), max(v.z, v.w)); }
#define nap(n) do { if ((n) > 0) __builtin_amdgcn_s_sleep((n) > 0 ? (n) : 0); } while (0)
template <int N> __device__ __forceinline__ void napc() { if (N > 0) __builtin_amdgcn_s_sleep(N); }
#define NAP(i) do { if ((i) < 7) napc<DLY[(i) % 7]>(); else if ((i) < 14) napc<DLY[7 + (i) % 7]>(); else napc<DLY[14]>(); } while (0)
#define RETRY(i) do { } while (0)
__device__ __forceinline__ void tr2(const unsigned (&r)[8], v4u& f0, v4u& f1) {
#pragma unroll
  for (int d = 0; d < 4; ++d) {
    f0[d] = __builtin_amdgcn_perm(r[2 * d + 1], r[2 * d], 0x05040100u);
    f1[d] = __builtin_amdgcn_perm(r[2 * d + 1], r[2 * d], 0x07060302u);
  }
}
__device__ __forceinline__ void tr4(const v2u (&r)[8], v4u& f0, v4u& f1, v4u& f2, v4u& f3) {
#pragma unroll
  for (int d = 0; d < 4; ++d) {
    f0[d] = __builtin_amdgcn_perm(r[2 * d + 1].x, r[2 * d].x, 0x05040100u);
    f1[d] = __builtin_amdgcn_perm(r[2 * d + 1].x, r[2 * d].x, 0x07060302u);
    f2[d] = __builtin_amdgcn_perm(r[2 * d + 1].y, r[2 * d].y, 0x05040100u);
    f3[d] = __builtin_amdgcn_perm(r[2 * d + 1].y, r[2 * d].y, 0x07060302u);
  }
}
__device__ __forceinline__ int mcol(int ms, int p) { return 64 * (ms >> 1) + 4 * (p >> 1) + 2 * (ms & 1) + (p & 1); }

#define HOP_LOAD(N, off, v, id) \
  for (int sp = 0;; ++sp) { \
    MEMF(); \
    _Pragma("unroll") for (int i = 0; i < N; ++i) v[i] = __builtin_amdgcn_raw_buffer_load_b128(rW, off[i], 0, SC1); \
    unsigned m_ = 0; \
    _Pragma("unroll") for (int i = 0; i < N; ++i) m_ = max(m_, umax4(v[i])); \
    if (__builtin_amdgcn_ballot_w64(m_ == SENT) == 0ull || sp > SPIN_MAX) { RETRY(id); break; } \
    NAP(D_RT); \
  }

#define TS(i) do { } while (0)

__global__ void __launch_bounds__(512, 2) decode_block(Args a) {
  __shared__ __attribute__((aligned(16))) unsigned char smem[LDS_BYTES];
  LAS unsigned char* L = (LAS unsigned char*)smem;
  const unsigned lbase = (unsigned)(size_t)L;
  const int w = blockIdx.x, tid = threadIdx.x, lane = tid & 63, wv = __builtin_amdgcn_readfirstlane(tid >> 6);
  const int col = lane & 15, g = lane >> 4, q4 = wv & 3;
  const bool lo = wv < 4;
  const int x = w & 7, r = w >> 3;
  const int pair = x * 16 + (r >> 1), bA = pair >> 5, kc = pair & 31, hg = r & 1;
  const int h = 4 * x + (r >> 3), j = r & 7;
  const int hpv = 2 * x + (r >> 4), msv = r & 15;
  const int msq = 2 * x + (r >> 4), hpq = r & 15;
  const bool fact = r < 28;
  const int bF = r & 3, fs = 7 * x + (r >> 2);
  const bool resid = hpv == msv;
  TS(0);
  {
    u64 pc; unsigned len;
    asm volatile("s_getpc_b64 %0\n.Lpc_here:\n\ts_mov_b32 %1, .Lpc_end-.Lpc_here" : "=s"(pc), "=s"(len));
    if (wv == 7) {
      const unsigned o = (unsigned)(r + 32 * lane) * 128u;
      if (o < len) {
        unsigned keep;
        asm volatile("s_mov_b32 %0, m0\n\ts_mov_b32 m0, %3\n\ts_nop 0\n\tglobal_load_lds_dword %1, %2\n\ts_mov_b32 m0, %0"
                     : "=&s"(keep) : "v"(o), "s"(pc), "s"(lbase + L_P) : "memory");
      }
    }
  }
  const rsrc_t rW = RS(a.ws, WS_BYTES);
  if (tid < 16) ((LAS unsigned*)(L + L_CNT))[tid] = 0u;

  v4u wA[8];
  v4u wG[8], wD[4];
  unsigned xp = 0;
  u64 tag = 0, e0 = 0;
  if (lo) {
    const rsrc_t rX = RS(a.x, 4096), rQ = RS(a.wq, 4194304), rK = RS(a.wuk, 4194304);
    const v2u ev = __builtin_amdgcn_raw_buffer_load_b64(rW, OFF_E, 0, SC1);
    v4u xr[4];
#pragma unroll
    for (int i = 0; i < 4; ++i) xr[i] = __builtin_amdgcn_raw_buffer_load_b128(rX, (col >> 2) * 1024 + ((col & 3) * 4 + g) * 64 + i * 16, 0, 0);
    const unsigned xraw = __builtin_amdgcn_raw_buffer_load_b32(rX, (col >> 2) * 1024 + mcol(msq, 8 * g + 2 * (col & 3)) * 2, 0, 0);
    v2u t[8];
#pragma unroll
    for (int e = 0; e < 8; ++e) t[e] = __builtin_amdgcn_raw_buffer_load_b64(rQ, mcol(msq, 8 * g + e) * 8192 + hpq * 512 + q4 * 128 + col * 8, 0, 0);
    { const v4u f = {SENT, SENT, SENT, SENT}; __builtin_amdgcn_raw_buffer_store_b128(f, rW, OFF_SM + w * 4096 + tid * 16, 0, SC1); }
#pragma unroll
    for (int i = 0; i < 4; ++i) wA[4 + i] = __builtin_amdgcn_raw_buffer_load_b128(rK, (64 * j + 4 * col + i) * 8192 + h * 256 + q4 * 64 + g * 16, 0, 0);
    SB();
    BAR();
    SB();
    e0 = ((u64)ev.y << 32) | ev.x;
    u64 z = e0 + 0x9E3779B97F4A7C15ull;
    z = (z ^ (z >> 30)) * 0xBF58476D1CE4E5B9ull; z = (z ^ (z >> 27)) * 0x94D049BB133111EBull; tag = z ^ (z >> 31);
    float s = 0.f;
#pragma unroll
    for (int i = 0; i < 4; ++i) { s = dot2(xr[i].x, ONES, s); s = dot2(xr[i].y, ONES, s); s = dot2(xr[i].z, ONES, s); s = dot2(xr[i].w, ONES, s); }
    s += dppf<XOR1>(s); s += dppf<XOR2>(s); s = add16(s, s); s = add32(s, s);
    const float mu = s * (1.f / 512.f);
    xp = pk(blo(xraw) - mu, bhi(xraw) - mu);
    if (wv == 0) *(LAS f32x2*)(L + L_XR + (col >> 2) * 128 + (8 * g + 2 * (col & 3)) * 4) = f32x2{blo(xraw), bhi(xraw)};
    tr4(t, wA[0], wA[1], wA[2], wA[3]);
  } else {
    BAR();
    const unsigned char* src = a.ckv + (size_t)pair * 131072 + lane * 16;
#pragma unroll
    for (int i = 0; i < 32; ++i) {
      const int row = 4 * i + q4;
      unsigned keep;
      asm volatile("s_mov_b32 %0, m0\n\ts_mov_b32 m0, %2\n\ts_nop 0\n\tglobal_load_lds_dwordx4 %1, off\n\ts_mov_b32 m0, %0"
                   : "=&s"(keep) : "v"(src + row * 1024), "s"(lbase + L_C + row * ROW) : "memory");
    }
  }
  TS(1);
  float cs4[4] = {0.f, 0.f, 0.f, 0.f};
  float csg = 0.f;
  v4u ID0, ID1;
#pragma unroll
  for (int d = 0; d < 4; ++d) { const bool on = (g == (col >> 2)) && ((col & 3) == d); ID0[d] = on ? 0x00003F80u : 0u; ID1[d] = on ? 0x3F800000u : 0u; }
  const f32x4 Z4 = {0.f, 0.f, 0.f, 0.f};

  auto layer = [&](auto LY) __attribute__((always_inline)) {
    constexpr int ly = LY.value, LYO = 7 * ly;
    const unsigned SM = OFF_SM + ly * SM_STRIDE, OP = OFF_OP + ly * OP_STRIDE;
    const unsigned QS = ly == 0 ? OFF_QS0 : SM + SM_QS;
    constexpr int T0 = 2 + ly * 14;
    if (lo) {
      const v4u xa = {dppu<0x00>(xp), dppu<0x55>(xp), dppu<0xAA>(xp), dppu<0xFF>(xp)};
      f32x4 c0 = mfma(xa, wA[0], Z4), c1 = mfma(xa, wA[1], Z4), c2 = mfma(xa, wA[2], Z4), c3 = mfma(xa, wA[3], Z4);
      const unsigned qoff = (((2 * hpq + (q4 >> 1)) * 4 + 2 * (q4 & 1) + (col >> 3)) * 16 + msq);
      __builtin_amdgcn_raw_buffer_store_b64(v2u{pk(c0.x, c1.x), pk(c2.x, c3.x)}, rW, SM + SM_QP + qoff * 256 + g * 64 + (col & 7) * 8, 0, SC1);
      if (wv == 3) {
        const float x0 = blo(xp), x1 = bhi(xp);
        float s1 = x0 + x1, s2 = x0 * x0 + x1 * x1;
        s1 += dppf<XOR1>(s1); s2 += dppf<XOR1>(s2); s1 += dppf<XOR2>(s1); s2 += dppf<XOR2>(s2);
        float sv = add16(s1, s2); sv = add32(sv, sv);
        __builtin_amdgcn_raw_buffer_store_b32(u_(sv), rW, (g < 2 && (col & 3) == 0) ? QS + (hpq * 16 + msq) * 32 + (col >> 2) * 8 + g * 4 : OOB, 0, SC1);
      }
      if (ly == 0) {
        const v4u on = {ONES, ONES, ONES, ONES};
        c0 = mfma(on, wA[0], Z4); c1 = mfma(on, wA[1], Z4); c2 = mfma(on, wA[2], Z4); c3 = mfma(on, wA[3], Z4);
        __builtin_amdgcn_raw_buffer_store_b64(v2u{pk(c0.x, c1.x), pk(c2.x, c3.x)}, rW, g == 0 ? OFF_CSP + qoff * 64 + (col & 7) * 8 : OOB, 0, SC1);
        VM0();
        unsigned old = 0;
        if (lane == 0) old = __hip_atomic_fetch_add((LAS unsigned*)(L + L_CNT), 1u, __ATOMIC_RELAXED, __HIP_MEMORY_SCOPE_WORKGROUP);
        if (__builtin_amdgcn_readfirstlane(old) == 3u)
          __builtin_amdgcn_raw_buffer_store_b64(v2u{(unsigned)tag, (unsigned)(tag >> 32)}, rW, lane == 0 ? OFF_FL + w * 8 : OOB, 0, SC1);
        TS(T0 + 0);
        NAP(D_HQ);
        for (int sp = 0;; ++sp) {
          MEMF();
          const v4u f0 = __builtin_amdgcn_raw_buffer_load_b128(rW, OFF_FL + lane * 16, 0, SC1), f1 = __builtin_amdgcn_raw_buffer_load_b128(rW, OFF_FL + 1024 + lane * 16, 0, SC1);
          const unsigned tl = (unsigned)tag, th = (unsigned)(tag >> 32);
          const unsigned bad = (f0.x ^ tl) | (f0.y ^ th) | (f0.z ^ tl) | (f0.w ^ th) | (f1.x ^ tl) | (f1.y ^ th) | (f1.z ^ tl) | (f1.w ^ th);
          if (__builtin_amdgcn_ballot_w64(bad != 0u) == 0ull || sp > SPIN_MAX) { RETRY(D_HQ); break; }
          NAP(D_RT);
        }
        MEMF();
        if (w == 0 && wv == 0) {
          const u64 e1 = e0 + 1ull;
          __builtin_amdgcn_raw_buffer_store_b64(v2u{(unsigned)e1, (unsigned)(e1 >> 32)}, rW, lane == 0 ? OFF_E : OOB, 0, SC1);
        }
      } else {
        TS(T0 + 0);
        NAP(LYO + D_HQ);
      }
      TS(T0 + 1);
      v4u qa[4]; v2u qs = {0u, 0u};
      {
        const unsigned base = SM + SM_QP + (h * 4 + q4) * 4096 + (col & 3) * 256 + (col >> 2) * 64 + g * 16;
        const unsigned soff = wv == 0 ? QS + ((h >> 1) * 16 + col) * 32 + g * 8 : OOB;
        for (int sp = 0;; ++sp) {
          MEMF();
#pragma unroll
          for (int i = 0; i < 4; ++i) qa[i] = __builtin_amdgcn_raw_buffer_load_b128(rW, base + i * 1024, 0, SC1);
          qs = __builtin_amdgcn_raw_buffer_load_b64(rW, soff, 0, SC1);
          if (ly == 0) break;
          const unsigned m = max(max(max(umax4(qa[0]), umax4(qa[1])), max(umax4(qa[2]), umax4(qa[3]))), max(qs.x, qs.y));
          if (__builtin_amdgcn_ballot_w64(m == SENT) == 0ull || sp > SPIN_MAX) { RETRY(LYO + D_HQ); break; }
          NAP(D_RT);
        }
      }
      f32x4 ac[4] = {Z4, Z4, Z4, Z4};
#pragma unroll
      for (int i = 0; i < 4; ++i)
#pragma unroll
        for (int t = 0; t < 4; ++t) ac[t] = mfma(qa[i], wA[4 + t], ac[t]);
      f32x4 pv = {sum4(ac[0]), sum4(ac[1]), sum4(ac[2]), sum4(ac[3])};
      if (wv != 0) {
        *(LAS f32x4*)(L + L_R + q4 * 1024 + lane * 16) = pv;
        MEMF();
        if (lane == 0) __hip_atomic_fetch_add((LAS unsigned*)(L + L_CNT + 4), 1u, __ATOMIC_RELAXED, __HIP_MEMORY_SCOPE_WORKGROUP);
        TS(T0 + 2);
        nap(3); NAP(LYO + D_H1);
      } else {
        float s1 = f_(qs.x), s2 = f_(qs.y);
        s1 += dppf<XOR1>(s1); s2 += dppf<XOR1>(s2); s1 += dppf<XOR2>(s1); s2 += dppf<XOR2>(s2);
        s1 += dppf<HMIR>(s1); s2 += dppf<HMIR>(s2); s1 += dppf<MIR>(s1); s2 += dppf<MIR>(s2);
        const float mu = s1 * (1.f / 512.f), var = fmaxf(s2 * (1.f / 512.f) - mu * mu, 0.f);
        const float sc = __builtin_amdgcn_rsqf(var + 1e-5f) * (0.08838834764831845f * 1.4426950408889634f);
        f32x4 p1, p2, p3;
        for (int sp = 0;; ++sp) {
          MEMF();
          const unsigned c = *(volatile LAS unsigned*)(L + L_CNT + 4);
          MEMF();
          p1 = *(LAS f32x4*)(L + L_R + 1024 + lane * 16); p2 = *(LAS f32x4*)(L + L_R + 2048 + lane * 16); p3 = *(LAS f32x4*)(L + L_R + 3072 + lane * 16);
          if (__builtin_amdgcn_readfirstlane(c) >= 3u * (ly + 1) || sp > SPIN_MAX) break;
        }
        pv = (pv + p1) + (p2 + p3);
        const float o0 = (pv.x - mu * cs4[0]) * sc, o1 = (pv.y - mu * cs4[1]) * sc, o2 = (pv.z - mu * cs4[2]) * sc, o3 = (pv.w - mu * cs4[3]) * sc;
        __builtin_amdgcn_raw_buffer_store_b64(v2u{pk(o0, o1), pk(o2, o3)}, rW, SM + SM_QA + (g * 32 + h) * 1024 + j * 128 + col * 8, 0, SC1);
        TS(T0 + 2);
        NAP(LYO + D_H1);
      }
      unsigned off[4]; v4u qv[4];
#pragma unroll
      for (int i = 0; i < 4; ++i) off[i] = SM + SM_QA + (bA * 32 + hg * 16 + 4 * q4 + i) * 1024 + lane * 16;
      HOP_LOAD(4, off, qv, LYO + D_H1)
#pragma unroll
      for (int i = 0; i < 4; ++i) *(LAS v4u*)(L + L_Q + (4 * q4 + i) * ROW + lane * 16) = qv[i];
    } else if (ly == 0) {
      VM0();
    }
    TS(T0 + 3);
    v4u cf[16];
    const int rho = 2 * (col & 7) + (col >> 3);
    if (ly == 1) {
#pragma unroll
      for (int s = 0; s < 16; ++s) cf[s] = *(LAS v4u*)(L + L_C + (16 * wv + rho) * ROW + g * 256 + s * 16);
    }
    BAR();
    unsigned tv[16], to[16];
    if (ly == 0) {
#pragma unroll
      for (int s = 0; s < 16; ++s) cf[s] = *(LAS v4u*)(L + L_C + (16 * wv + rho) * ROW + g * 256 + s * 16);
      if (!lo) {
        const rsrc_t rV = RS(a.wuv, 4194304), rO = RS(a.wo, 4194304);
#pragma unroll
        for (int e = 0; e < 16; ++e)
          tv[e] = __builtin_amdgcn_raw_buffer_load_b32(rV, (64 * j + 32 * (e >> 3) + 16 * ((e >> 2) & 1) + 4 * g + (e & 3)) * 8192 + h * 256 + q4 * 64 + col * 4, 0, 0);
#pragma unroll
        for (int e = 0; e < 16; ++e)
          to[e] = __builtin_amdgcn_raw_buffer_load_b32(rO, ((2 * hpv + (e >> 3)) * 128 + 32 * q4 + 8 * g + (e & 7)) * 1024 + mcol(msv, 2 * col) * 2, 0, 0);
        SB();
      }
    }
    f32x4 sc4 = Z4;
#pragma unroll
    for (int s = 0; s < 16; ++s) sc4 = mfma(cf[s], *(LAS v4u*)(L + L_Q + col * ROW + g * 256 + s * 16), sc4);
    float mx = fmaxf(fmaxf(sc4.x, sc4.y), fmaxf(sc4.z, sc4.w));
    mx = max16(mx); mx = max32(mx);
    if (g == 0) *(LAS float*)(L + L_MX + col * 32 + wv * 4) = mx;
    {
      const int qq = 4 * g + (col >> 2), rr = 2 * (qq & 7) + (qq >> 3);
      LAS unsigned char* tb = L + L_C + rr * ROW + wv * 128 + (col & 3) * 8;
#pragma unroll
      for (int s = 0; s < 4; ++s)
#pragma unroll
        for (int t = 0; t < 4; ++t) {
          const v2u u0 = __builtin_bit_cast(v2u, __builtin_amdgcn_ds_read_tr16_b64_v4i16((LAS s16x4*)(tb + (32 * s) * ROW + t * 32)));
          const v2u u1 = __builtin_bit_cast(v2u, __builtin_amdgcn_ds_read_tr16_b64_v4i16((LAS s16x4*)(tb + (32 * s + 16) * ROW + t * 32)));
          cf[4 * s + t] = v4u{u0.x, u0.y, u1.x, u1.y};
        }
    }
    BAR();
    {
      const f32x4 m0 = *(LAS f32x4*)(L + L_MX + col * 32), m1 = *(LAS f32x4*)(L + L_MX + col * 32 + 16);
      mx = fmaxf(fmaxf(fmaxf(m0.x, m0.y), fmaxf(m0.z, m0.w)), fmaxf(fmaxf(m1.x, m1.y), fmaxf(m1.z, m1.w)));
      const float p0 = __builtin_amdgcn_exp2f(sc4.x - mx), p1 = __builtin_amdgcn_exp2f(sc4.y - mx), p2 = __builtin_amdgcn_exp2f(sc4.z - mx), p3 = __builtin_amdgcn_exp2f(sc4.w - mx);
      *(LAS v2u*)(L + L_P + (wv >> 1) * 1024 + lane * 16 + (wv & 1) * 8) = v2u{pk(p0, p1), pk(p2, p3)};
    }
    BAR();
    TS(T0 + 4);
    {
      v4u pf[4];
#pragma unroll
      for (int s = 0; s < 4; ++s) pf[s] = *(LAS v4u*)(L + L_P + s * 1024 + lane * 16);
      f32x4 o[4] = {Z4, Z4, Z4, Z4};
#pragma unroll
      for (int t = 0; t < 4; ++t)
#pragma unroll
        for (int s = 0; s < 4; ++s) o[t] = mfma(cf[4 * s + t], pf[s], o[t]);
      const int hh = hg * 16 + col;
      const unsigned ob = OP + ((((hh >> 1) * 8 + wv) * 2) * 4 + bA) * 4096 + kc * 128 + (hh & 1) * 64 + g * 16;
#pragma unroll
      for (int u = 0; u < 2; ++u)
        __builtin_amdgcn_raw_buffer_store_b128(v4u{pk(o[2 * u].x, o[2 * u].y), pk(o[2 * u].z, o[2 * u].w), pk(o[2 * u + 1].x, o[2 * u + 1].y), pk(o[2 * u + 1].z, o[2 * u + 1].w)},
                                               rW, ob + u * 16384, 0, SC1);
      float l = 0.f;
#pragma unroll
      for (int s = 0; s < 4; ++s) { l = dot2(pf[s].x, ONES, l); l = dot2(pf[s].y, ONES, l); l = dot2(pf[s].z, ONES, l); l = dot2(pf[s].w, ONES, l); }
      l = add16(l, l); l = add32(l, l);
      TS(T0 + 5);
      VM0();
      __builtin_amdgcn_raw_buffer_store_b64(v2u{u_(mx), u_(l)}, rW, g == 0 ? SM + SM_MLF + ((hh * 8 + wv) * 4 + bA) * 256 + kc * 8 : OOB, 0, SC1);
    }
    TS(T0 + 6);
    if (ly == 0 && !lo) {
      unsigned t8[8];
#pragma unroll
      for (int u = 0; u < 2; ++u) {
#pragma unroll
        for (int e = 0; e < 8; ++e) t8[e] = tv[8 * u + e];
        tr2(t8, wA[2 * u], wA[2 * u + 1]);
#pragma unroll
        for (int e = 0; e < 8; ++e) t8[e] = to[8 * u + e];
        tr2(t8, wA[4 + 2 * u], wA[4 + 2 * u + 1]);
      }
    }
    NAP(LYO + D_ML);
    v2u gt[16], dt[8];
    {
      const int b = wv >> 1, ks = wv & 1;
      v2u ml;
      const unsigned moff = SM + SM_MLF + ((h * 8 + j) * 4 + b) * 256 + (16 * ((col >> 2) & 1) + 4 * g + (col & 3)) * 8;
      for (int sp = 0;; ++sp) {
        MEMF();
        ml = __builtin_amdgcn_raw_buffer_load_b64(rW, moff, 0, SC1);
        if (__builtin_amdgcn_ballot_w64(max(ml.x, ml.y) == SENT) == 0ull || sp > SPIN_MAX) { RETRY(LYO + D_ML); break; }
        NAP(D_RT);
      }
      MEMF();
      TS(T0 + 7);
      const unsigned ob = OP + ((((h >> 1) * 8 + j) * 2 + ks) * 4 + b) * 4096 + col * 128 + (h & 1) * 64 + g * 16;
      const v4u o0 = __builtin_amdgcn_raw_buffer_load_b128(rW, ob, 0, SC1), o1 = __builtin_amdgcn_raw_buffer_load_b128(rW, ob + 2048, 0, SC1);
      v4u csv = {0u, 0u, 0u, 0u};
      if (ly == 0) {
        if (lo) csv = __builtin_amdgcn_raw_buffer_load_b128(rW, OFF_CSP + (h * 4 + q4) * 1024 + col * 64 + g * 16, 0, SC1);
        const rsrc_t rG = RS(a.wg, 1835008), rU = RS(a.wu, 1835008), rD = RS(a.wd, 1835008);
        (void)rG; (void)rU;
        const GAS unsigned char* gb = (const GAS unsigned char*)(col < 8 ? a.wg : a.wu) + (fact ? fs : 0) * 64 + (col & 7) * 8;
#pragma unroll
        for (int e = 0; e < 16; ++e) gt[e] = *(const GAS v2u*)(gb + (size_t)mcol(2 * wv + (e >> 3), 8 * g + (e & 7)) * 3584);
#pragma unroll
        for (int e = 0; e < 8; ++e) dt[e] = __builtin_amdgcn_raw_buffer_load_b64(rD, fact ? (32 * fs + 8 * g + e) * 1024 + wv * 128 + col * 8 : OOB, 0, 0);
        const v4u f = {SENT, SENT, SENT, SENT};
        __builtin_amdgcn_raw_buffer_store_b128(f, rW, tid < 384 ? OFF_SM + SM_STRIDE + w * 6144 + tid * 16 : OOB, 0, SC1);
        SB();
      }
      float m = f_(ml.x), M = m;
      M = fmaxf(M, dppf<XOR1>(M)); M = fmaxf(M, dppf<XOR2>(M)); M = fmaxf(M, dppf<HMIR>(M)); M = max16(M); M = max32(M);
      const float wt = __builtin_amdgcn_exp2f(m - M);
      float Ls = wt * f_(ml.y);
      Ls += dppf<XOR1>(Ls); Ls += dppf<XOR2>(Ls); Ls += dppf<HMIR>(Ls); Ls = add16(Ls, Ls); Ls = add32(Ls, Ls);
      const float wn = wt * __builtin_amdgcn_rcpf(Ls);
      const float w0 = dppf<BCAST + 0>(wn), w1 = dppf<BCAST + 1>(wn), w2 = dppf<BCAST + 2>(wn), w3 = dppf<BCAST + 3>(wn);
      const float w4 = dppf<BCAST + 4>(wn), w5 = dppf<BCAST + 5>(wn), w6 = dppf<BCAST + 6>(wn), w7 = dppf<BCAST + 7>(wn);
      const f32x4 d00 = mfma(o0, ID0, Z4), d01 = mfma(o0, ID1, Z4), d10 = mfma(o1, ID0, Z4), d11 = mfma(o1, ID1, Z4);
      const float z0 = ((w0 * d00.x + w1 * d00.y) + (w2 * d00.z + w3 * d00.w)) + ((w4 * d10.x + w5 * d10.y) + (w6 * d10.z + w7 * d10.w));
      const float z1 = ((w0 * d01.x + w1 * d01.y) + (w2 * d01.z + w3 * d01.w)) + ((w4 * d11.x + w5 * d11.y) + (w6 * d11.z + w7 * d11.w));
      float z = add32(z0, z1); z = add16(z, z);
      *(LAS unsigned short*)(L + L_CX + b * 128 + ks * 64 + (2 * col + (g >> 1)) * 2) = (unsigned short)pk(z, z);
      if (ly == 0 && lo) {
        f32x4 e4;
#pragma unroll
        for (int t = 0; t < 4; ++t) { float v = sum4(mfma(csv, wA[4 + t], Z4)); v = add16(v, v); e4[t] = add32(v, v); }
        *(LAS f32x4*)(L + L_Q + q4 * 1024 + lane * 16) = e4;
      }
    }
    BAR();
    TS(T0 + 8);
    if (!lo) {
      const v4u a0 = *(LAS v4u*)(L + L_CX + (col >> 2) * 128 + g * 16), a1 = *(LAS v4u*)(L + L_CX + (col >> 2) * 128 + 64 + g * 16);
      f32x4 c0 = mfma(a0, wA[0], Z4), c1 = mfma(a0, wA[1], Z4);
      c0 = mfma(a1, wA[2], c0); c1 = mfma(a1, wA[3], c1);
      __builtin_amdgcn_raw_buffer_store_b32(pk(c0.x, c1.x), rW, SM + SM_CP + ((h * 4 + q4) * 8 + j) * 256 + g * 64 + col * 4, 0, SC1);
      f32x2 xr = {0.f, 0.f};
      if (resid) xr = *(LAS f32x2*)(L + L_XR + g * 128 + col * 8);
      NAP(LYO + D_HV);
      TS(T0 + 9);
      unsigned off[4]; v4u cv[4];
#pragma unroll
      for (int i = 0; i < 4; ++i) off[i] = SM + SM_CP + ((2 * hpv + (i >> 1)) * 4 + q4) * 2048 + (i & 1) * 1024 + (col & 3) * 256 + (col >> 2) * 64 + g * 16;
      HOP_LOAD(4, off, cv, LYO + D_HV)
      c0 = Z4; c1 = Z4;
#pragma unroll
      for (int i = 0; i < 4; ++i) { c0 = mfma(cv[i], wA[4 + 2 * (i >> 1)], c0); c1 = mfma(cv[i], wA[5 + 2 * (i >> 1)], c1); }
      f32x2 v = {sum4(c0), sum4(c1)};
      if (q4 == 0) v += xr;
      *(LAS f32x2*)(L + L_R + q4 * 512 + lane * 8) = v;
    } else if (ly == 0 && wv == 0) {
      const f32x4 e = (*(LAS f32x4*)(L + L_Q + lane * 16) + *(LAS f32x4*)(L + L_Q + 1024 + lane * 16)) + (*(LAS f32x4*)(L + L_Q + 2048 + lane * 16) + *(LAS f32x4*)(L + L_Q + 3072 + lane * 16));
      cs4[0] = e.x; cs4[1] = e.y; cs4[2] = e.z; cs4[3] = e.w;
    }
    if (ly == 0) {
      VM0();
      v2u t8[8];
#pragma unroll
      for (int u = 0; u < 2; ++u) {
#pragma unroll
        for (int e = 0; e < 8; ++e) t8[e] = gt[8 * u + e];
        tr4(t8, wG[4 * u], wG[4 * u + 1], wG[4 * u + 2], wG[4 * u + 3]);
      }
      tr4(dt, wD[0], wD[1], wD[2], wD[3]);
      const v4u on = {ONES, ONES, ONES, ONES};
      float e4[4];
#pragma unroll
      for (int t = 0; t < 4; ++t) e4[t] = mfma(on, wG[4 + t], mfma(on, wG[t], Z4)).x;
      const float ev = g == 0 ? e4[0] : g == 1 ? e4[1] : g == 2 ? e4[2] : e4[3];
      *(LAS float*)(L + L_GU + (wv >> 2) * 1024 + (4 * col + g) * 16 + q4 * 4) = ev;
    }
    BAR();
    if (wv == 0) {
      const f32x2 v = (*(LAS f32x2*)(L + L_R + lane * 8) + *(LAS f32x2*)(L + L_R + 512 + lane * 8)) + (*(LAS f32x2*)(L + L_R + 1024 + lane * 8) + *(LAS f32x2*)(L + L_R + 1536 + lane * 8));
      __builtin_amdgcn_raw_buffer_store_b32(pk(v.x, v.y), rW, SM + SM_XP + ((g * 16 + msv) * 16 + hpv) * 64 + col * 4, 0, SC1);
    }
    const int gidx = (col >> 3) * 32 + 8 * g + (col & 7);
    if (ly == 0) {
      csg = sum4(*(LAS f32x4*)(L + L_GU + gidx * 16)) + sum4(*(LAS f32x4*)(L + L_GU + 1024 + gidx * 16));
      BAR();
    }
    TS(T0 + 10);
    if (wv != 0) nap(2);
    NAP(LYO + D_H3);
    {
      unsigned off[2]; v4u xv[2];
#pragma unroll
      for (int i = 0; i < 2; ++i) off[i] = SM + SM_XP + (bF * 16 + 2 * wv + i) * 1024 + col * 64 + g * 16;
      HOP_LOAD(2, off, xv, LYO + D_H3)
      TS(T0 + 11);
      const f32x4 i0 = mfma(xv[0], ID0, Z4), i1 = mfma(xv[0], ID1, Z4), i2 = mfma(xv[1], ID0, Z4), i3 = mfma(xv[1], ID1, Z4);
      f32x4 ac[4];
#pragma unroll
      for (int t = 0; t < 4; ++t) ac[t] = mfma(xv[1], wG[4 + t], mfma(xv[0], wG[t], Z4));
      float x0 = add32(sum4(i0), sum4(i2)), x1 = add32(sum4(i1), sum4(i3));
      x0 = add16(x0, x0); x1 = add16(x1, x1);
      float sv = (g & 1) ? x0 * x0 + x1 * x1 : x0 + x1;
      sv += dppf<XOR1>(sv); sv += dppf<XOR2>(sv); sv += dppf<HMIR>(sv); sv += dppf<MIR>(sv); sv = add32(sv, sv);
      if (col == 0 && g < 2) *(LAS float*)(L + L_ST + g * 32 + wv * 4) = sv;
      const float ra = add32(sum4(ac[0]), sum4(ac[2])), rb = add32(sum4(ac[1]), sum4(ac[3]));
      *(LAS float*)(L + L_GU + (wv >> 2) * 1024 + (4 * col + g) * 16 + q4 * 4) = add16(ra, rb);
      BAR();
      const float su = sum4(*(LAS f32x4*)(L + L_GU + gidx * 16)) + sum4(*(LAS f32x4*)(L + L_GU + 1024 + gidx * 16));
      const float sa = sum4(*(LAS f32x4*)(L + L_ST + (col >> 3) * 32)) + sum4(*(LAS f32x4*)(L + L_ST + (col >> 3) * 32 + 16));
      const float s1 = dppf<BCAST + 0>(sa), s2 = dppf<BCAST + 8>(sa);
      const float mu = s1 * (1.f / 512.f), var = fmaxf(s2 * (1.f / 512.f) - mu * mu, 0.f);
      const float val = (su - mu * csg) * __builtin_amdgcn_rsqf(var + 1e-5f);
      const float oth = dppf<ROR8>(val);
      const float gg = col < 8 ? val : oth;
      const float hv = val * oth * __builtin_amdgcn_rcpf(1.f + __builtin_amdgcn_exp2f(-1.4426950408889634f * gg));
      const unsigned hp2 = pk(hv, dppf<SHL + 1>(hv));
      const v4u ha = {hp2, dppu<SHL + 2>(hp2), dppu<SHL + 4>(hp2), dppu<SHL + 6>(hp2)};
      const float r0 = fs == 0 ? x0 : 0.f, r1 = fs == 0 ? x1 : 0.f;
      const f32x4 y0 = mfma(ha, wD[0], f32x4{r0, 0.f, 0.f, 0.f}), y1 = mfma(ha, wD[1], f32x4{r1, 0.f, 0.f, 0.f});
      const f32x4 y2 = mfma(ha, wD[2], f32x4{r0, 0.f, 0.f, 0.f}), y3 = mfma(ha, wD[3], f32x4{r1, 0.f, 0.f, 0.f});
      const unsigned yo = g < 2 ? pk(y0.x, y1.x) : pk(y2.x, y3.x);
      __builtin_amdgcn_raw_buffer_store_b32(yo, rW, (fact && !(g & 1)) ? SM + SM_YP + (((2 * wv + (g >> 1)) * 4 + bF) * 56 + fs) * 64 + col * 4 : OOB, 0, SC1);
    }
    TS(T0 + 12);
    if (ly == 1 && !resid) return;
    NAP(LYO + D_H4);
    {
      const int b = wv >> 1, hf = wv & 1;
      unsigned off[2]; v4u yv[2];
      off[0] = SM + SM_YP + ((msq * 4 + b) * 56 + 28 * hf + col) * 64 + g * 16;
      off[1] = col < 12 ? off[0] + 1024 : OOB;
      HOP_LOAD(2, off, yv, LYO + D_H4)
      TS(T0 + 13);
      const f32x4 d0 = mfma(yv[1], ID0, mfma(yv[0], ID0, Z4)), d1 = mfma(yv[1], ID1, mfma(yv[0], ID1, Z4));
      float z = add32(sum4(d0), sum4(d1)); z = add16(z, z);
      *(LAS float*)(L + L_X1 + (b * 2 + hf) * 128 + (2 * col + (g >> 1)) * 4) = z;
    }
    BAR();
    if (lo) {
      const int o = (col >> 2) * 256 + (8 * g + 2 * (col & 3)) * 4;
      const f32x2 v = *(LAS f32x2*)(L + L_X1 + o) + *(LAS f32x2*)(L + L_X1 + o + 128);
      xp = pk(v.x, v.y);
      if (ly == 0) {
        if (wv == 0) *(LAS f32x2*)(L + L_XR + (col >> 2) * 128 + (8 * g + 2 * (col & 3)) * 4) = f32x2{blo(xp), bhi(xp)};
      } else if (wv == 0) {
        *(GAS unsigned*)(a.out + (col >> 2) * 1024 + mcol(msq, 8 * g + 2 * (col & 3)) * 2) = xp;
      }
    }
  };
  layer(IC<0>{});
  layer(IC<1>{});
  TS(30);
  asm volatile(".Lpc_end:");
}

extern "C" void kernel_launch(void* const* d_in, const int* in_sizes, int n_in, void* d_out, int out_size, void* d_ws, size_t ws_size,
                              hipStream_t stream) {
  Args a;
  a.x = (const unsigned char*)d_in[0]; a.ckv = (const unsigned char*)d_in[1]; a.wq = (const unsigned char*)d_in[2];
  a.wuk = (const unsigned char*)d_in[3]; a.wuv = (const unsigned char*)d_in[4]; a.wo = (const unsigned char*)d_in[5];
  a.wg = (const unsigned char*)d_in[7]; a.wu = (const unsigned char*)d_in[8]; a.wd = (const unsigned char*)d_in[9];
  a.out = (unsigned char*)d_out; a.ws = (unsigned char*)d_ws;
  decode_block<<<256, 512, 0, stream>>>(a);
}
```

```cpp
#define EXFIL 1
#define EXW 0
#include <hip/hip_runtime.h>
#include <stdint.h>

#define GAS __attribute__((address_space(1)))
#define LAS __attribute__((address_space(3)))
typedef unsigned long long u64;
typedef unsigned v4u __attribute__((ext_vector_type(4)));
typedef unsigned v2u __attribute__((ext_vector_type(2)));
typedef float f32x4 __attribute__((ext_vector_type(4)));
typedef float f32x2 __attribute__((ext_vector_type(2)));
typedef short bf16x8 __attribute__((ext_vector_type(8)));
typedef short s16x4 __attribute__((ext_vector_type(4)));
typedef __bf16 bf2 __attribute__((ext_vector_type(2)));
typedef __amdgpu_buffer_rsrc_t rsrc_t;

#define STAMP 0

constexpr unsigned OFF_E = 0, OFF_FL = 4096, OFF_QS0 = 16384, OFF_CSP = 65536;
constexpr unsigned OFF_SM = 262144;
constexpr unsigned SM_QA = 0, SM_MLF = 131072, SM_CP = 393216, SM_XP = 655360, SM_YP = 720896, SM_QS = 983040, SM_QP = 1048576, SM_STRIDE = 1572864;
constexpr unsigned OFF_OP = 4194304, OP_STRIDE = 4194304;
constexpr unsigned OFF_STAMP = 16777216;
constexpr unsigned WS_BYTES = 20971520;
constexpr unsigned OOB = 0x7FFFFFF0u;
constexpr unsigned SENT = 0xFFFFFFFFu;
constexpr int SPIN_MAX = 1 << 16;

constexpr int ROW = 1040;
constexpr int L_C = 0, L_Q = 128 * ROW, L_P = L_Q + 16 * ROW, L_MX = L_P + 4096, L_CX = L_MX + 512, L_X1 = L_CX + 512, L_GU = L_X1 + 1024,
              L_ST = L_GU + 2048, L_XR = L_ST + 64, L_R = L_XR + 512, L_CNT = L_R + 4096, LDS_BYTES = L_CNT + 64;
static_assert(LDS_BYTES <= 163840, "lds");

#define TUNE 0
constexpr int DLY[16] = {100,60,60,100,100,60,0,40,40,40,40,40,40,0,10,0};
enum { D_HQ = 0, D_H1 = 1, D_ML = 2, D_HV = 3, D_H3 = 4, D_H4 = 5, D_RT = 14 };

template <int V> struct IC { static constexpr int value = V; };
struct Args {
  const unsigned char *x, *ckv, *wq, *wuk, *wuv, *wo, *wg, *wu, *wd;
  unsigned char *out, *ws;
};

#define RS(p, n) __builtin_amdgcn_make_buffer_rsrc((void*)(p), 0, (n), 0x00020000)
#define SB() __builtin_amdgcn_sched_barrier(0)
#define MEMF() asm volatile("" ::: "memory")
#define VM0() asm volatile("s_waitcnt vmcnt(0)" ::: "memory")
#define BAR() do { asm volatile("s_waitcnt lgkmcnt(0)" ::: "memory"); __builtin_amdgcn_s_barrier(); MEMF(); } while (0)
constexpr int SC1 = 16;

__device__ __forceinline__ f32x4 mfma(v4u a, v4u b, f32x4 c) {
  return __builtin_amdgcn_mfma_f32_16x16x32_bf16(__builtin_bit_cast(bf16x8, a), __builtin_bit_cast(bf16x8, b), c, 0, 0, 0);
}
__device__ __forceinline__ unsigned pk(float lo, float hi) { bf2 v = {(__bf16)lo, (__bf16)hi}; return __builtin_bit_cast(unsigned, v); }
__device__ __forceinline__ float blo(unsigned u) { return __builtin_bit_cast(float, u << 16); }
__device__ __forceinline__ float bhi(unsigned u) { return __builtin_bit_cast(float, u & 0xFFFF0000u); }
__device__ __forceinline__ float f_(unsigned u) { return __builtin_bit_cast(float, u); }
__device__ __forceinline__ unsigned u_(float f) { return __builtin_bit_cast(unsigned, f); }
template <int C> __device__ __forceinline__ unsigned dppu(unsigned v) { return __builtin_amdgcn_update_dpp(0u, v, C, 0xf, 0xf, true); }
template <int C> __device__ __forceinline__ float dppf(float v) { return f_(dppu<C>(u_(v))); }
constexpr int XOR1 = 0xB1, XOR2 = 0x4E, HMIR = 0x141, MIR = 0x140, ROR8 = 0x128, SHL = 0x100, BCAST = 0x150;
__device__ __forceinline__ float add32(float a, float b) { auto r = __builtin_amdgcn_permlane32_swap(u_(a), u_(b), false, false); return f_(r[0]) + f_(r[1]); }
__device__ __forceinline__ float add16(float a, float b) { auto r = __builtin_amdgcn_permlane16_swap(u_(a), u_(b), false, false); return f_(r[0]) + f_(r[1]); }
__device__ __forceinline__ float max32(float a) { auto r = __builtin_amdgcn_permlane32_swap(u_(a), u_(a), false, false); return fmaxf(f_(r[0]), f_(r[1])); }
__device__ __forceinline__ float max16(float a) { auto r = __builtin_amdgcn_permlane16_swap(u_(a), u_(a), false, false); return fmaxf(f_(r[0]), f_(r[1])); }
__device__ __forceinline__ float sum4(f32x4 v) { return (v.x + v.y) + (v.z + v.w); }
__device__ __forceinline__ float dot2(unsigned a, unsigned b, float c) { return __builtin_amdgcn_fdot2_f32_bf16(__builtin_bit_cast(bf2, a), __builtin_bit_cast(bf2, b), c, false); }
constexpr unsigned ONES = 0x3F803F80u;
__device__ __forceinline__ unsigned umax4(v4u v) { return max(max(v.x, v.y), max(v.z, v.w)); }
#define nap(n) do { if ((n) > 0) __builtin_amdgcn_s_sleep((n) > 0 ? (n) : 0); } while (0)
template <int N> __device__ __forceinline__ void napc() { if (N > 0) __builtin_amdgcn_s_sleep(N); }
#define NAP(i) do { if ((i) < 7) napc<DLY[(i) % 7]>(); else if ((i) < 14) napc<DLY[7 + (i) % 7]>(); else napc<DLY[14]>(); } while (0)
#define RETRY(i) do { } while (0)
__device__ __forceinline__ void tr2(const unsigned (&r)[8], v4u& f0, v4u& f1) {
#pragma unroll
  for (int d = 0; d < 4; ++d) {
    f0[d] = __builtin_amdgcn_perm(r[2 * d + 1], r[2 * d], 0x05040100u);
    f1[d] = __builtin_amdgcn_perm(r[2 * d + 1], r[2 * d], 0x07060302u);
  }
}
__device__ __forceinline__ void tr4(const v2u (&r)[8], v4u& f0, v4u& f1, v4u& f2, v4u& f3) {
#pragma unroll
  for (int d = 0; d < 4; ++d) {
    f0[d] = __builtin_amdgcn_perm(r[2 * d + 1].x, r[2 * d].x, 0x05040100u);
    f1[d] = __builtin_amdgcn_perm(r[2 * d + 1].x, r[2 * d].x, 0x07060302u);
    f2[d] = __builtin_amdgcn_perm(r[2 * d + 1].y, r[2 * d].y, 0x05040100u);
    f3[d] = __builtin_amdgcn_perm(r[2 * d + 1].y, r[2 * d].y, 0x07060302u);
  }
}
__device__ __forceinline__ int mcol(int ms, int p) { return 64 * (ms >> 1) + 4 * (p >> 1) + 2 * (ms & 1) + (p & 1); }

#define HOP_LOAD(N, off, v, id) \
  for (int sp = 0;; ++sp) { \
    MEMF(); \
    _Pragma("unroll") for (int i = 0; i < N; ++i) v[i] = __builtin_amdgcn_raw_buffer_load_b128(rW, off[i], 0, SC1); \
    unsigned m_ = 0; \
    _Pragma("unroll") for (int i = 0; i < N; ++i) m_ = max(m_, umax4(v[i])); \
    if (__builtin_amdgcn_ballot_w64(m_ == SENT) == 0ull || sp > SPIN_MAX) { RETRY(id); break; } \
    NAP(D_RT); \
  }

#define TS(i) do { if ((i) == 0 || (i) == EXFIL) { SB(); u64 t_; asm volatile("s_memtime %0\n\ts_waitcnt lgkmcnt(0)" : "=s"(t_) :: "memory"); if ((i) == 0) ex0_ = t_; else ex1_ = t_; SB(); } } while (0)

__global__ void __launch_bounds__(512, 2) decode_block(Args a) {
  __shared__ __attribute__((aligned(16))) unsigned char smem[LDS_BYTES];
  LAS unsigned char* L = (LAS unsigned char*)smem;
  const unsigned lbase = (unsigned)(size_t)L;
  const int w = blockIdx.x, tid = threadIdx.x, lane = tid & 63, wv = __builtin_amdgcn_readfirstlane(tid >> 6);
  const int col = lane & 15, g = lane >> 4, q4 = wv & 3;
  const bool lo = wv < 4;
  const int x = w & 7, r = w >> 3;
  const int pair = x * 16 + (r >> 1), bA = pair >> 5, kc = pair & 31, hg = r & 1;
  const int h = 4 * x + (r >> 3), j = r & 7;
  const int hpv = 2 * x + (r >> 4), msv = r & 15;
  const int msq = 2 * x + (r >> 4), hpq = r & 15;
  const bool fact = r < 28;
  const int bF = r & 3, fs = 7 * x + (r >> 2);
  const bool resid = hpv == msv;
  u64 ex0_ = 0, ex1_ = 0;
  TS(0);
  {
    u64 pc; unsigned len;
    asm volatile("s_getpc_b64 %0\n.Lpc_here:\n\ts_mov_b32 %1, .Lpc_end-.Lpc_here" : "=s"(pc), "=s"(len));
    if (wv == 7) {
      const unsigned o = (unsigned)(r + 32 * lane) * 128u;
      if (o < len) {
        unsigned keep;
        asm volatile("s_mov_b32 %0, m0\n\ts_mov_b32 m0, %3\n\ts_nop 0\n\tglobal_load_lds_dword %1, %2\n\ts_mov_b32 m0, %0"
                     : "=&s"(keep) : "v"(o), "s"(pc), "s"(lbase + L_P) : "memory");
      }
    }
  }
  const rsrc_t rW = RS(a.ws, WS_BYTES);
  if (tid < 16) ((LAS unsigned*)(L + L_CNT))[tid] = 0u;

  v4u wA[8];
  v4u wG[8], wD[4];
  unsigned xp = 0;
  u64 tag = 0, e0 = 0;
  if (lo) {
    const rsrc_t rX = RS(a.x, 4096), rQ = RS(a.wq, 4194304), rK = RS(a.wuk, 4194304);
    const v2u ev = __builtin_amdgcn_raw_buffer_load_b64(rW, OFF_E, 0, SC1);
    v4u xr[4];
#pragma unroll
    for (int i = 0; i < 4; ++i) xr[i] = __builtin_amdgcn_raw_buffer_load_b128(rX, (col >> 2) * 1024 + ((col & 3) * 4 + g) * 64 + i * 16, 0, 0);
    const unsigned xraw = __builtin_amdgcn_raw_buffer_load_b32(rX, (col >> 2) * 1024 + mcol(msq, 8 * g + 2 * (col & 3)) * 2, 0, 0);
    v2u t[8];
#pragma unroll
    for (int e = 0; e < 8; ++e) t[e] = __builtin_amdgcn_raw_buffer_load_b64(rQ, mcol(msq, 8 * g + e) * 8192 + hpq * 512 + q4 * 128 + col * 8, 0, 0);
    { const v4u f = {SENT, SENT, SENT, SENT}; __builtin_amdgcn_raw_buffer_store_b128(f, rW, OFF_SM + w * 4096 + tid * 16, 0, SC1); }
#pragma unroll
    for (int i = 0; i < 4; ++i) wA[4 + i] = __builtin_amdgcn_raw_buffer_load_b128(rK, (64 * j + 4 * col + i) * 8192 + h * 256 + q4 * 64 + g * 16, 0, 0);
    SB();
    BAR();
    SB();
    e0 = ((u64)ev.y << 32) | ev.x;
    u64 z = e0 + 0x9E3779B97F4A7C15ull;
    z = (z ^ (z >> 30)) * 0xBF58476D1CE4E5B9ull; z = (z ^ (z >> 27)) * 0x94D049BB133111EBull; tag = z ^ (z >> 31);
    float s = 0.f;
#pragma unroll
    for (int i = 0; i < 4; ++i) { s = dot2(xr[i].x, ONES, s); s = dot2(xr[i].y, ONES, s); s = dot2(xr[i].z, ONES, s); s = dot2(xr[i].w, ONES, s); }
    s += dppf<XOR1>(s); s += dppf<XOR2>(s); s = add16(s, s); s = add32(s, s);
    const float mu = s * (1.f / 512.f);
    xp = pk(blo(xraw) - mu, bhi(xraw) - mu);
    if (wv == 0) *(LAS f32x2*)(L + L_XR + (col >> 2) * 128 + (8 * g + 2 * (col & 3)) * 4) = f32x2{blo(xraw), bhi(xraw)};
    tr4(t, wA[0], wA[1], wA[2], wA[3]);
  } else {
    BAR();
    const unsigned char* src = a.ckv + (size_t)pair * 131072 + lane * 16;
#pragma unroll
    for (int i = 0; i < 32; ++i) {
      const int row = 4 * i + q4;
      unsigned keep;
      asm volatile("s_mov_b32 %0, m0\n\ts_mov_b32 m0, %2\n\ts_nop 0\n\tglobal_load_lds_dwordx4 %1, off\n\ts_mov_b32 m0, %0"
                   : "=&s"(keep) : "v"(src + row * 1024), "s"(lbase + L_C + row * ROW) : "memory");
    }
  }
  TS(1);
  float cs4[4] = {0.f, 0.f, 0.f, 0.f};
  float csg = 0.f;
  v4u ID0, ID1;
#pragma unroll
  for (int d = 0; d < 4; ++d) { const bool on = (g == (col >> 2)) && ((col & 3) == d); ID0[d] = on ? 0x00003F80u : 0u; ID1[d] = on ? 0x3F800000u : 0u; }
  const f32x4 Z4 = {0.f, 0.f, 0.f, 0.f};

  auto layer = [&](auto LY) __attribute__((always_inline)) {
    constexpr int ly = LY.value, LYO = 7 * ly;
    const unsigned SM = OFF_SM + ly * SM_STRIDE, OP = OFF_OP + ly * OP_STRIDE;
    const unsigned QS = ly == 0 ? OFF_QS0 : SM + SM_QS;
    constexpr int T0 = 2 + ly * 14;
    if (lo) {
      const v4u xa = {dppu<0x00>(xp), dppu<0x55>(xp), dppu<0xAA>(xp), dppu<0xFF>(xp)};
      f32x4 c0 = mfma(xa, wA[0], Z4), c1 = mfma(xa, wA[1], Z4), c2 = mfma(xa, wA[2], Z4), c3 = mfma(xa, wA[3], Z4);
      const unsigned qoff = (((2 * hpq + (q4 >> 1)) * 4 + 2 * (q4 & 1) + (col >> 3)) * 16 + msq);
      __builtin_amdgcn_raw_buffer_store_b64(v2u{pk(c0.x, c1.x), pk(c2.x, c3.x)}, rW, SM + SM_QP + qoff * 256 + g * 64 + (col & 7) * 8, 0, SC1);
      if (wv == 3) {
        const float x0 = blo(xp), x1 = bhi(xp);
        float s1 = x0 + x1, s2 = x0 * x0 + x1 * x1;
        s1 += dppf<XOR1>(s1); s2 += dppf<XOR1>(s2); s1 += dppf<XOR2>(s1); s2 += dppf<XOR2>(s2);
        float sv = add16(s1, s2); sv = add32(sv, sv);
        __builtin_amdgcn_raw_buffer_store_b32(u_(sv), rW, (g < 2 && (col & 3) == 0) ? QS + (hpq * 16 + msq) * 32 + (col >> 2) * 8 + g * 4 : OOB, 0, SC1);
      }
      if (ly == 0) {
        const v4u on = {ONES, ONES, ONES, ONES};
        c0 = mfma(on, wA[0], Z4); c1 = mfma(on, wA[1], Z4); c2 = mfma(on, wA[2], Z4); c3 = mfma(on, wA[3], Z4);
        __builtin_amdgcn_raw_buffer_store_b64(v2u{pk(c0.x, c1.x), pk(c2.x, c3.x)}, rW, g == 0 ? OFF_CSP + qoff * 64 + (col & 7) * 8 : OOB, 0, SC1);
        VM0();
        unsigned old = 0;
        if (lane == 0) old = __hip_atomic_fetch_add((LAS unsigned*)(L + L_CNT), 1u, __ATOMIC_RELAXED, __HIP_MEMORY_SCOPE_WORKGROUP);
        if (__builtin_amdgcn_readfirstlane(old) == 3u)
          __builtin_amdgcn_raw_buffer_store_b64(v2u{(unsigned)tag, (unsigned)(tag >> 32)}, rW, lane == 0 ? OFF_FL + w * 8 : OOB, 0, SC1);
        TS(T0 + 0);
        NAP(D_HQ);
        for (int sp = 0;; ++sp) {
          MEMF();
          const v4u f0 = __builtin_amdgcn_raw_buffer_load_b128(rW, OFF_FL + lane * 16, 0, SC1), f1 = __builtin_amdgcn_raw_buffer_load_b128(rW, OFF_FL + 1024 + lane * 16, 0, SC1);
          const unsigned tl = (unsigned)tag, th = (unsigned)(tag >> 32);
          const unsigned bad = (f0.x ^ tl) | (f0.y ^ th) | (f0.z ^ tl) | (f0.w ^ th) | (f1.x ^ tl) | (f1.y ^ th) | (f1.z ^ tl) | (f1.w ^ th);
          if (__builtin_amdgcn_ballot_w64(bad != 0u) == 0ull || sp > SPIN_MAX) { RETRY(D_HQ); break; }
          NAP(D_RT);
        }
        MEMF();
        if (w == 0 && wv == 0) {
          const u64 e1 = e0 + 1ull;
          __builtin_amdgcn_raw_buffer_store_b64(v2u{(unsigned)e1, (unsigned)(e1 >> 32)}, rW, lane == 0 ? OFF_E : OOB, 0, SC1);
        }
      } else {
        TS(T0 + 0);
        NAP(LYO + D_HQ);
      }
      TS(T0 + 1);
      v4u qa[4]; v2u qs = {0u, 0u};
      {
        const unsigned base = SM + SM_QP + (h * 4 + q4) * 4096 + (col & 3) * 256 + (col >> 2) * 64 + g * 16;
        const unsigned soff = wv == 0 ? QS + ((h >> 1) * 16 + col) * 32 + g * 8 : OOB;
        for (int sp = 0;; ++sp) {
          MEMF();
#pragma unroll
          for (int i = 0; i < 4; ++i) qa[i] = __builtin_amdgcn_raw_buffer_load_b128(rW, base + i * 1024, 0, SC1);
          qs = __builtin_amdgcn_raw_buffer_load_b64(rW, soff, 0, SC1);
          if (ly == 0) break;
          const unsigned m = max(max(max(umax4(qa[0]), umax4(qa[1])), max(umax4(qa[2]), umax4(qa[3]))), max(qs.x, qs.y));
          if (__builtin_amdgcn_ballot_w64(m == SENT) == 0ull || sp > SPIN_MAX) { RETRY(LYO + D_HQ); break; }
          NAP(D_RT);
        }
      }
      f32x4 ac[4] = {Z4, Z4, Z4, Z4};
#pragma unroll
      for (int i = 0; i < 4; ++i)
#pragma unroll
        for (int t = 0; t < 4; ++t) ac[t] = mfma(qa[i], wA[4 + t], ac[t]);
      f32x4 pv = {sum4(ac[0]), sum4(ac[1]), sum4(ac[2]), sum4(ac[3])};
      if (wv != 0) {
        *(LAS f32x4*)(L + L_R + q4 * 1024 + lane * 16) = pv;
        MEMF();
        if (lane == 0) __hip_atomic_fetch_add((LAS unsigned*)(L + L_CNT + 4), 1u, __ATOMIC_RELAXED, __HIP_MEMORY_SCOPE_WORKGROUP);
        TS(T0 + 2);
        nap(3); NAP(LYO + D_H1);
      } else {
        float s1 = f_(qs.x), s2 = f_(qs.y);
        s1 += dppf<XOR1>(s1); s2 += dppf<XOR1>(s2); s1 += dppf<XOR2>(s1); s2 += dppf<XOR2>(s2);
        s1 += dppf<HMIR>(s1); s2 += dppf<HMIR>(s2); s1 += dppf<MIR>(s1); s2 += dppf<MIR>(s2);
        const float mu = s1 * (1.f / 512.f), var = fmaxf(s2 * (1.f / 512.f) - mu * mu, 0.f);
        const float sc = __builtin_amdgcn_rsqf(var + 1e-5f) * (0.08838834764831845f * 1.4426950408889634f);
        f32x4 p1, p2, p3;
        for (int sp = 0;; ++sp) {
          MEMF();
          const unsigned c = *(volatile LAS unsigned*)(L + L_CNT + 4);
          MEMF();
          p1 = *(LAS f32x4*)(L + L_R + 1024 + lane * 16); p2 = *(LAS f32x4*)(L + L_R + 2048 + lane * 16); p3 = *(LAS f32x4*)(L + L_R + 3072 + lane * 16);
          if (__builtin_amdgcn_readfirstlane(c) >= 3u * (ly + 1) || sp > SPIN_MAX) break;
        }
        pv = (pv + p1) + (p2 + p3);
        const float o0 = (pv.x - mu * cs4[0]) * sc, o1 = (pv.y - mu * cs4[1]) * sc, o2 = (pv.z - mu * cs4[2]) * sc, o3 = (pv.w - mu * cs4[3]) * sc;
        __builtin_amdgcn_raw_buffer_store_b64(v2u{pk(o0, o1), pk(o2, o3)}, rW, SM + SM_QA + (g * 32 + h) * 1024 + j * 128 + col * 8, 0, SC1);
        TS(T0 + 2);
        NAP(LYO + D_H1);
      }
      unsigned off[4]; v4u qv[4];
#pragma unroll
      for (int i = 0; i < 4; ++i) off[i] = SM + SM_QA + (bA * 32 + hg * 16 + 4 * q4 + i) * 1024 + lane * 16;
      HOP_LOAD(4, off, qv, LYO + D_H1)
#pragma unroll
      for (int i = 0; i < 4; ++i) *(LAS v4u*)(L + L_Q + (4 * q4 + i) * ROW + lane * 16) = qv[i];
    } else if (ly == 0) {
      VM0();
    }
    TS(T0 + 3);
    v4u cf[16];
    const int rho = 2 * (col & 7) + (col >> 3);
    if (ly == 1) {
#pragma unroll
      for (int s = 0; s < 16; ++s) cf[s] = *(LAS v4u*)(L + L_C + (16 * wv + rho) * ROW + g * 256 + s * 16);
    }
    BAR();
    unsigned tv[16], to[16];
    if (ly == 0) {
#pragma unroll
      for (int s = 0; s < 16; ++s) cf[s] = *(LAS v4u*)(L + L_C + (16 * wv + rho) * ROW + g * 256 + s * 16);
      if (!lo) {
        const rsrc_t rV = RS(a.wuv, 4194304), rO = RS(a.wo, 4194304);
#pragma unroll
        for (int e = 0; e < 16; ++e)
          tv[e] = __builtin_amdgcn_raw_buffer_load_b32(rV, (64 * j + 32 * (e >> 3) + 16 * ((e >> 2) & 1) + 4 * g + (e & 3)) * 8192 + h * 256 + q4 * 64 + col * 4, 0, 0);
#pragma unroll
        for (int e = 0; e < 16; ++e)
          to[e] = __builtin_amdgcn_raw_buffer_load_b32(rO, ((2 * hpv + (e >> 3)) * 128 + 32 * q4 + 8 * g + (e & 7)) * 1024 + mcol(msv, 2 * col) * 2, 0, 0);
        SB();
      }
    }
    f32x4 sc4 = Z4;
#pragma unroll
    for (int s = 0; s < 16; ++s) sc4 = mfma(cf[s], *(LAS v4u*)(L + L_Q + col * ROW + g * 256 + s * 16), sc4);
    float mx = fmaxf(fmaxf(sc4.x, sc4.y), fmaxf(sc4.z, sc4.w));
    mx = max16(mx); mx = max32(mx);
    if (g == 0) *(LAS float*)(L + L_MX + col * 32 + wv * 4) = mx;
    {
      const int qq = 4 * g + (col >> 2), rr = 2 * (qq & 7) + (qq >> 3);
      LAS unsigned char* tb = L + L_C + rr * ROW + wv * 128 + (col & 3) * 8;
#pragma unroll
      for (int s = 0; s < 4; ++s)
#pragma unroll
        for (int t = 0; t < 4; ++t) {
          const v2u u0 = __builtin_bit_cast(v2u, __builtin_amdgcn_ds_read_tr16_b64_v4i16((LAS s16x4*)(tb + (32 * s) * ROW + t * 32)));
          const v2u u1 = __builtin_bit_cast(v2u, __builtin_amdgcn_ds_read_tr16_b64_v4i16((LAS s16x4*)(tb + (32 * s + 16) * ROW + t * 32)));
          cf[4 * s + t] = v4u{u0.x, u0.y, u1.x, u1.y};
        }
    }
    BAR();
    {
      const f32x4 m0 = *(LAS f32x4*)(L + L_MX + col * 32), m1 = *(LAS f32x4*)(L + L_MX + col * 32 + 16);
      mx = fmaxf(fmaxf(fmaxf(m0.x, m0.y), fmaxf(m0.z, m0.w)), fmaxf(fmaxf(m1.x, m1.y), fmaxf(m1.z, m1.w)));
      const float p0 = __builtin_amdgcn_exp2f(sc4.x - mx), p1 = __builtin_amdgcn_exp2f(sc4.y - mx), p2 = __builtin_amdgcn_exp2f(sc4.z - mx), p3 = __builtin_amdgcn_exp2f(sc4.w - mx);
      *(LAS v2u*)(L + L_P + (wv >> 1) * 1024 + lane * 16 + (wv & 1) * 8) = v2u{pk(p0, p1), pk(p2, p3)};
    }
    BAR();
    TS(T0 + 4);
    {
      v4u pf[4];
#pragma unroll
      for (int s = 0; s < 4; ++s) pf[s] = *(LAS v4u*)(L + L_P + s * 1024 + lane * 16);
      f32x4 o[4] = {Z4, Z4, Z4, Z4};
#pragma unroll
      for (int t = 0; t < 4; ++t)
#pragma unroll
        for (int s = 0; s < 4; ++s) o[t] = mfma(cf[4 * s + t], pf[s], o[t]);
      const int hh = hg * 16 + col;
      const unsigned ob = OP + ((((hh >> 1) * 8 + wv) * 2) * 4 + bA) * 4096 + kc * 128 + (hh & 1) * 64 + g * 16;
#pragma unroll
      for (int u = 0; u < 2; ++u)
        __builtin_amdgcn_raw_buffer_store_b128(v4u{pk(o[2 * u].x, o[2 * u].y), pk(o[2 * u].z, o[2 * u].w), pk(o[2 * u + 1].x, o[2 * u + 1].y), pk(o[2 * u + 1].z, o[2 * u + 1].w)},
                                               rW, ob + u * 16384, 0, SC1);
      float l = 0.f;
#pragma unroll
      for (int s = 0; s < 4; ++s) { l = dot2(pf[s].x, ONES, l); l = dot2(pf[s].y, ONES, l); l = dot2(pf[s].z, ONES, l); l = dot2(pf[s].w, ONES, l); }
      l = add16(l, l); l = add32(l, l);
      TS(T0 + 5);
      VM0();
      __builtin_amdgcn_raw_buffer_store_b64(v2u{u_(mx), u_(l)}, rW, g == 0 ? SM + SM_MLF + ((hh * 8 + wv) * 4 + bA) * 256 + kc * 8 : OOB, 0, SC1);
    }
    TS(T0 + 6);
    if (ly == 0 && !lo) {
      unsigned t8[8];
#pragma unroll
      for (int u = 0; u < 2; ++u) {
#pragma unroll
        for (int e = 0; e < 8; ++e) t8[e] = tv[8 * u + e];
        tr2(t8, wA[2 * u], wA[2 * u + 1]);
#pragma unroll
        for (int e = 0; e < 8; ++e) t8[e] = to[8 * u + e];
        tr2(t8, wA[4 + 2 * u], wA[4 + 2 * u + 1]);
      }
    }
    NAP(LYO + D_ML);
    v2u gt[16], dt[8];
    {
      const int b = wv >> 1, ks = wv & 1;
      v2u ml;
      const unsigned moff = SM + SM_MLF + ((h * 8 + j) * 4 + b) * 256 + (16 * ((col >> 2) & 1) + 4 * g + (col & 3)) * 8;
      for (int sp = 0;; ++sp) {
        MEMF();
        ml = __builtin_amdgcn_raw_buffer_load_b64(rW, moff, 0, SC1);
        if (__builtin_amdgcn_ballot_w64(max(ml.x, ml.y) == SENT) == 0ull || sp > SPIN_MAX) { RETRY(LYO + D_ML); break; }
        NAP(D_RT);
      }
      MEMF();
      TS(T0 + 7);
      const unsigned ob = OP + ((((h >> 1) * 8 + j) * 2 + ks) * 4 + b) * 4096 + col * 128 + (h & 1) * 64 + g * 16;
      const v4u o0 = __builtin_amdgcn_raw_buffer_load_b128(rW, ob, 0, SC1), o1 = __builtin_amdgcn_raw_buffer_load_b128(rW, ob + 2048, 0, SC1);
      v4u csv = {0u, 0u, 0u, 0u};
      if (ly == 0) {
        if (lo) csv = __builtin_amdgcn_raw_buffer_load_b128(rW, OFF_CSP + (h * 4 + q4) * 1024 + col * 64 + g * 16, 0, SC1);
        const rsrc_t rG = RS(a.wg, 1835008), rU = RS(a.wu, 1835008), rD = RS(a.wd, 1835008);
        (void)rG; (void)rU;
        const GAS unsigned char* gb = (const GAS unsigned char*)(col < 8 ? a.wg : a.wu) + (fact ? fs : 0) * 64 + (col & 7) * 8;
#pragma unroll
        for (int e = 0; e < 16; ++e) gt[e] = *(const GAS v2u*)(gb + (size_t)mcol(2 * wv + (e >> 3), 8 * g + (e & 7)) * 3584);
#pragma unroll
        for (int e = 0; e < 8; ++e) dt[e] = __builtin_amdgcn_raw_buffer_load_b64(rD, fact ? (32 * fs + 8 * g + e) * 1024 + wv * 128 + col * 8 : OOB, 0, 0);
        const v4u f = {SENT, SENT, SENT, SENT};
        __builtin_amdgcn_raw_buffer_store_b128(f, rW, tid < 384 ? OFF_SM + SM_STRIDE + w * 6144 + tid * 16 : OOB, 0, SC1);
        SB();
      }
      float m = f_(ml.x), M = m;
      M = fmaxf(M, dppf<XOR1>(M)); M = fmaxf(M, dppf<XOR2>(M)); M = fmaxf(M, dppf<HMIR>(M)); M = max16(M); M = max32(M);
      const float wt = __builtin_amdgcn_exp2f(m - M);
      float Ls = wt * f_(ml.y);
      Ls += dppf<XOR1>(Ls); Ls += dppf<XOR2>(Ls); Ls += dppf<HMIR>(Ls); Ls = add16(Ls, Ls); Ls = add32(Ls, Ls);
      const float wn = wt * __builtin_amdgcn_rcpf(Ls);
      const float w0 = dppf<BCAST + 0>(wn), w1 = dppf<BCAST + 1>(wn), w2 = dppf<BCAST + 2>(wn), w3 = dppf<BCAST + 3>(wn);
      const float w4 = dppf<BCAST + 4>(wn), w5 = dppf<BCAST + 5>(wn), w6 = dppf<BCAST + 6>(wn), w7 = dppf<BCAST + 7>(wn);
      const f32x4 d00 = mfma(o0, ID0, Z4), d01 = mfma(o0, ID1, Z4), d10 = mfma(o1, ID0, Z4), d11 = mfma(o1, ID1, Z4);
      const float z0 = ((w0 * d00.x + w1 * d00.y) + (w2 * d00.z + w3 * d00.w)) + ((w4 * d10.x + w5 * d10.y) + (w6 * d10.z + w7 * d10.w));
      const float z1 = ((w0 * d01.x + w1 * d01.y) + (w2 * d01.z + w3 * d01.w)) + ((w4 * d11.x + w5 * d11.y) + (w6 * d11.z + w7 * d11.w));
      float z = add32(z0, z1); z = add16(z, z);
      *(LAS unsigned short*)(L + L_CX + b * 128 + ks * 64 + (2 * col + (g >> 1)) * 2) = (unsigned short)pk(z, z);
      if (ly == 0 && lo) {
        f32x4 e4;
#pragma unroll
        for (int t = 0; t < 4; ++t) { float v = sum4(mfma(csv, wA[4 + t], Z4)); v = add16(v, v); e4[t] = add32(v, v); }
        *(LAS f32x4*)(L + L_Q + q4 * 1024 + lane * 16) = e4;
      }
    }
    BAR();
    TS(T0 + 8);
    if (!lo) {
      const v4u a0 = *(LAS v4u*)(L + L_CX + (col >> 2) * 128 + g * 16), a1 = *(LAS v4u*)(L + L_CX + (col >> 2) * 128 + 64 + g * 16);
      f32x4 c0 = mfma(a0, wA[0], Z4), c1 = mfma(a0, wA[1], Z4);
      c0 = mfma(a1, wA[2], c0); c1 = mfma(a1, wA[3], c1);
      __builtin_amdgcn_raw_buffer_store_b32(pk(c0.x, c1.x), rW, SM + SM_CP + ((h * 4 + q4) * 8 + j) * 256 + g * 64 + col * 4, 0, SC1);
      f32x2 xr = {0.f, 0.f};
      if (resid) xr = *(LAS f32x2*)(L + L_XR + g * 128 + col * 8);
      NAP(LYO + D_HV);
      TS(T0 + 9);
      unsigned off[4]; v4u cv[4];
#pragma unroll
      for (int i = 0; i < 4; ++i) off[i] = SM + SM_CP + ((2 * hpv + (i >> 1)) * 4 + q4) * 2048 + (i & 1) * 1024 + (col & 3) * 256 + (col >> 2) * 64 + g * 16;
      HOP_LOAD(4, off, cv, LYO + D_HV)
      c0 = Z4; c1 = Z4;
#pragma unroll
      for (int i = 0; i < 4; ++i) { c0 = mfma(cv[i], wA[4 + 2 * (i >> 1)], c0); c1 = mfma(cv[i], wA[5 + 2 * (i >> 1)], c1); }
      f32x2 v = {sum4(c0), sum4(c1)};
      if (q4 == 0) v += xr;
      *(LAS f32x2*)(L + L_R + q4 * 512 + lane * 8) = v;
    } else if (ly == 0 && wv == 0) {
      const f32x4 e = (*(LAS f32x4*)(L + L_Q + lane * 16) + *(LAS f32x4*)(L + L_Q + 1024 + lane * 16)) + (*(LAS f32x4*)(L + L_Q + 2048 + lane * 16) + *(LAS f32x4*)(L + L_Q + 3072 + lane * 16));
      cs4[0] = e.x; cs4[1] = e.y; cs4[2] = e.z; cs4[3] = e.w;
    }
    if (ly == 0) {
      VM0();
      v2u t8[8];
#pragma unroll
      for (int u = 0; u < 2; ++u) {
#pragma unroll
        for (int e = 0; e < 8; ++e) t8[e] = gt[8 * u + e];
        tr4(t8, wG[4 * u], wG[4 * u + 1], wG[4 * u + 2], wG[4 * u + 3]);
      }
      tr4(dt, wD[0], wD[1], wD[2], wD[3]);
      const v4u on = {ONES, ONES, ONES, ONES};
      float e4[4];
#pragma unroll
      for (int t = 0; t < 4; ++t) e4[t] = mfma(on, wG[4 + t], mfma(on, wG[t], Z4)).x;
      const float ev = g == 0 ? e4[0] : g == 1 ? e4[1] : g == 2 ? e4[2] : e4[3];
      *(LAS float*)(L + L_GU + (wv >> 2) * 1024 + (4 * col + g) * 16 + q4 * 4) = ev;
    }
    BAR();
    if (wv == 0) {
      const f32x2 v = (*(LAS f32x2*)(L + L_R + lane * 8) + *(LAS f32x2*)(L + L_R + 512 + lane * 8)) + (*(LAS f32x2*)(L + L_R + 1024 + lane * 8) + *(LAS f32x2*)(L + L_R + 1536 + lane * 8));
      __builtin_amdgcn_raw_buffer_store_b32(pk(v.x, v.y), rW, SM + SM_XP + ((g * 16 + msv) * 16 + hpv) * 64 + col * 4, 0, SC1);
    }
    const int gidx = (col >> 3) * 32 + 8 * g + (col & 7);
    if (ly == 0) {
      csg = sum4(*(LAS f32x4*)(L + L_GU + gidx * 16)) + sum4(*(LAS f32x4*)(L + L_GU + 1024 + gidx * 16));
      BAR();
    }
    TS(T0 + 10);
    if (wv != 0) nap(2);
    NAP(LYO + D_H3);
    {
      unsigned off[2]; v4u xv[2];
#pragma unroll
      for (int i = 0; i < 2; ++i) off[i] = SM + SM_XP + (bF * 16 + 2 * wv + i) * 1024 + col * 64 + g * 16;
      HOP_LOAD(2, off, xv, LYO + D_H3)
      TS(T0 + 11);
      const f32x4 i0 = mfma(xv[0], ID0, Z4), i1 = mfma(xv[0], ID1, Z4), i2 = mfma(xv[1], ID0, Z4), i3 = mfma(xv[1], ID1, Z4);
      f32x4 ac[4];
#pragma unroll
      for (int t = 0; t < 4; ++t) ac[t] = mfma(xv[1], wG[4 + t], mfma(xv[0], wG[t], Z4));
      float x0 = add32(sum4(i0), sum4(i2)), x1 = add32(sum4(i1), sum4(i3));
      x0 = add16(x0, x0); x1 = add16(x1, x1);
      float sv = (g & 1) ? x0 * x0 + x1 * x1 : x0 + x1;
      sv += dppf<XOR1>(sv); sv += dppf<XOR2>(sv); sv += dppf<HMIR>(sv); sv += dppf<MIR>(sv); sv = add32(sv, sv);
      if (col == 0 && g < 2) *(LAS float*)(L + L_ST + g * 32 + wv * 4) = sv;
      const float ra = add32(sum4(ac[0]), sum4(ac[2])), rb = add32(sum4(ac[1]), sum4(ac[3]));
      *(LAS float*)(L + L_GU + (wv >> 2) * 1024 + (4 * col + g) * 16 + q4 * 4) = add16(ra, rb);
      BAR();
      const float su = sum4(*(LAS f32x4*)(L + L_GU + gidx * 16)) + sum4(*(LAS f32x4*)(L + L_GU + 1024 + gidx * 16));
      const float sa = sum4(*(LAS f32x4*)(L + L_ST + (col >> 3) * 32)) + sum4(*(LAS f32x4*)(L + L_ST + (col >> 3) * 32 + 16));
      const float s1 = dppf<BCAST + 0>(sa), s2 = dppf<BCAST + 8>(sa);
      const float mu = s1 * (1.f / 512.f), var = fmaxf(s2 * (1.f / 512.f) - mu * mu, 0.f);
      const float val = (su - mu * csg) * __builtin_amdgcn_rsqf(var + 1e-5f);
      const float oth = dppf<ROR8>(val);
      const float gg = col < 8 ? val : oth;
      const float hv = val * oth * __builtin_amdgcn_rcpf(1.f + __builtin_amdgcn_exp2f(-1.4426950408889634f * gg));
      const unsigned hp2 = pk(hv, dppf<SHL + 1>(hv));
      const v4u ha = {hp2, dppu<SHL + 2>(hp2), dppu<SHL + 4>(hp2), dppu<SHL + 6>(hp2)};
      const float r0 = fs == 0 ? x0 : 0.f, r1 = fs == 0 ? x1 : 0.f;
      const f32x4 y0 = mfma(ha, wD[0], f32x4{r0, 0.f, 0.f, 0.f}), y1 = mfma(ha, wD[1], f32x4{r1, 0.f, 0.f, 0.f});
      const f32x4 y2 = mfma(ha, wD[2], f32x4{r0, 0.f, 0.f, 0.f}), y3 = mfma(ha, wD[3], f32x4{r1, 0.f, 0.f, 0.f});
      const unsigned yo = g < 2 ? pk(y0.x, y1.x) : pk(y2.x, y3.x);
      __builtin_amdgcn_raw_buffer_store_b32(yo, rW, (fact && !(g & 1)) ? SM + SM_YP + (((2 * wv + (g >> 1)) * 4 + bF) * 56 + fs) * 64 + col * 4 : OOB, 0, SC1);
    }
    TS(T0 + 12);
    if (ly == 1 && !resid) return;
    NAP(LYO + D_H4);
    {
      const int b = wv >> 1, hf = wv & 1;
      unsigned off[2]; v4u yv[2];
      off[0] = SM + SM_YP + ((msq * 4 + b) * 56 + 28 * hf + col) * 64 + g * 16;
      off[1] = col < 12 ? off[0] + 1024 : OOB;
      HOP_LOAD(2, off, yv, LYO + D_H4)
      TS(T0 + 13);
      const f32x4 d0 = mfma(yv[1], ID0, mfma(yv[0], ID0, Z4)), d1 = mfma(yv[1], ID1, mfma(yv[0], ID1, Z4));
      float z = add32(sum4(d0), sum4(d1)); z = add16(z, z);
      *(LAS float*)(L + L_X1 + (b * 2 + hf) * 128 + (2 * col + (g >> 1)) * 4) = z;
    }
    BAR();
    if (lo) {
      const int o = (col >> 2) * 256 + (8 * g + 2 * (col & 3)) * 4;
      const f32x2 v = *(LAS f32x2*)(L + L_X1 + o) + *(LAS f32x2*)(L + L_X1 + o + 128);
      xp = pk(v.x, v.y);
      if (ly == 0) {
        if (wv == 0) *(LAS f32x2*)(L + L_XR + (col >> 2) * 128 + (8 * g + 2 * (col & 3)) * 4) = f32x2{blo(xp), bhi(xp)};
      } else if (wv == 0) {
        *(GAS unsigned*)(a.out + (col >> 2) * 1024 + mcol(msq, 8 * g + 2 * (col & 3)) * 2) = xp;
      }
    }
  };
  layer(IC<0>{});
  layer(IC<1>{});
  TS(30);
  if (w == 0 && wv == EXW) { const u64 e_ = __builtin_amdgcn_s_memtime() + 20ull * (ex1_ - ex0_); for (int i = 0; i < 10000000 && __builtin_amdgcn_s_memtime() < e_; ++i) {} }
  asm volatile(".Lpc_end:");
}

extern "C" void kernel_launch(void* const* d_in, const int* in_sizes, int n_in, void* d_out, int out_size, void* d_ws, size_t ws_size,
                              hipStream_t stream) {
  Args a;
  a.x = (const unsigned char*)d_in[0]; a.ckv = (const unsigned char*)d_in[1]; a.wq = (const unsigned char*)d_in[2];
  a.wuk = (const unsigned char*)d_in[3]; a.wuv = (const unsigned char*)d_in[4]; a.wo = (const unsigned char*)d_in[5];
  a.wg = (const unsigned char*)d_in[7]; a.wu = (const unsigned char*)d_in[8]; a.wd = (const unsigned char*)d_in[9];
  a.out = (unsigned char*)d_out; a.ws = (unsigned char*)d_ws;
  decode_block<<<256, 512, 0, stream>>>(a);
}
```

```cpp
#include <hip/hip_runtime.h>
#include <stdint.h>

#define GAS __attribute__((address_space(1)))
#define LAS __attribute__((address_space(3)))
typedef unsigned long long u64;
typedef unsigned v4u __attribute__((ext_vector_type(4)));
typedef unsigned v2u __attribute__((ext_vector_type(2)));
typedef float f32x4 __attribute__((ext_vector_type(4)));
typedef float f32x2 __attribute__((ext_vector_type(2)));
typedef short bf16x8 __attribute__((ext_vector_type(8)));
typedef short s16x4 __attribute__((ext_vector_type(4)));
typedef __bf16 bf2 __attribute__((ext_vector_type(2)));
typedef __amdgpu_buffer_rsrc_t rsrc_t;

#define STAMP 0

constexpr unsigned OFF_E = 0, OFF_FL = 4096, OFF_QS0 = 16384, OFF_CSP = 65536;
constexpr unsigned OFF_SM = 262144;
constexpr unsigned SM_QA = 0, SM_MLF = 131072, SM_CP = 393216, SM_XP = 655360, SM_YP = 720896, SM_QS = 983040, SM_QP = 1048576, SM_STRIDE = 1572864;
constexpr unsigned OFF_OP = 4194304, OP_STRIDE = 4194304;
constexpr unsigned OFF_STAMP = 16777216;
constexpr unsigned WS_BYTES = 20971520;
constexpr unsigned OOB = 0x7FFFFFF0u;
constexpr unsigned SENT = 0xFFFFFFFFu;
constexpr int SPIN_MAX = 1 << 16;

constexpr int ROW = 1040;
constexpr int L_C = 0, L_Q = 128 * ROW, L_P = L_Q + 16 * ROW, L_MX = L_P + 4096, L_CX = L_MX + 512, L_X1 = L_CX + 512, L_GU = L_X1 + 1024,
              L_ST = L_GU + 2048, L_XR = L_ST + 64, L_R = L_XR + 512, L_CNT = L_R + 4096, LDS_BYTES = L_CNT + 64;
static_assert(LDS_BYTES <= 163840, "lds");

#define TUNE 0
constexpr int DLY[16] = {100,60,60,100,100,60,0,40,40,40,40,40,40,0,10,0};
enum { D_HQ = 0, D_H1 = 1, D_ML = 2, D_HV = 3, D_H3 = 4, D_H4 = 5, D_RT = 14 };

template <int V> struct IC { static constexpr int value = V; };
struct Args {
  const unsigned char *x, *ckv, *wq, *wuk, *wuv, *wo, *wg, *wu, *wd;
  unsigned char *out, *ws;
};

#define RS(p, n) __builtin_amdgcn_make_buffer_rsrc((void*)(p), 0, (n), 0x00020000)
#define SB() __builtin_amdgcn_sched_barrier(0)
#define MEMF() asm volatile("" ::: "memory")
#define VM0() asm volatile("s_waitcnt vmcnt(0)" ::: "memory")
#define BAR() do { asm volatile("s_waitcnt lgkmcnt(0)" ::: "memory"); __builtin_amdgcn_s_barrier(); MEMF(); } while (0)
constexpr int SC1 = 16;

__device__ __forceinline__ f32x4 mfma(v4u a, v4u b, f32x4 c) {
  return __builtin_amdgcn_mfma_f32_16x16x32_bf16(__builtin_bit_cast(bf16x8, a), __builtin_bit_cast(bf16x8, b), c, 0, 0, 0);
}
__device__ __forceinline__ unsigned pk(float lo, float hi) { bf2 v = {(__bf16)lo, (__bf16)hi}; return __builtin_bit_cast(unsigned, v); }
__device__ __forceinline__ float blo(unsigned u) { return __builtin_bit_cast(float, u << 16); }
__device__ __forceinline__ float bhi(unsigned u) { return __builtin_bit_cast(float, u & 0xFFFF0000u); }
__device__ __forceinline__ float f_(unsigned u) { return __builtin_bit_cast(float, u); }
__device__ __forceinline__ unsigned u_(float f) { return __builtin_bit_cast(unsigned, f); }
template <int C> __device__ __forceinline__ unsigned dppu(unsigned v) { return __builtin_amdgcn_update_dpp(0u, v, C, 0xf, 0xf, true); }
template <int C> __device__ __forceinline__ float dppf(float v) { return f_(dppu<C>(u_(v))); }
constexpr int XOR1 = 0xB1, XOR2 = 0x4E, HMIR = 0x141, MIR = 0x140, ROR8 = 0x128, SHL = 0x100, BCAST = 0x150;
__device__ __forceinline__ float add32(float a, float b) { auto r = __builtin_amdgcn_permlane32_swap(u_(a), u_(b), false, false); return f_(r[0]) + f_(r[1]); }
__device__ __forceinline__ float add16(float a, float b) { auto r = __builtin_amdgcn_permlane16_swap(u_(a), u_(b), false, false); return f_(r[0]) + f_(r[1]); }
__device__ __forceinline__ float max32(float a) { auto r = __builtin_amdgcn_permlane32_swap(u_(a), u_(a), false, false); return fmaxf(f_(r[0]), f_(r[1])); }
__device__ __forceinline__ float max16(float a) { auto r = __builtin_amdgcn_permlane16_swap(u_(a), u_(a), false, false); return fmaxf(f_(r[0]), f_(r[1])); }
__device__ __forceinline__ float sum4(f32x4 v) { return (v.x + v.y) + (v.z + v.w); }
__device__ __forceinline__ float dot2(unsigned a, unsigned b, float c) { return __builtin_amdgcn_fdot2_f32_bf16(__builtin_bit_cast(bf2, a), __builtin_bit_cast(bf2, b), c, false); }
constexpr unsigned ONES = 0x3F803F80u;
__device__ __forceinline__ unsigned umax4(v4u v) { return max(max(v.x, v.y), max(v.z, v.w)); }
#define nap(n) do { if ((n) > 0) __builtin_amdgcn_s_sleep((n) > 0 ? (n) : 0); } while (0)
template <int N> __device__ __forceinline__ void napc() { if (N > 0) __builtin_amdgcn_s_sleep(N); }
#define NAP(i) do { if ((i) < 7) napc<DLY[(i) % 7]>(); else if ((i) < 14) napc<DLY[7 + (i) % 7]>(); else napc<DLY[14]>(); } while (0)
#define RETRY(i) do { } while (0)
__device__ __forceinline__ void tr2(const unsigned (&r)[8], v4u& f0, v4u& f1) {
#pragma unroll
  for (int d = 0; d < 4; ++d) {
    f0[d] = __builtin_amdgcn_perm(r[2 * d + 1], r[2 * d], 0x05040100u);
    f1[d] = __builtin_amdgcn_perm(r[2 * d + 1], r[2 * d], 0x07060302u);
  }
}
__device__ __forceinline__ void tr4(const v2u (&r)[8], v4u& f0, v4u& f1, v4u& f2, v4u& f3) {
#pragma unroll
  for (int d = 0; d < 4; ++d) {
    f0[d] = __builtin_amdgcn_perm(r[2 * d + 1].x, r[2 * d].x, 0x05040100u);
    f1[d] = __builtin_amdgcn_perm(r[2 * d + 1].x, r[2 * d].x, 0x07060302u);
    f2[d] = __builtin_amdgcn_perm(r[2 * d + 1].y, r[2 * d].y, 0x05040100u);
    f3[d] = __builtin_amdgcn_perm(r[2 * d + 1].y, r[2 * d].y, 0x07060302u);
  }
}
__device__ __forceinline__ int mcol(int ms, int p) { return 64 * (ms >> 1) + 4 * (p >> 1) + 2 * (ms & 1) + (p & 1); }

#define HOP_LOAD(N, off, v, id) \
  for (int sp = 0;; ++sp) { \
    MEMF(); \
    _Pragma("unroll") for (int i = 0; i < N; ++i) v[i] = __builtin_amdgcn_raw_buffer_load_b128(rW, off[i], 0, SC1); \
    unsigned m_ = 0; \
    _Pragma("unroll") for (int i = 0; i < N; ++i) m_ = max(m_, umax4(v[i])); \
    if (__builtin_amdgcn_ballot_w64(m_ == SENT) == 0ull || sp > SPIN_MAX) { RETRY(id); break; } \
    NAP(D_RT); \
  }

#define TS(i) do { } while (0)

__global__ void __launch_bounds__(512, 2) decode_block(Args a) {
  __shared__ __attribute__((aligned(16))) unsigned char smem[LDS_BYTES];
  LAS unsigned char* L = (LAS unsigned char*)smem;
  const unsigned lbase = (unsigned)(size_t)L;
  const int w = blockIdx.x, tid = threadIdx.x, lane = tid & 63, wv = __builtin_amdgcn_readfirstlane(tid >> 6);
  const int col = lane & 15, g = lane >> 4, q4 = wv & 3;
  const bool lo = wv < 4;
  const int x = w & 7, r = w >> 3;
  const int pair = x * 16 + (r >> 1), bA = pair >> 5, kc = pair & 31, hg = r & 1;
  const int h = 4 * x + (r >> 3), j = r & 7;
  const int hpv = 2 * x + (r >> 4), msv = r & 15;
  const int msq = 2 * x + (r >> 4), hpq = r & 15;
  const bool fact = r < 28;
  const int bF = r & 3, fs = 7 * x + (r >> 2);
  const bool resid = hpv == msv;
  TS(0);
  {
    u64 pc; unsigned len;
    asm volatile("s_getpc_b64 %0\n.Lpc_here:\n\ts_mov_b32 %1, .Lpc_end-.Lpc_here" : "=s"(pc), "=s"(len));
    if (wv == 7) {
      const unsigned o = (unsigned)(r + 32 * lane) * 128u;
      if (o < len) {
        unsigned keep;
        asm volatile("s_mov_b32 %0, m0\n\ts_mov_b32 m0, %3\n\ts_nop 0\n\tglobal_load_lds_dword %1, %2\n\ts_mov_b32 m0, %0"
                     : "=&s"(keep) : "v"(o), "s"(pc), "s"(lbase + L_P) : "memory");
      }
    }
  }
  const rsrc_t rW = RS(a.ws, WS_BYTES);
  if (tid < 16) ((LAS unsigned*)(L + L_CNT))[tid] = 0u;

  v4u wA[8];
  v4u wG[8], wD[4];
  unsigned xp = 0;
  u64 tag = 0, e0 = 0;
  if (lo) {
    const rsrc_t rX = RS(a.x, 4096), rQ = RS(a.wq, 4194304), rK = RS(a.wuk, 4194304);
    const v2u ev = __builtin_amdgcn_raw_buffer_load_b64(rW, OFF_E, 0, SC1);
    v4u xr[4];
#pragma unroll
    for (int i = 0; i < 4; ++i) xr[i] = __builtin_amdgcn_raw_buffer_load_b128(rX, (col >> 2) * 1024 + ((col & 3) * 4 + g) * 64 + i * 16, 0, 0);
    const unsigned xraw = __builtin_amdgcn_raw_buffer_load_b32(rX, (col >> 2) * 1024 + mcol(msq, 8 * g + 2 * (col & 3)) * 2, 0, 0);
    v2u t[8];
#pragma unroll
    for (int e = 0; e < 8; ++e) t[e] = __builtin_amdgcn_raw_buffer_load_b64(rQ, mcol(msq, 8 * g + e) * 8192 + hpq * 512 + q4 * 128 + col * 8, 0, 0);
    { const v4u f = {SENT, SENT, SENT, SENT}; __builtin_amdgcn_raw_buffer_store_b128(f, rW, OFF_SM + w * 4096 + tid * 16, 0, SC1); }
#pragma unroll
    for (int i = 0; i < 4; ++i) wA[4 + i] = __builtin_amdgcn_raw_buffer_load_b128(rK, (64 * j + 4 * col + i) * 8192 + h * 256 + q4 * 64 + g * 16, 0, 0);
    SB();
    BAR();
    SB();
    e0 = ((u64)ev.y << 32) | ev.x;
    u64 z = e0 + 0x9E3779B97F4A7C15ull;
    z = (z ^ (z >> 30)) * 0xBF58476D1CE4E5B9ull; z = (z ^ (z >> 27)) * 0x94D049BB133111EBull; tag = z ^ (z >> 31);
    float s = 0.f;
#pragma unroll
    for (int i = 0; i < 4; ++i) { s = dot2(xr[i].x, ONES, s); s = dot2(xr[i].y, ONES, s); s = dot2(xr[i].z, ONES, s); s = dot2(xr[i].w, ONES, s); }
    s += dppf<XOR1>(s); s += dppf<XOR2>(s); s = add16(s, s); s = add32(s, s);
    const float mu = s * (1.f / 512.f);
    xp = pk(blo(xraw) - mu, bhi(xraw) - mu);
    if (wv == 0) *(LAS f32x2*)(L + L_XR + (col >> 2) * 128 + (8 * g + 2 * (col & 3)) * 4) = f32x2{blo(xraw), bhi(xraw)};
    tr4(t, wA[0], wA[1], wA[2], wA[3]);
  } else {
    BAR();
    const unsigned char* src = a.ckv + (size_t)pair * 131072 + lane * 16;
#pragma unroll
    for (int i = 0; i < 32; ++i) {
      const int row = 4 * i + q4;
      unsigned keep;
      asm volatile("s_mov_b32 %0, m0\n\ts_mov_b32 m0, %2\n\ts_nop 0\n\tglobal_load_lds_dwordx4 %1, off\n\ts_mov_b32 m0, %0"
                   : "=&s"(keep) : "v"(src + row * 1024), "s"(lbase + L_C + row * ROW) : "memory");
    }
  }
  TS(1);
  float cs4[4] = {0.f, 0.f, 0.f, 0.f};
  float csg = 0.f;
  v4u ID0, ID1;
#pragma unroll
  for (int d = 0; d < 4; ++d) { const bool on = (g == (col >> 2)) && ((col & 3) == d); ID0[d] = on ? 0x00003F80u : 0u; ID1[d] = on ? 0x3F800000u : 0u; }
  const f32x4 Z4 = {0.f, 0.f, 0.f, 0.f};

  auto layer = [&](auto LY) __attribute__((always_inline)) {
    constexpr int ly = LY.value, LYO = 7 * ly;
    const unsigned SM = OFF_SM + ly * SM_STRIDE, OP = OFF_OP + ly * OP_STRIDE;
    const unsigned QS = ly == 0 ? OFF_QS0 : SM + SM_QS;
    constexpr int T0 = 2 + ly * 14;
    if (lo) {
      const v4u xa = {dppu<0x00>(xp), dppu<0x55>(xp), dppu<0xAA>(xp), dppu<0xFF>(xp)};
      f32x4 c0 = mfma(xa, wA[0], Z4), c1 = mfma(xa, wA[1], Z4), c2 = mfma(xa, wA[2], Z4), c3 = mfma(xa, wA[3], Z4);
      const unsigned qoff = (((2 * hpq + (q4 >> 1)) * 4 + 2 * (q4 & 1) + (col >> 3)) * 16 + msq);
      __builtin_amdgcn_raw_buffer_store_b64(v2u{pk(c0.x, c1.x), pk(c2.x, c3.x)}, rW, SM + SM_QP + qoff * 256 + g * 64 + (col & 7) * 8, 0, SC1);
      if (wv == 3) {
        const float x0 = blo(xp), x1 = bhi(xp);
        float s1 = x0 + x1, s2 = x0 * x0 + x1 * x1;
        s1 += dppf<XOR1>(s1); s2 += dppf<XOR1>(s2); s1 += dppf<XOR2>(s1); s2 += dppf<XOR2>(s2);
        float sv = add16(s1, s2); sv = add32(sv, sv);
        __builtin_amdgcn_raw_buffer_store_b32(u_(sv), rW, (g < 2 && (col & 3) == 0) ? QS + (hpq * 16 + msq) * 32 + (col >> 2) * 8 + g * 4 : OOB, 0, SC1);
      }
      if (ly == 0) {
        const v4u on = {ONES, ONES, ONES, ONES};
        c0 = mfma(on, wA[0], Z4); c1 = mfma(on, wA[1], Z4); c2 = mfma(on, wA[2], Z4); c3 = mfma(on, wA[3], Z4);
        __builtin_amdgcn_raw_buffer_store_b64(v2u{pk(c0.x, c1.x), pk(c2.x, c3.x)}, rW, g == 0 ? OFF_CSP + qoff * 64 + (col & 7) * 8 : OOB, 0, SC1);
        VM0();
        unsigned old = 0;
        if (lane == 0) old = __hip_atomic_fetch_add((LAS unsigned*)(L + L_CNT), 1u, __ATOMIC_RELAXED, __HIP_MEMORY_SCOPE_WORKGROUP);
        if (__builtin_amdgcn_readfirstlane(old) == 3u)
          __builtin_amdgcn_raw_buffer_store_b64(v2u{(unsigned)tag, (unsigned)(tag >> 32)}, rW, lane == 0 ? OFF_FL + w * 8 : OOB, 0, SC1);
        TS(T0 + 0);
        NAP(D_HQ);
        for (int sp = 0;; ++sp) {
          MEMF();
          const v4u f0 = __builtin_amdgcn_raw_buffer_load_b128(rW, OFF_FL + lane * 16, 0, SC1), f1 = __builtin_amdgcn_raw_buffer_load_b128(rW, OFF_FL + 1024 + lane * 16, 0, SC1);
          const unsigned tl = (unsigned)tag, th = (unsigned)(tag >> 32);
          const unsigned bad = (f0.x ^ tl) | (f0.y ^ th) | (f0.z ^ tl) | (f0.w ^ th) | (f1.x ^ tl) | (f1.y ^ th) | (f1.z ^ tl) | (f1.w ^ th);
          if (__builtin_amdgcn_ballot_w64(bad != 0u) == 0ull || sp > SPIN_MAX) { RETRY(D_HQ); break; }
          NAP(D_RT);
        }
        MEMF();
        if (w == 0 && wv == 0) {
          const u64 e1 = e0 + 1ull;
          __builtin_amdgcn_raw_buffer_store_b64(v2u{(unsigned)e1, (unsigned)(e1 >> 32)}, rW, lane == 0 ? OFF_E : OOB, 0, SC1);
        }
      } else {
        TS(T0 + 0);
        NAP(LYO + D_HQ);
      }
      TS(T0 + 1);
      v4u qa[4]; v2u qs = {0u, 0u};
      {
        const unsigned base = SM + SM_QP + (h * 4 + q4) * 4096 + (col & 3) * 256 + (col >> 2) * 64 + g * 16;
        const unsigned soff = wv == 0 ? QS + ((h >> 1) * 16 + col) * 32 + g * 8 : OOB;
        for (int sp = 0;; ++sp) {
          MEMF();
#pragma unroll
          for (int i = 0; i < 4; ++i) qa[i] = __builtin_amdgcn_raw_buffer_load_b128(rW, base + i * 1024, 0, SC1);
          qs = __builtin_amdgcn_raw_buffer_load_b64(rW, soff, 0, SC1);
          if (ly == 0) break;
          const unsigned m = max(max(max(umax4(qa[0]), umax4(qa[1])), max(umax4(qa[2]), umax4(qa[3]))), max(qs.x, qs.y));
          if (__builtin_amdgcn_ballot_w64(m == SENT) == 0ull || sp > SPIN_MAX) { RETRY(LYO + D_HQ); break; }
          NAP(D_RT);
        }
      }
      f32x4 ac[4] = {Z4, Z4, Z4, Z4};
#pragma unroll
      for (int i = 0; i < 4; ++i)
#pragma unroll
        for (int t = 0; t < 4; ++t) ac[t] = mfma(qa[i], wA[4 + t], ac[t]);
      f32x4 pv = {sum4(ac[0]), sum4(ac[1]), sum4(ac[2]), sum4(ac[3])};
      if (wv != 0) {
        *(LAS f32x4*)(L + L_R + q4 * 1024 + lane * 16) = pv;
        MEMF();
        if (lane == 0) __hip_atomic_fetch_add((LAS unsigned*)(L + L_CNT + 4), 1u, __ATOMIC_RELAXED, __HIP_MEMORY_SCOPE_WORKGROUP);
        TS(T0 + 2);
        nap(3); NAP(LYO + D_H1);
      } else {
        float s1 = f_(qs.x), s2 = f_(qs.y);
        s1 += dppf<XOR1>(s1); s2 += dppf<XOR1>(s2); s1 += dppf<XOR2>(s1); s2 += dppf<XOR2>(s2);
        s1 += dppf<HMIR>(s1); s2 += dppf<HMIR>(s2); s1 += dppf<MIR>(s1); s2 += dppf<MIR>(s2);
        const float mu = s1 * (1.f / 512.f), var = fmaxf(s2 * (1.f / 512.f) - mu * mu, 0.f);
        const float sc = __builtin_amdgcn_rsqf(var + 1e-5f) * (0.08838834764831845f * 1.4426950408889634f);
        f32x4 p1, p2, p3;
        for (int sp = 0;; ++sp) {
          MEMF();
          const unsigned c = *(volatile LAS unsigned*)(L + L_CNT + 4);
          MEMF();
          p1 = *(LAS f32x4*)(L + L_R + 1024 + lane * 16); p2 = *(LAS f32x4*)(L + L_R + 2048 + lane * 16); p3 = *(LAS f32x4*)(L + L_R + 3072 + lane * 16);
          if (__builtin_amdgcn_readfirstlane(c) >= 3u * (ly + 1) || sp > SPIN_MAX) break;
        }
        pv = (pv + p1) + (p2 + p3);
        const float o0 = (pv.x - mu * cs4[0]) * sc, o1 = (pv.y - mu * cs4[1]) * sc, o2 = (pv.z - mu * cs4[2]) * sc, o3 = (pv.w - mu * cs4[3]) * sc;
        __builtin_amdgcn_raw_buffer_store_b64(v2u{pk(o0, o1), pk(o2, o3)}, rW, SM + SM_QA + (g * 32 + h) * 1024 + j * 128 + col * 8, 0, SC1);
        TS(T0 + 2);
        NAP(LYO + D_H1);
      }
      unsigned off[4]; v4u qv[4];
#pragma unroll
      for (int i = 0; i < 4; ++i) off[i] = SM + SM_QA + (bA * 32 + hg * 16 + 4 * q4 + i) * 1024 + lane * 16;
      HOP_LOAD(4, off, qv, LYO + D_H1)
#pragma unroll
      for (int i = 0; i < 4; ++i) *(LAS v4u*)(L + L_Q + (4 * q4 + i) * ROW + lane * 16) = qv[i];
    } else if (ly == 0) {
      VM0();
    }
    TS(T0 + 3);
    v4u cf[16];
    const int rho = 2 * (col & 7) + (col >> 3);
    if (ly == 1) {
#pragma unroll
      for (int s = 0; s < 16; ++s) cf[s] = *(LAS v4u*)(L + L_C + (16 * wv + rho) * ROW + g * 256 + s * 16);
    }
    BAR();
    unsigned tv[16], to[16];
    if (ly == 0) {
#pragma unroll
      for (int s = 0; s < 16; ++s) cf[s] = *(LAS v4u*)(L + L_C + (16 * wv + rho) * ROW + g * 256 + s * 16);
      if (!lo) {
        const rsrc_t rV = RS(a.wuv, 4194304), rO = RS(a.wo, 4194304);
#pragma unroll
        for (int e = 0; e < 16; ++e)
          tv[e] = __builtin_amdgcn_raw_buffer_load_b32(rV, (64 * j + 32 * (e >> 3) + 16 * ((e >> 2) & 1) + 4 * g + (e & 3)) * 8192 + h * 256 + q4 * 64 + col * 4, 0, 0);
#pragma unroll
        for (int e = 0; e < 16; ++e)
          to[e] = __builtin_amdgcn_raw_buffer_load_b32(rO, ((2 * hpv + (e >> 3)) * 128 + 32 * q4 + 8 * g + (e & 7)) * 1024 + mcol(msv, 2 * col) * 2, 0, 0);
        SB();
      }
    }
    f32x4 sc4 = Z4;
#pragma unroll
    for (int s = 0; s < 16; ++s) sc4 = mfma(cf[s], *(LAS v4u*)(L + L_Q + col * ROW + g * 256 + s * 16), sc4);
    float mx = fmaxf(fmaxf(sc4.x, sc4.y), fmaxf(sc4.z, sc4.w));
    mx = max16(mx); mx = max32(mx);
    if (g == 0) *(LAS float*)(L + L_MX + col * 32 + wv * 4) = mx;
    {
      const int qq = 4 * g + (col >> 2), rr = 2 * (qq & 7) + (qq >> 3);
      LAS unsigned char* tb = L + L_C + rr * ROW + wv * 128 + (col & 3) * 8;
#pragma unroll
      for (int s = 0; s < 4; ++s)
#pragma unroll
        for (int t = 0; t < 4; ++t) {
          const v2u u0 = __builtin_bit_cast(v2u, __builtin_amdgcn_ds_read_tr16_b64_v4i16((LAS s16x4*)(tb + (32 * s) * ROW + t * 32)));
          const v2u u1 = __builtin_bit_cast(v2u, __builtin_amdgcn_ds_read_tr16_b64_v4i16((LAS s16x4*)(tb + (32 * s + 16) * ROW + t * 32)));
          cf[4 * s + t] = v4u{u0.x, u0.y, u1.x, u1.y};
        }
    }
    BAR();
    {
      const f32x4 m0 = *(LAS f32x4*)(L + L_MX + col * 32), m1 = *(LAS f32x4*)(L + L_MX + col * 32 + 16);
      mx = fmaxf(fmaxf(fmaxf(m0.x, m0.y), fmaxf(m0.z, m0.w)), fmaxf(fmaxf(m1.x, m1.y), fmaxf(m1.z, m1.w)));
      const float p0 = __builtin_amdgcn_exp2f(sc4.x - mx), p1 = __builtin_amdgcn_exp2f(sc4.y - mx), p2 = __builtin_amdgcn_exp2f(sc4.z - mx), p3 = __builtin_amdgcn_exp2f(sc4.w - mx);
      *(LAS v2u*)(L + L_P + (wv >> 1) * 1024 + lane * 16 + (wv & 1) * 8) = v2u{pk(p0, p1), pk(p2, p3)};
    }
    BAR();
    TS(T0 + 4);
    {
      v4u pf[4];
#pragma unroll
      for (int s = 0; s < 4; ++s) pf[s] = *(LAS v4u*)(L + L_P + s * 1024 + lane * 16);
      f32x4 o[4] = {Z4, Z4, Z4, Z4};
#pragma unroll
      for (int t = 0; t < 4; ++t)
#pragma unroll
        for (int s = 0; s < 4; ++s) o[t] = mfma(cf[4 * s + t], pf[s], o[t]);
      const int hh = hg * 16 + col;
      const unsigned ob = OP + ((((hh >> 1) * 8 + wv) * 2) * 4 + bA) * 4096 + kc * 128 + (hh & 1) * 64 + g * 16;
#pragma unroll
      for (int u = 0; u < 2; ++u)
        __builtin_amdgcn_raw_buffer_store_b128(v4u{pk(o[2 * u].x, o[2 * u].y), pk(o[2 * u].z, o[2 * u].w), pk(o[2 * u + 1].x, o[2 * u + 1].y), pk(o[2 * u + 1].z, o[2 * u + 1].w)},
                                               rW, ob + u * 16384, 0, SC1);
      float l = 0.f;
#pragma unroll
      for (int s = 0; s < 4; ++s) { l = dot2(pf[s].x, ONES, l); l = dot2(pf[s].y, ONES, l); l = dot2(pf[s].z, ONES, l); l = dot2(pf[s].w, ONES, l); }
      l = add16(l, l); l = add32(l, l);
      TS(T0 + 5);
      VM0();
      __builtin_amdgcn_raw_buffer_store_b64(v2u{u_(mx), u_(l)}, rW, g == 0 ? SM + SM_MLF + ((hh * 8 + wv) * 4 + bA) * 256 + kc * 8 : OOB, 0, SC1);
    }
    TS(T0 + 6);
    if (ly == 0 && !lo) {
      unsigned t8[8];
#pragma unroll
      for (int u = 0; u < 2; ++u) {
#pragma unroll
        for (int e = 0; e < 8; ++e) t8[e] = tv[8 * u + e];
        tr2(t8, wA[2 * u], wA[2 * u + 1]);
#pragma unroll
        for (int e = 0; e < 8; ++e) t8[e] = to[8 * u + e];
        tr2(t8, wA[4 + 2 * u], wA[4 + 2 * u + 1]);
      }
    }
    NAP(LYO + D_ML);
    v2u gt[16], dt[8];
    {
      const int b = wv >> 1, ks = wv & 1;
      v2u ml;
      const unsigned moff = SM + SM_MLF + ((h * 8 + j) * 4 + b) * 256 + (16 * ((col >> 2) & 1) + 4 * g + (col & 3)) * 8;
      for (int sp = 0;; ++sp) {
        MEMF();
        ml = __builtin_amdgcn_raw_buffer_load_b64(rW, moff, 0, SC1);
        if (__builtin_amdgcn_ballot_w64(max(ml.x, ml.y) == SENT) == 0ull || sp > SPIN_MAX) { RETRY(LYO + D_ML); break; }
        NAP(D_RT);
      }
      MEMF();
      TS(T0 + 7);
      const unsigned ob = OP + ((((h >> 1) * 8 + j) * 2 + ks) * 4 + b) * 4096 + col * 128 + (h & 1) * 64 + g * 16;
      const v4u o0 = __builtin_amdgcn_raw_buffer_load_b128(rW, ob, 0, SC1), o1 = __builtin_amdgcn_raw_buffer_load_b128(rW, ob + 2048, 0, SC1);
      v4u csv = {0u, 0u, 0u, 0u};
      if (ly == 0) {
        if (lo) csv = __builtin_amdgcn_raw_buffer_load_b128(rW, OFF_CSP + (h * 4 + q4) * 1024 + col * 64 + g * 16, 0, SC1);
        const rsrc_t rG = RS(a.wg, 1835008), rU = RS(a.wu, 1835008), rD = RS(a.wd, 1835008);
        (void)rG; (void)rU;
        const GAS unsigned char* gb = (const GAS unsigned char*)(col < 8 ? a.wg : a.wu) + (fact ? fs : 0) * 64 + (col & 7) * 8;
#pragma unroll
        for (int e = 0; e < 16; ++e) gt[e] = *(const GAS v2u*)(gb + (size_t)mcol(2 * wv + (e >> 3), 8 * g + (e & 7)) * 3584);
#pragma unroll
        for (int e = 0; e < 8; ++e) dt[e] = __builtin_amdgcn_raw_buffer_load_b64(rD, fact ? (32 * fs + 8 * g + e) * 1024 + wv * 128 + col * 8 : OOB, 0, 0);
        const v4u f = {SENT, SENT, SENT, SENT};
        __builtin_amdgcn_raw_buffer_store_b128(f, rW, tid < 384 ? OFF_SM + SM_STRIDE + w * 6144 + tid * 16 : OOB, 0, SC1);
        SB();
      }
      float m = f_(ml.x), M = m;
      M = fmaxf(M, dppf<XOR1>(M)); M = fmaxf(M, dppf<XOR2>(M)); M = fmaxf(M, dppf<HMIR>(M)); M = max16(M); M = max32(M);
      const float wt = __builtin_amdgcn_exp2f(m - M);
      float Ls = wt * f_(ml.y);
      Ls += dppf<XOR1>(Ls); Ls += dppf<XOR2>(Ls); Ls += dppf<HMIR>(Ls); Ls = add16(Ls, Ls); Ls = add32(Ls, Ls);
      const float wn = wt * __builtin_amdgcn_rcpf(Ls);
      const float w0 = dppf<BCAST + 0>(wn), w1 = dppf<BCAST + 1>(wn), w2 = dppf<BCAST + 2>(wn), w3 = dppf<BCAST + 3>(wn);
      const float w4 = dppf<BCAST + 4>(wn), w5 = dppf<BCAST + 5>(wn), w6 = dppf<BCAST + 6>(wn), w7 = dppf<BCAST + 7>(wn);
      const f32x4 d00 = mfma(o0, ID0, Z4), d01 = mfma(o0, ID1, Z4), d10 = mfma(o1, ID0, Z4), d11 = mfma(o1, ID1, Z4);
      const float z0 = ((w0 * d00.x + w1 * d00.y) + (w2 * d00.z + w3 * d00.w)) + ((w4 * d10.x + w5 * d10.y) + (w6 * d10.z + w7 * d10.w));
      const float z1 = ((w0 * d01.x + w1 * d01.y) + (w2 * d01.z + w3 * d01.w)) + ((w4 * d11.x + w5 * d11.y) + (w6 * d11.z + w7 * d11.w));
      float z = add32(z0, z1); z = add16(z, z);
      *(LAS unsigned short*)(L + L_CX + b * 128 + ks * 64 + (2 * col + (g >> 1)) * 2) = (unsigned short)pk(z, z);
      if (ly == 0 && lo) {
        f32x4 e4;
#pragma unroll
        for (int t = 0; t < 4; ++t) { float v = sum4(mfma(csv, wA[4 + t], Z4)); v = add16(v, v); e4[t] = add32(v, v); }
        *(LAS f32x4*)(L + L_Q + q4 * 1024 + lane * 16) = e4;
      }
    }
    BAR();
    TS(T0 + 8);
    if (!lo) {
      const v4u a0 = *(LAS v4u*)(L + L_CX + (col >> 2) * 128 + g * 16), a1 = *(LAS v4u*)(L + L_CX + (col >> 2) * 128 + 64 + g * 16);
      f32x4 c0 = mfma(a0, wA[0], Z4), c1 = mfma(a0, wA[1], Z4);
      c0 = mfma(a1, wA[2], c0); c1 = mfma(a1, wA[3], c1);
      __builtin_amdgcn_raw_buffer_store_b32(pk(c0.x, c1.x), rW, SM + SM_CP + ((h * 4 + q4) * 8 + j) * 256 + g * 64 + col * 4, 0, SC1);
      f32x2 xr = {0.f, 0.f};
      if (resid) xr = *(LAS f32x2*)(L + L_XR + g * 128 + col * 8);
      NAP(LYO + D_HV);
      TS(T0 + 9);
      unsigned off[4]; v4u cv[4];
#pragma unroll
      for (int i = 0; i < 4; ++i) off[i] = SM + SM_CP + ((2 * hpv + (i >> 1)) * 4 + q4) * 2048 + (i & 1) * 1024 + (col & 3) * 256 + (col >> 2) * 64 + g * 16;
      HOP_LOAD(4, off, cv, LYO + D_HV)
      c0 = Z4; c1 = Z4;
#pragma unroll
      for (int i = 0; i < 4; ++i) { c0 = mfma(cv[i], wA[4 + 2 * (i >> 1)], c0); c1 = mfma(cv[i], wA[5 + 2 * (i >> 1)], c1); }
      f32x2 v = {sum4(c0), sum4(c1)};
      if (q4 == 0) v += xr;
      *(LAS f32x2*)(L + L_R + q4 * 512 + lane * 8) = v;
    } else if (ly == 0 && wv == 0) {
      const f32x4 e = (*(LAS f32x4*)(L + L_Q + lane * 16) + *(LAS f32x4*)(L + L_Q + 1024 + lane * 16)) + (*(LAS f32x4*)(L + L_Q + 2048 + lane * 16) + *(LAS f32x4*)(L + L_Q + 3072 + lane * 16));
      cs4[0] = e.x; cs4[1] = e.y; cs4[2] = e.z; cs4[3] = e.w;
    }
    if (ly == 0) {
      VM0();
      v2u t8[8];
#pragma unroll
      for (int u = 0; u < 2; ++u) {
#pragma unroll
        for (int e = 0; e < 8; ++e) t8[e] = gt[8 * u + e];
        tr4(t8, wG[4 * u], wG[4 * u + 1], wG[4 * u + 2], wG[4 * u + 3]);
      }
      tr4(dt, wD[0], wD[1], wD[2], wD[3]);
      const v4u on = {ONES, ONES, ONES, ONES};
      float e4[4];
#pragma unroll
      for (int t = 0; t < 4; ++t) e4[t] = mfma(on, wG[4 + t], mfma(on, wG[t], Z4)).x;
      const float ev = g == 0 ? e4[0] : g == 1 ? e4[1] : g == 2 ? e4[2] : e4[3];
      *(LAS float*)(L + L_GU + (wv >> 2) * 1024 + (4 * col + g) * 16 + q4 * 4) = ev;
    }
    BAR();
    if (wv == 0) {
      const f32x2 v = (*(LAS f32x2*)(L + L_R + lane * 8) + *(LAS f32x2*)(L + L_R + 512 + lane * 8)) + (*(LAS f32x2*)(L + L_R + 1024 + lane * 8) + *(LAS f32x2*)(L + L_R + 1536 + lane * 8));
      __builtin_amdgcn_raw_buffer_store_b32(pk(v.x, v.y), rW, SM + SM_XP + ((g * 16 + msv) * 16 + hpv) * 64 + col * 4, 0, SC1);
    }
    const int gidx = (col >> 3) * 32 + 8 * g + (col & 7);
    if (ly == 0) {
      csg = sum4(*(LAS f32x4*)(L + L_GU + gidx * 16)) + sum4(*(LAS f32x4*)(L + L_GU + 1024 + gidx * 16));
      BAR();
    }
    TS(T0 + 10);
    if (wv != 0) nap(2);
    NAP(LYO + D_H3);
    {
      unsigned off[2]; v4u xv[2];
#pragma unroll
      for (int i = 0; i < 2; ++i) off[i] = SM + SM_XP + (bF * 16 + 2 * wv + i) * 1024 + col * 64 + g * 16;
      HOP_LOAD(2, off, xv, LYO + D_H3)
      TS(T0 + 11);
      const f32x4 i0 = mfma(xv[0], ID0, Z4), i1 = mfma(xv[0], ID1, Z4), i2 = mfma(xv[1], ID0, Z4), i3 = mfma(xv[1], ID1, Z4);
      f32x4 ac[4];
#pragma unroll
      for (int t = 0; t < 4; ++t) ac[t] = mfma(xv[1], wG[4 + t], mfma(xv[0], wG[t], Z4));
      float x0 = add32(sum4(i0), sum4(i2)), x1 = add32(sum4(i1), sum4(i3));
      x0 = add16(x0, x0); x1 = add16(x1, x1);
      float sv = (g & 1) ? x0 * x0 + x1 * x1 : x0 + x1;
      sv += dppf<XOR1>(sv); sv += dppf<XOR2>(sv); sv += dppf<HMIR>(sv); sv += dppf<MIR>(sv); sv = add32(sv, sv);
      if (col == 0 && g < 2) *(LAS float*)(L + L_ST + g * 32 + wv * 4) = sv;
      const float ra = add32(sum4(ac[0]), sum4(ac[2])), rb = add32(sum4(ac[1]), sum4(ac[3]));
      *(LAS float*)(L + L_GU + (wv >> 2) * 1024 + (4 * col + g) * 16 + q4 * 4) = add16(ra, rb);
      BAR();
      const float su = sum4(*(LAS f32x4*)(L + L_GU + gidx * 16)) + sum4(*(LAS f32x4*)(L + L_GU + 1024 + gidx * 16));
      const float sa = sum4(*(LAS f32x4*)(L + L_ST + (col >> 3) * 32)) + sum4(*(LAS f32x4*)(L + L_ST + (col >> 3) * 32 + 16));
      const float s1 = dppf<BCAST + 0>(sa), s2 = dppf<BCAST + 8>(sa);
      const float mu = s1 * (1.f / 512.f), var = fmaxf(s2 * (1.f / 512.f) - mu * mu, 0.f);
      const float val = (su - mu * csg) * __builtin_amdgcn_rsqf(var + 1e-5f);
      const float oth = dppf<ROR8>(val);
      const float gg = col < 8 ? val : oth;
      const float hv = val * oth * __builtin_amdgcn_rcpf(1.f + __builtin_amdgcn_exp2f(-1.4426950408889634f * gg));
      const unsigned hp2 = pk(hv, dppf<SHL + 1>(hv));
      const v4u ha = {hp2, dppu<SHL + 2>(hp2), dppu<SHL + 4>(hp2), dppu<SHL + 6>(hp2)};
      const float r0 = fs == 0 ? x0 : 0.f, r1 = fs == 0 ? x1 : 0.f;
      const f32x4 y0 = mfma(ha, wD[0], f32x4{r0, 0.f, 0.f, 0.f}), y1 = mfma(ha, wD[1], f32x4{r1, 0.f, 0.f, 0.f});
      const f32x4 y2 = mfma(ha, wD[2], f32x4{r0, 0.f, 0.f, 0.f}), y3 = mfma(ha, wD[3], f32x4{r1, 0.f, 0.f, 0.f});
      const unsigned yo = g < 2 ? pk(y0.x, y1.x) : pk(y2.x, y3.x);
      __builtin_amdgcn_raw_buffer_store_b32(yo, rW, (fact && !(g & 1)) ? SM + SM_YP + (((2 * wv + (g >> 1)) * 4 + bF) * 56 + fs) * 64 + col * 4 : OOB, 0, SC1);
    }
    TS(T0 + 12);
    if (ly == 1 && !resid) return;
    NAP(LYO + D_H4);
    {
      const int b = wv >> 1, hf = wv & 1;
      unsigned off[2]; v4u yv[2];
      off[0] = SM + SM_YP + ((msq * 4 + b) * 56 + 28 * hf + col) * 64 + g * 16;
      off[1] = col < 12 ? off[0] + 1024 : OOB;
      HOP_LOAD(2, off, yv, LYO + D_H4)
      TS(T0 + 13);
      const f32x4 d0 = mfma(yv[1], ID0, mfma(yv[0], ID0, Z4)), d1 = mfma(yv[1], ID1, mfma(yv[0], ID1, Z4));
      float z = add32(sum4(d0), sum4(d1)); z = add16(z, z);
      *(LAS float*)(L + L_X1 + (b * 2 + hf) * 128 + (2 * col + (g >> 1)) * 4) = z;
    }
    BAR();
    if (lo) {
      const int o = (col >> 2) * 256 + (8 * g + 2 * (col & 3)) * 4;
      const f32x2 v = *(LAS f32x2*)(L + L_X1 + o) + *(LAS f32x2*)(L + L_X1 + o + 128);
      xp = pk(v.x, v.y);
      if (ly == 0) {
        if (wv == 0) *(LAS f32x2*)(L + L_XR + (col >> 2) * 128 + (8 * g + 2 * (col & 3)) * 4) = f32x2{blo(xp), bhi(xp)};
      } else if (wv == 0) {
        *(GAS unsigned*)(a.out + (col >> 2) * 1024 + mcol(msq, 8 * g + 2 * (col & 3)) * 2) = xp;
      }
    }
  };
  layer(IC<0>{});
  layer(IC<1>{});
  TS(30);
  asm volatile(".Lpc_end:");
}

extern "C" void kernel_launch(void* const* d_in, const int* in_sizes, int n_in, void* d_out, int out_size, void* d_ws, size_t ws_size,
                              hipStream_t stream) {
  Args a;
  a.x = (const unsigned char*)d_in[0]; a.ckv = (const unsigned char*)d_in[1]; a.wq = (const unsigned char*)d_in[2];
  a.wuk = (const unsigned char*)d_in[3]; a.wuv = (const unsigned char*)d_in[4]; a.wo = (const unsigned char*)d_in[5];
  a.wg = (const unsigned char*)d_in[7]; a.wu = (const unsigned char*)d_in[8]; a.wd = (const unsigned char*)d_in[9];
  a.out = (unsigned char*)d_out; a.ws = (unsigned char*)d_ws;
  decode_block<<<256, 512, 0, stream>>>(a);
}
```

```cpp
#include <hip/hip_runtime.h>

struct Buf { const void* p; unsigned bytes, flags; };
struct Args {
  Buf ws, x, wq, wuk, ckv, wuv, wo, wg, wu, wd, out;
};

__global__ void __launch_bounds__(512, 2) decode_block(Args a) {
  __shared__ __attribute__((aligned(16))) unsigned char smem[163712];
  unsigned tid = threadIdx.x;
  asm volatile(
      "s_load_dwordx16 s[4:19], s[0:1], 0x0\n"
      "s_load_dwordx16 s[20:35], s[0:1], 0x40\n"
      "s_load_dwordx8 s[36:43], s[0:1], 0x80\n"
      "s_load_dwordx4 s[44:47], s[0:1], 0xa0\n"
      "v_readfirstlane_b32 s48, v0\n"
      "v_and_b32 v0, 63, v0\n"
      "s_lshr_b32 s51, s2, 3\n"
      "s_and_b32 s50, s2, 7\n"
      "v_lshlrev_b32 v3, 4, v0\n"
      "s_lshr_b32 s48, s48, 6\n"
      "s_and_b32 s49, s48, 3\n"
      "v_and_b32_e64 v1, 15, v0\n"
      "v_lshrrev_b32_e64 v2, 4, v0\n"
      "s_lshr_b32 s66, s51, 4\n"
      "s_lshl1_add_u32 s54, s50, s66\n"
      "s_and_b32 s55, s51, 15\n"
      "s_cmp_gt_u32 s48, 3\n"
      "s_cbranch_scc1 .Lhi0\n"
      "s_setprio 3\n"
      "v_lshrrev_b32_e64 v253, 3, v1\n"
      "v_lshlrev_b32_e64 v168, 12, v253\n"
      "v_lshl_add_u32 v168, v2, 6, v168\n"
      "v_and_b32 v253, 7, v1\n"
      "v_lshl_add_u32 v168, v253, 3, v168\n"
      "s_lshl_b32 s90, s55, 15\n"
      "s_lshl_b32 s71, s49, 13\n"
      "s_add_i32 s90, s90, s71\n"
      "s_lshl_b32 s71, s54, 8\n"
      "s_add_i32 s90, s90, s71\n"
      "s_add_i32 s90, s90, 0x140000\n"
      "v_lshlrev_b32 v7, 16, v2\n"
      "v_lshl_add_u32 v7, v1, 3, v7\n"
      "s_lshl_b32 s67, s54, 18\n"
      "s_lshl_b32 s71, s55, 9\n"
      "s_add_i32 s67, s67, s71\n"
      "s_lshl_b32 s71, s49, 7\n"
      "s_add_i32 s67, s67, s71\n"
      "v_mov_b32_e64 v212, -1\n"
      "v_mov_b32_e64 v213, -1\n"
      "v_mov_b32_e64 v214, -1\n"
      "v_mov_b32_e64 v215, -1\n"
      "s_lshl2_add_u32 s70, s2, s49\n"
      "s_lshl_b32 s70, s70, 10\n"
      "s_add_i32 s70, s70, 0x40000\n"
      "s_lshl_b32 s66, s49, 10\n"
      "s_movk_i32 s68, 0x2000\n"
      "s_waitcnt lgkmcnt(0)\n"
      "s_add_u32 s4, s4, 0x3fffff\n"
      "s_addc_u32 s5, s5, 0\n"
      "s_and_b32 s4, s4, 0xffc00000\n"
      "buffer_store_dwordx2 v[212:213], v168, s[4:7], s90 offen sc1\n"
      "buffer_load_dwordx2 v[228:229], off, s[4:7], 0 sc1\n"
      "s_setprio 2\n"
      "s_memrealtime s[64:65]\n"
      "buffer_load_dwordx4 v[208:211], v3, s[8:11], s66 offen\n"
      "buffer_load_dwordx2 v[144:145], v7, s[12:15], s67 offen\n"
      "s_add_i32 s67, s67, s68\n"
      "buffer_load_dwordx2 v[146:147], v7, s[12:15], s67 offen\n"
      "s_add_i32 s67, s67, s68\n"
      "buffer_load_dwordx2 v[148:149], v7, s[12:15], s67 offen\n"
      "s_add_i32 s67, s67, s68\n"
      "buffer_load_dwordx2 v[150:151], v7, s[12:15], s67 offen\n"
      "s_add_i32 s67, s67, s68\n"
      "buffer_load_dwordx2 v[152:153], v7, s[12:15], s67 offen\n"
      "s_add_i32 s67, s67, s68\n"
      "buffer_load_dwordx2 v[154:155], v7, s[12:15], s67 offen\n"
      "s_add_i32 s67, s67, s68\n"
      "buffer_load_dwordx2 v[156:157], v7, s[12:15], s67 offen\n"
      "s_add_i32 s67, s67, s68\n"
      "buffer_load_dwordx2 v[158:159], v7, s[12:15], s67 offen\n"
      "buffer_store_dwordx4 v[212:215], v3, s[4:7], s70 offen sc1\n"
      "s_barrier\n"
      "s_branch .Llo\n"
      ".Lhi0:\n"
      "s_getpc_b64 s[66:67]\n"
      ".Lpc_here:\n"
      "s_cmp_lg_u32 s48, 4\n"
      "s_cbranch_scc1 .Lprobe\n"
      "s_lshl_b32 s68, s51, 7\n"
      "v_lshl_add_u32 v5, v0, 12, s68\n"
      "s_mov_b32 s68, .Lpc_end-.Lpc_here\n"
      "v_cmp_gt_u32 vcc, s68, v5\n"
      "s_and_b64 exec, exec, vcc\n"
      "s_cbranch_execz .Lprobe\n"
      "global_load_dword v248, v5, s[66:67]\n"
      ".Lprobe:\n"
      "s_mov_b64 exec, -1\n"
      "v_mov_b32 v252, 133120\n"
      "v_mov_b32_e64 v254, 0\n"
      "v_lshl_add_u32 v5, v1, 2, v252\n"
      "ds_write_b32 v5, v254 offset:24352\n"
      "ds_write_b32 v5, v254 offset:24384\n"
      "v_mov_b32_e64 v254, 1\n"
      "s_lshr_b32 s66, s51, 3\n"
      "s_lshl2_add_u32 s52, s50, s66\n"
      "s_and_b32 s53, s51, 7\n"
      "s_lshr_b32 s56, s50, 1\n"
      "s_bfe_u32 s71, s50, 0x10000\n"
      "s_lshl_b32 s57, s71, 4\n"
      "s_bfe_u32 s71, s51, 0x40001\n"
      "s_add_i32 s57, s57, s71\n"
      "s_bfe_u32 s71, s2, 0x20006\n"
      "s_lshl_b32 s70, s71, 14\n"
      "s_bfe_u32 s71, s2, 0x60000\n"
      "s_lshl_b32 s71, s71, 7\n"
      "s_add_i32 s70, s70, s71\n"
      "s_bfe_u32 s71, s49, 0x10000\n"
      "s_lshl_b32 s71, s71, 13\n"
      "s_add_i32 s70, s70, s71\n"
      "s_add_i32 s70, s70, 0x470000\n"
      "s_lshl_b32 s66, s49, 12\n"
      "s_add_i32 m0, s66, 133120\n"
      "s_lshl_b32 s69, s53, 19\n"
      "s_lshl_b32 s71, s52, 8\n"
      "s_add_i32 s69, s69, s71\n"
      "s_lshl_b32 s71, s49, 17\n"
      "s_add_i32 s69, s69, s71\n"
      "s_lshl_b32 s67, s49, 2\n"
      "v_xor_b32 v6, s67, v1\n"
      "v_lshlrev_b32 v7, 13, v2\n"
      "v_xor_b32_e64 v5, 0, v6\n"
      "v_lshl_add_u32 v244, v5, 4, v7\n"
      "v_xor_b32 v5, 1, v6\n"
      "v_lshl_add_u32 v245, v5, 4, v7\n"
      "v_xor_b32 v5, 2, v6\n"
      "v_lshl_add_u32 v246, v5, 4, v7\n"
      "v_xor_b32 v5, 3, v6\n"
      "v_lshl_add_u32 v247, v5, 4, v7\n"
      "s_lshl_b32 s68, s56, 22\n"
      "s_lshl_b32 s71, s57, 17\n"
      "s_add_i32 s68, s68, s71\n"
      "s_lshl_b32 s71, s49, 15\n"
      "s_add_i32 s68, s68, s71\n"
      "s_mul_i32 s67, s49, 33280\n"
      "s_waitcnt lgkmcnt(0)\n"
      "s_add_u32 s4, s4, 0x3fffff\n"
      "s_addc_u32 s5, s5, 0\n"
      "s_and_b32 s4, s4, 0xffc00000\n"
      "s_memtime s[84:85]\n"
      "s_load_dword s80, s[4:5], s70\n"
      "s_barrier\n"
      "s_waitcnt lgkmcnt(0)\n"
      "s_memtime s[82:83]\n"
      ".Lcw_1:\n"
      "ds_read_b32 v240, v252 offset:24412\n"
      "s_waitcnt lgkmcnt(0)\n"
      "v_cmp_gt_i32_e64 vcc, 4, v240\n"
      "s_cbranch_vccnz .Lcw_1\n"
      "buffer_load_dwordx4 v244, s[16:19], s69 offen lds\n"
      "s_addk_i32 m0, 1024\n"
      "s_add_i32 s69, s69, 0x8000\n"
      "buffer_load_dwordx4 v245, s[16:19], s69 offen lds\n"
      "s_addk_i32 m0, 1024\n"
      "s_add_i32 s69, s69, 0x8000\n"
      "buffer_load_dwordx4 v246, s[16:19], s69 offen lds\n"
      "s_addk_i32 m0, 1024\n"
      "s_add_i32 s69, s69, 0x8000\n"
      "buffer_load_dwordx4 v247, s[16:19], s69 offen lds\n"
      "s_mov_b32 m0, s67\n"
      "s_nop 0\n"
      "buffer_load_dwordx4 v3, s[20:23], s68 offen lds\n"
      "s_addk_i32 m0, 1040\n"
      "s_addk_i32 s68, 1024\n"
      "buffer_load_dwordx4 v3, s[20:23], s68 offen lds\n"
      "s_addk_i32 m0, 1040\n"
      "s_addk_i32 s68, 1024\n"
      "buffer_load_dwordx4 v3, s[20:23], s68 offen lds\n"
      "s_addk_i32 m0, 1040\n"
      "s_addk_i32 s68, 1024\n"
      "s_branch .Lhi\n"
      ".Llo:\n"
      "v_mov_b32 v252, 133120\n"
      "v_mov_b32_e64 v254, 1\n"
      "s_waitcnt lgkmcnt(0)\n"
      "s_mov_b32 s63, s64\n"
      "s_lshl_b32 s70, s55, 12\n"
      "s_lshl_b32 s71, s49, 10\n"
      "s_add_i32 s70, s70, s71\n"
      "s_lshl_b32 s71, s54, 6\n"
      "s_add_i32 s70, s70, s71\n"
      "s_add_i32 s70, s70, 0x10000\n"
      "v_lshlrev_b32_e64 v5, 3, v0\n"
      "s_waitcnt vmcnt(10)\n"
      "v_readfirstlane_b32 s82, v228\n"
      "v_readfirstlane_b32 s83, v229\n"
      "s_add_u32 s80, s82, 0x7f4a7c15\n"
      "s_addc_u32 s81, s83, 0x9e3779b9\n"
      "s_lshr_b64 s[66:67], s[80:81], 30\n"
      "s_xor_b64 s[80:81], s[80:81], s[66:67]\n"
      "s_mul_hi_u32 s68, s80, 0x1ce4e5b9\n"
      "s_mul_i32 s69, s80, 0xbf58476d\n"
      "s_add_i32 s68, s68, s69\n"
      "s_mul_i32 s69, s81, 0x1ce4e5b9\n"
      "s_add_i32 s81, s68, s69\n"
      "s_mul_i32 s80, s80, 0x1ce4e5b9\n"
      "s_lshr_b64 s[66:67], s[80:81], 31\n"
      "s_xor_b64 s[80:81], s[80:81], s[66:67]\n"
      "s_and_b32 s81, s81, 0xfff00000\n"
      "s_and_b32 s66, s63, 0xfff\n"
      "s_or_b32 s66, s66, s81\n"
      "v_mov_b32 v230, s80\n"
      "v_mov_b32 v231, s66\n"
      "s_mov_b64 exec, 0xff\n"
      "buffer_store_dwordx2 v[230:231], v5, s[4:7], s70 offen sc1\n"
      "s_mov_b64 exec, 0x1\n"
      "ds_add_u32 v252, v254 offset:24412\n"
      "s_mov_b64 exec, -1\n"
      "s_lshr_b32 s66, s51, 3\n"
      "s_lshl2_add_u32 s52, s50, s66\n"
      "s_and_b32 s53, s51, 7\n"
      "s_lshr_b32 s56, s50, 1\n"
      "s_bfe_u32 s71, s50, 0x10000\n"
      "s_lshl_b32 s57, s71, 4\n"
      "s_bfe_u32 s71, s51, 0x40001\n"
      "s_add_i32 s57, s57, s71\n"
      "s_movk_i32 s78, -8\n"
      "s_movk_i32 s79, 88\n"
      "s_movk_i32 s3, 0\n"
      "s_and_b32 s58, s51, 1\n"
      "s_mul_i32 s59, s50, 7\n"
      "s_bfe_u32 s71, s51, 0x30002\n"
      "s_add_i32 s59, s59, s71\n"
      "s_and_b32 s60, s51, 3\n"
      "s_mov_b32 s72, 0x05040100\n"
      "s_mov_b32 s73, 0x07060302\n"
      "s_mov_b32 s74, 0\n"
      "v_and_b32 v253, 1, v1\n"
      "v_lshlrev_b32 v5, 4, v253\n"
      "v_mov_b32 v6, 0xffff\n"
      "v_lshlrev_b32 v5, v5, v6\n"
      "v_bfe_u32 v6, v1, 1, 2\n"
      "v_cmp_eq_u32 vcc, 0, v6\n"
      "s_nop 1\n"
      "v_cndmask_b32 v12, 0, v5, vcc\n"
      "v_and_b32 v8, 0x3f803f80, v12\n"
      "v_cmp_eq_u32 vcc, 1, v6\n"
      "s_nop 1\n"
      "v_cndmask_b32 v13, 0, v5, vcc\n"
      "v_and_b32 v9, 0x3f803f80, v13\n"
      "v_cmp_eq_u32 vcc, 2, v6\n"
      "s_nop 1\n"
      "v_cndmask_b32 v14, 0, v5, vcc\n"
      "v_and_b32 v10, 0x3f803f80, v14\n"
      "v_cmp_eq_u32 vcc, 3, v6\n"
      "s_nop 1\n"
      "v_cndmask_b32_e64 v15, 0, v5, vcc\n"
      "v_and_b32 v11, 0x3f803f80, v15\n"
      "s_lshl_b32 s88, s48, 15\n"
      "s_bfe_u32 s71, s56, 0x10001\n"
      "s_lshl_b32 s71, s71, 14\n"
      "s_add_i32 s88, s88, s71\n"
      "s_bfe_u32 s71, s56, 0x10000\n"
      "s_lshl_b32 s71, s71, 12\n"
      "s_add_i32 s88, s88, s71\n"
      "s_lshl_b32 s71, s57, 7\n"
      "s_add_i32 s88, s88, s71\n"
      "s_add_i32 s88, s88, 0x800000\n"
      "s_lshl_b32 s89, s58, 13\n"
      "s_lshl_b32 s71, s56, 10\n"
      "s_add_i32 s89, s89, s71\n"
      "s_bfe_u32 s71, s57, 0x30000\n"
      "s_lshl_b32 s71, s71, 7\n"
      "s_add_i32 s89, s89, s71\n"
      "s_bfe_u32 s71, s57, 0x20003\n"
      "s_lshl_b32 s71, s71, 5\n"
      "s_add_i32 s89, s89, s71\n"
      "s_add_i32 s89, s89, 0x60000\n"
      "v_and_b32 v253, 3, v1\n"
      "v_lshlrev_b32 v12, 0, v253\n"
      "v_and_b32 v13, 2, v2\n"
      "v_sub_u32 v13, v12, v13\n"
      "v_cmp_eq_u32 vcc, 0, v13\n"
      "s_nop 1\n"
      "v_cndmask_b32 v12, 0, -1, vcc\n"
      "v_cmp_eq_u32 vcc, 1, v13\n"
      "s_nop 1\n"
      "v_cndmask_b32 v13, 0, -1, vcc\n"
      "v_add_u32 v14, v3, v252\n"
      "s_lshl_b32 s66, s49, 10\n"
      "v_add_u32 v15, s66, v14\n"
      "v_lshrrev_b32_e64 v253, 2, v1\n"
      "v_lshl_add_u32 v167, v253, 6, v252\n"
      "v_lshl_add_u32 v167, v2, 4, v167\n"
      "v_and_b32 v253, 1, v2\n"
      "v_lshlrev_b32 v171, 16, v253\n"
      "v_lshrrev_b32_e64 v253, 1, v2\n"
      "v_lshl_add_u32 v171, v253, 13, v171\n"
      "v_lshl_add_u32 v171, v1, 3, v171\n"
      "s_bfe_u32 s71, s52, 0x10004\n"
      "s_lshl_b32 s93, s71, 13\n"
      "s_bfe_u32 s71, s52, 0x30001\n"
      "s_lshl_b32 s71, s71, 9\n"
      "s_add_i32 s93, s93, s71\n"
      "s_add_i32 s93, s93, 0x468000\n"
      "s_lshl_b32 s92, s52, 14\n"
      "s_lshl_b32 s71, s49, 12\n"
      "s_add_i32 s92, s92, s71\n"
      "s_add_i32 s92, s92, 0x140000\n"
      "s_bfe_u32 s71, s52, 0x20003\n"
      "s_lshl_b32 s94, s71, 14\n"
      "s_bfe_u32 s71, s52, 0x30000\n"
      "s_lshl_b32 s71, s71, 10\n"
      "s_add_i32 s94, s94, s71\n"
      "s_lshl_b32 s71, s53, 7\n"
      "s_add_i32 s94, s94, s71\n"
      "s_add_i32 s94, s94, 0x40000\n"
      "s_bfe_u32 s71, s56, 0x10000\n"
      "s_lshl_b32 s95, s71, 16\n"
      "s_lshl_b32 s71, s58, 15\n"
      "s_add_i32 s95, s95, s71\n"
      "s_bfe_u32 s71, s49, 0x10001\n"
      "s_lshl_b32 s71, s71, 14\n"
      "s_add_i32 s95, s95, s71\n"
      "s_bfe_u32 s71, s56, 0x10001\n"
      "s_lshl_b32 s71, s71, 13\n"
      "s_add_i32 s95, s95, s71\n"
      "s_bfe_u32 s71, s49, 0x10000\n"
      "s_lshl_b32 s71, s71, 12\n"
      "s_add_i32 s95, s95, s71\n"
      "s_add_i32 s95, s95, 0x40000\n"
      "s_bfe_u32 s71, s56, 0x10000\n"
      "s_lshl_b32 s96, s71, 17\n"
      "s_lshl_b32 s71, s60, 15\n"
      "s_add_i32 s96, s96, s71\n"
      "s_bfe_u32 s71, s49, 0x10001\n"
      "s_lshl_b32 s71, s71, 14\n"
      "s_add_i32 s96, s96, s71\n"
      "s_bfe_u32 s71, s56, 0x10001\n"
      "s_lshl_b32 s71, s71, 13\n"
      "s_add_i32 s96, s96, s71\n"
      "s_bfe_u32 s71, s49, 0x10000\n"
      "s_lshl_b32 s71, s71, 12\n"
      "s_add_i32 s96, s96, s71\n"
      "s_add_i32 s96, s96, 0xc0000\n"
      "s_bfe_u32 s71, s49, 0x10000\n"
      "s_lshl_b32 s97, s71, 17\n"
      "s_bfe_u32 s71, s60, 0x10001\n"
      "s_lshl_b32 s71, s71, 14\n"
      "s_add_i32 s97, s97, s71\n"
      "s_bfe_u32 s71, s49, 0x10001\n"
      "s_lshl_b32 s71, s71, 13\n"
      "s_add_i32 s97, s97, s71\n"
      "s_bfe_u32 s71, s60, 0x10000\n"
      "s_lshl_b32 s71, s71, 12\n"
      "s_add_i32 s97, s97, s71\n"
      "s_lshl_b32 s71, s59, 6\n"
      "s_add_i32 s97, s97, s71\n"
      "s_add_i32 s97, s97, 0x100000\n"
      "s_bfe_u32 s71, s54, 0x30000\n"
      "s_lshl_b32 s98, s71, 15\n"
      "s_bfe_u32 s71, s49, 0x10001\n"
      "s_lshl_b32 s71, s71, 14\n"
      "s_add_i32 s98, s98, s71\n"
      "s_bfe_u32 s71, s54, 0x10003\n"
      "s_lshl_b32 s71, s71, 13\n"
      "s_add_i32 s98, s98, s71\n"
      "s_bfe_u32 s71, s49, 0x10000\n"
      "s_lshl_b32 s71, s71, 12\n"
      "s_add_i32 s98, s98, s71\n"
      "s_add_i32 s98, s98, 0x100000\n"
      "s_waitcnt vmcnt(10)\n"
      "v_lshlrev_b32_e64 v216, 16, v208\n"
      "v_and_b32 v217, 0xffff0000, v208\n"
      "v_lshlrev_b32_e64 v218, 16, v209\n"
      "v_and_b32 v219, 0xffff0000, v209\n"
      "v_lshlrev_b32_e64 v220, 16, v210\n"
      "v_and_b32 v221, 0xffff0000, v210\n"
      "v_lshlrev_b32_e64 v222, 16, v211\n"
      "v_and_b32 v223, 0xffff0000, v211\n"
      "v_pk_add_f32 v[208:209], v[216:217], v[218:219]\n"
      "v_pk_add_f32 v[210:211], v[220:221], v[222:223]\n"
      "v_pk_add_f32 v[208:209], v[208:209], v[210:211]\n"
      "v_add_f32 v232, v208, v209\n"
      "s_nop 1\n"
      "v_add_f32_dpp v232, v232, v232 quad_perm:[1,0,3,2] row_mask:0xf bank_mask:0xf\n"
      "s_nop 1\n"
      "v_add_f32_dpp v232, v232, v232 quad_perm:[2,3,0,1] row_mask:0xf bank_mask:0xf\n"
      "s_nop 1\n"
      "v_add_f32_dpp v232, v232, v232 row_half_mirror row_mask:0xf bank_mask:0xf\n"
      "s_nop 1\n"
      "v_add_f32_dpp v232, v232, v232 row_mirror row_mask:0xf bank_mask:0xf\n"
      "v_mov_b32 v233, v232\n"
      "s_nop 1\n"
      "v_permlane16_swap_b32 v232, v233\n"
      "v_add_f32 v232, v232, v233\n"
      "v_mov_b32 v233, v232\n"
      "s_nop 1\n"
      "v_permlane32_swap_b32 v232, v233\n"
      "v_add_f32_e64 v232, v232, v233\n"
      "v_mul_f32 v232, 0x3b000000, v232\n"
      "v_pk_add_f32 v[216:217], v[216:217], v[232:233] op_sel_hi:[1,0] neg_lo:[0,1] neg_hi:[0,1]\n"
      "v_pk_add_f32 v[218:219], v[218:219], v[232:233] op_sel_hi:[1,0] neg_lo:[0,1] neg_hi:[0,1]\n"
      "v_pk_add_f32 v[220:221], v[220:221], v[232:233] op_sel_hi:[1,0] neg_lo:[0,1] neg_hi:[0,1]\n"
      "v_pk_add_f32 v[222:223], v[222:223], v[232:233] op_sel_hi:[1,0] neg_lo:[0,1] neg_hi:[0,1]\n"
      "v_pk_mul_f32 v[208:209], v[216:217], v[216:217]\n"
      "v_pk_fma_f32 v[208:209], v[218:219], v[218:219], v[208:209]\n"
      "v_pk_fma_f32 v[208:209], v[220:221], v[220:221], v[208:209]\n"
      "v_pk_fma_f32 v[208:209], v[222:223], v[222:223], v[208:209]\n"
      "v_add_f32 v232, v208, v209\n"
      "s_nop 1\n"
      "v_add_f32_dpp v232, v232, v232 quad_perm:[1,0,3,2] row_mask:0xf bank_mask:0xf\n"
      "s_nop 1\n"
      "v_add_f32_dpp v232, v232, v232 quad_perm:[2,3,0,1] row_mask:0xf bank_mask:0xf\n"
      "s_nop 1\n"
      "v_add_f32_dpp v232, v232, v232 row_half_mirror row_mask:0xf bank_mask:0xf\n"
      "s_nop 1\n"
      "v_add_f32_dpp v232, v232, v232 row_mirror row_mask:0xf bank_mask:0xf\n"
      "v_mov_b32 v233, v232\n"
      "s_nop 1\n"
      "v_permlane16_swap_b32 v232, v233\n"
      "v_add_f32_e64 v232, v232, v233\n"
      "v_mov_b32 v233, v232\n"
      "s_nop 1\n"
      "v_permlane32_swap_b32_e64 v232, v233\n"
      "v_add_f32_e64 v232, v232, v233\n"
      "v_mul_f32 v232, 0x3b000000, v232\n"
      "v_add_f32 v232, 0x3727c5ac, v232\n"
      "v_rsq_f32 v232, v232\n"
      "s_nop 0\n"
      "v_pk_mul_f32 v[216:217], v[216:217], v[232:233] op_sel_hi:[1,0]\n"
      "v_cvt_pk_bf16_f32 v208, v216, v217\n"
      "v_pk_mul_f32 v[218:219], v[218:219], v[232:233] op_sel_hi:[1,0]\n"
      "v_cvt_pk_bf16_f32 v209, v218, v219\n"
      "v_pk_mul_f32 v[220:221], v[220:221], v[232:233] op_sel_hi:[1,0]\n"
      "v_cvt_pk_bf16_f32 v210, v220, v221\n"
      "v_pk_mul_f32 v[222:223], v[222:223], v[232:233] op_sel_hi:[1,0]\n"
      "v_cvt_pk_bf16_f32 v211, v222, v223\n"
      "s_lshl_b32 s66, s49, 6\n"
      "v_and_b32 v253, 3, v0\n"
      "v_lshl_add_u32 v5, v253, 4, v252\n"
      "v_add_u32 v5, s66, v5\n"
      "s_lshl_b32 s66, s54, 2\n"
      "s_lshl_b64 exec, 15, s66\n"
      "ds_write_b128 v5, v[208:211] offset:22016\n"
      "s_mov_b64 exec, 0x1\n"
      "ds_add_u32 v252, v254 offset:24352\n"
      "s_mov_b64 exec, -1\n"
      "s_lshl_b32 s70, s52, 11\n"
      "s_bfe_u32 s71, s49, 0x10001\n"
      "s_lshl_b32 s71, s71, 10\n"
      "s_add_i32 s70, s70, s71\n"
      "s_add_i32 s70, s70, 0x10000\n"
      "s_mul_i32 s69, s51, 4352\n"
      "s_lshl_b32 s71, s50, 3\n"
      "s_add_i32 s69, s69, s71\n"
      "s_add_i32 s69, s69, 0x1c0000\n"
      ".Lcw_2:\n"
      "ds_read_b32 v232, v252 offset:24352\n"
      "ds_read_b128 v[224:227], v167 offset:22016\n"
      "s_waitcnt lgkmcnt(0)\n"
      "v_cmp_gt_i32 vcc, 4, v232\n"
      "s_cbranch_vccnz .Lcw_2\n"
      "s_add_i32 s62, s63, 150\n"
      "s_waitcnt vmcnt(2)\n"
      "v_perm_b32 v16, v146, v144, s72\n"
      "v_perm_b32 v17, v150, v148, s72\n"
      "v_perm_b32 v18, v154, v152, s72\n"
      "v_perm_b32 v19, v158, v156, s72\n"
      "v_perm_b32 v20, v146, v144, s73\n"
      "v_perm_b32 v21, v150, v148, s73\n"
      "v_perm_b32 v22, v154, v152, s73\n"
      "v_perm_b32 v23, v158, v156, s73\n"
      "v_perm_b32 v24, v147, v145, s72\n"
      "v_perm_b32 v25, v151, v149, s72\n"
      "v_perm_b32 v26, v155, v153, s72\n"
      "v_perm_b32 v27, v159, v157, s72\n"
      "v_perm_b32 v28, v147, v145, s73\n"
      "v_perm_b32 v29, v151, v149, s73\n"
      "v_perm_b32 v30, v155, v153, s73\n"
      "v_perm_b32 v31, v159, v157, s73\n"
      "v_mfma_f32_16x16x32_bf16 v[208:211], v[224:227], v[16:19], 0\n"
      "v_mfma_f32_16x16x32_bf16 v[212:215], v[224:227], v[20:23], 0\n"
      "v_mfma_f32_16x16x32_bf16 v[216:219], v[224:227], v[24:27], 0\n"
      "v_mfma_f32_16x16x32_bf16 v[220:223], v[224:227], v[28:31], 0\n"
      "s_nop 5\n"
      "v_cvt_pk_bf16_f32 v228, v208, v212\n"
      "s_nop 0\n"
      "v_cvt_pk_bf16_f32 v229, v216, v220\n"
      ".Lwe_3:\n"
      "s_memrealtime s[64:65]\n"
      "v_mov_b32 v253, v254\n"
      "v_mov_b32 v253, v254\n"
      "v_mov_b32 v253, v254\n"
      "v_mov_b32 v253, v254\n"
      "v_mov_b32 v253, v254\n"
      "v_mov_b32 v253, v254\n"
      "v_mov_b32 v253, v254\n"
      "v_mov_b32 v253, v254\n"
      "s_waitcnt lgkmcnt(0)\n"
      "s_sub_i32 s65, s64, s62\n"
      "s_cmp_lt_i32 s65, 0\n"
      "s_cbranch_scc1 .Lwe_3\n"
      "buffer_load_dwordx2 v[232:233], v3, s[4:7], s70 offen sc1\n"
      "buffer_store_dwordx2 v[228:229], v168, s[4:7], s90 offen sc1\n"
      "s_waitcnt vmcnt(3)\n"
      "s_mov_b64 exec, 0x1\n"
      "ds_add_rtn_u32 v234, v252, v254 offset:24368\n"
      "s_mov_b64 exec, -1\n"
      "ds_read_b32 v235, v252 offset:24424\n"
      "s_waitcnt lgkmcnt(0)\n"
      "v_ashrrev_i32_e64 v235, 4, v235\n"
      "v_max_i32 v235, 0xffffff81, v235\n"
      "v_min_i32 v235, 0x7f, v235\n"
      "v_and_b32 v235, 0xff, v235\n"
      "v_lshl_or_b32 v231, v235, 12, v231\n"
      "v_readfirstlane_b32 s66, v234\n"
      "s_cmp_lg_u32 s66, 3\n"
      "s_cbranch_scc1 .Lnb_5\n"
      "s_mov_b64 exec, 0x1\n"
      "buffer_store_dwordx2 v[230:231], off, s[4:7], s69 sc1\n"
      "s_mov_b64 exec, -1\n"
      "s_waitcnt vmcnt(2)\n"
      "s_branch .Lck_6\n"
      ".Lnb_5:\n"
      "s_waitcnt vmcnt(1)\n"
      "s_branch .Lck_6\n"
      ".Lwe_4:\n"
      "s_memrealtime s[64:65]\n"
      "v_mov_b32 v253, v254\n"
      "v_mov_b32 v253, v254\n"
      "v_mov_b32 v253, v254\n"
      "v_mov_b32 v253, v254\n"
      "v_mov_b32 v253, v254\n"
      "v_mov_b32 v253, v254\n"
      "v_mov_b32 v253, v254\n"
      "v_mov_b32 v253, v254\n"
      "s_waitcnt lgkmcnt(0)\n"
      "s_sub_i32 s65, s64, s62\n"
      "s_cmp_lt_i32 s65, 0\n"
      "s_cbranch_scc1 .Lwe_4\n"
      "buffer_load_dwordx2 v[232:233], v3, s[4:7], s70 offen sc1\n"
      "s_waitcnt vmcnt(0)\n"
      ".Lck_6:\n"
      "s_memrealtime s[64:65]\n"
      "v_xor_b32 v232, s80, v232\n"
      "v_xor_b32 v235, s81, v233\n"
      "v_lshrrev_b32 v235, 20, v235\n"
      "v_or_b32 v234, v232, v235\n"
      "s_waitcnt lgkmcnt(0)\n"
      "v_cmp_ne_u32 vcc, 0, v234\n"
      "s_add_i32 s62, s64, 20\n"
      "s_cbranch_vccnz .Lsl_7\n"
      ".Lok_8:\n"
      "v_sub_u32_e64 v233, s64, v233\n"
      "v_and_b32 v233, 0xfff, v233\n"
      "s_nop 1\n"
      "v_min_u32_dpp v233, v233, v233 quad_perm:[1,0,3,2] row_mask:0xf bank_mask:0xf\n"
      "s_nop 1\n"
      "v_min_u32_dpp v233, v233, v233 quad_perm:[2,3,0,1] row_mask:0xf bank_mask:0xf\n"
      "s_nop 1\n"
      "v_min_u32_dpp v233, v233, v233 row_half_mirror row_mask:0xf bank_mask:0xf\n"
      "s_nop 1\n"
      "v_min_u32_dpp v233, v233, v233 row_mirror row_mask:0xf bank_mask:0xf\n"
      "v_mov_b32 v234, v233\n"
      "s_nop 1\n"
      "v_permlane16_swap_b32 v233, v234\n"
      "v_min_u32 v233, v233, v234\n"
      "v_mov_b32 v234, v233\n"
      "s_nop 1\n"
      "v_permlane32_swap_b32 v233, v234\n"
      "v_min_u32 v233, v233, v234\n"
      "v_sub_u32 v233, s64, v233\n"
      "s_nop 0\n"
      "v_readfirstlane_b32 s61, v233\n"
      ".Lcw_9:\n"
      "ds_read_b32 v232, v252 offset:24388\n"
      "v_mov_b32 v253, v254\n"
      "v_mov_b32 v253, v254\n"
      "v_mov_b32 v253, v254\n"
      "v_mov_b32 v253, v254\n"
      "v_mov_b32 v253, v254\n"
      "v_mov_b32 v253, v254\n"
      "v_mov_b32 v253, v254\n"
      "v_mov_b32 v253, v254\n"
      "s_waitcnt lgkmcnt(0)\n"
      "v_cmp_gt_i32 vcc, 4, v232\n"
      "v_mov_b32 v253, v254\n"
      "v_mov_b32 v253, v254\n"
      "v_mov_b32 v253, v254\n"
      "v_mov_b32 v253, v254\n"
      "s_cbranch_vccnz .Lcw_9\n"
      "s_lshl_b32 s66, s49, 2\n"
      "v_and_b32 v5, 2, v2\n"
      "v_add_u32 v5, s66, v5\n"
      "v_xor_b32 v5, v5, v1\n"
      "v_xor_b32 v6, 1, v5\n"
      "v_lshl_add_u32 v7, v1, 10, v252\n"
      "v_and_b32 v253, 1, v2\n"
      "v_lshl_add_u32 v7, v253, 3, v7\n"
      "v_lshl_add_u32 v5, v5, 4, v7\n"
      "v_lshl_add_u32 v6, v6, 4, v7\n"
      "ds_read_b64 v[32:33], v5\n"
      "ds_read_b64 v[34:35], v6\n"
      "ds_read_b64 v[36:37], v5 offset:256\n"
      "ds_read_b64 v[38:39], v6 offset:256\n"
      "ds_read_b64 v[40:41], v5 offset:512\n"
      "ds_read_b64 v[42:43], v6 offset:512\n"
      "ds_read_b64 v[44:45], v5 offset:768\n"
      "ds_read_b64 v[46:47], v6 offset:768\n"
      "v_lshrrev_b32 v253, 3, v0\n"
      "v_lshlrev_b32 v6, 12, v253\n"
      "v_lshrrev_b32 v253, 3, v0\n"
      "v_lshl_add_u32 v6, v253, 8, v6\n"
      "v_and_b32 v253, 7, v0\n"
      "v_lshl_add_u32 v6, v253, 3, v6\n"
      "s_mul_i32 s70, s49, 34816\n"
      "s_add_i32 s70, s70, 0x1c0000\n"
      "v_mov_b32 v225, 0x3e0293ee\n"
      "s_mov_b32 s99, 305\n"
      ".Lwe_10:\n"
      "s_memrealtime s[64:65]\n"
      "v_mov_b32 v253, v254\n"
      "v_mov_b32 v253, v254\n"
      "v_mov_b32 v253, v254\n"
      "v_mov_b32 v253, v254\n"
      "v_mov_b32 v253, v254\n"
      "v_mov_b32 v253, v254\n"
      "v_mov_b32 v253, v254\n"
      "v_mov_b32 v253, v254\n"
      "s_waitcnt lgkmcnt(0)\n"
      "s_sub_i32 s64, s64, s99\n"
      "s_sub_i32 s65, s61, s64\n"
      "s_sub_i32 s65, s3, s65\n"
      "s_max_i32 s65, s65, 0\n"
      "s_add_i32 s99, s99, s65\n"
      "s_add_i32 s62, s61, s99\n"
      ".Lwf_13:\n"
      "s_memrealtime s[64:65]\n"
      "v_mov_b32 v253, v254\n"
      "v_mov_b32 v253, v254\n"
      "v_mov_b32 v253, v254\n"
      "v_mov_b32 v253, v254\n"
      "v_mov_b32 v253, v254\n"
      "v_mov_b32 v253, v254\n"
      "v_mov_b32 v253, v254\n"
      "v_mov_b32 v253, v254\n"
      "s_waitcnt lgkmcnt(0)\n"
      "s_sub_i32 s65, s62, s64\n"
      "s_cmp_gt_i32 s65, 3\n"
      "s_cbranch_scc1 .Lwf_13\n"
      "s_max_i32 s65, s65, 0\n"
      ".Lwd_14:\n"
      "s_sub_u32 s65, s65, 1\n"
      "s_cbranch_scc0 .Lwd_14\n"
      "buffer_load_dwordx2 v[236:237], v6, s[4:7], s70 offen sc1\n"
      "v_mov_b32 v253, v254\n"
      "v_mov_b32 v253, v254\n"
      "v_mov_b32 v253, v254\n"
      "v_mov_b32 v253, v254\n"
      "v_mov_b32 v253, v254\n"
      "v_mov_b32 v253, v254\n"
      "v_mov_b32 v253, v254\n"
      "v_mov_b32 v253, v254\n"
      "v_mov_b32 v253, v254\n"
      "v_mov_b32 v253, v254\n"
      "v_mov_b32 v253, v254\n"
      "v_mov_b32 v253, v254\n"
      "buffer_load_dwordx4 v[144:147], v3, s[4:7], s92 offen sc1\n"
      "buffer_load_dwordx4 v[148:151], v3, s[4:7], s92 offen offset:1024 sc1\n"
      "buffer_load_dwordx4 v[152:155], v3, s[4:7], s92 offen offset:2048 sc1\n"
      "buffer_load_dwordx4 v[156:159], v3, s[4:7], s92 offen offset:3072 sc1\n"
      "s_mul_i32 s66, s49, 4160\n"
      "v_add_u32 v7, s66, v14\n"
      "s_waitcnt vmcnt(4)\n"
      "s_memrealtime s[64:65]\n"
      "v_xor_b32 v236, s80, v236\n"
      "v_xor_b32 v239, s81, v237\n"
      "v_lshrrev_b32 v239, 20, v239\n"
      "v_or_b32 v238, v236, v239\n"
      "s_waitcnt lgkmcnt(0)\n"
      "v_cmp_ne_u32_e64 vcc, 0, v238\n"
      "s_nop 1\n"
      "v_cndmask_b32 v244, 0, -1, vcc\n"
      "v_bfe_i32 v236, v237, 12, 8\n"
      "s_nop 1\n"
      "v_add_u32_dpp v236, v236, v236 quad_perm:[1,0,3,2] row_mask:0xf bank_mask:0xf\n"
      "s_nop 1\n"
      "v_add_u32_dpp v236, v236, v236 quad_perm:[2,3,0,1] row_mask:0xf bank_mask:0xf\n"
      "s_nop 1\n"
      "v_add_u32_dpp v236, v236, v236 row_half_mirror row_mask:0xf bank_mask:0xf\n"
      "s_nop 1\n"
      "v_add_u32_dpp v236, v236, v236 row_mirror row_mask:0xf bank_mask:0xf\n"
      "v_mov_b32 v238, v236\n"
      "s_nop 1\n"
      "v_permlane16_swap_b32 v236, v238\n"
      "v_add_u32 v236, v236, v238\n"
      "v_mov_b32 v238, v236\n"
      "s_nop 1\n"
      "v_permlane32_swap_b32 v236, v238\n"
      "v_add_u32 v236, v236, v238\n"
      "v_sub_u32_e64 v237, s64, v237\n"
      "v_and_b32 v237, 0xfff, v237\n"
      "s_nop 1\n"
      "v_min_u32_dpp v237, v237, v237 quad_perm:[1,0,3,2] row_mask:0xf bank_mask:0xf\n"
      "s_nop 1\n"
      "v_min_u32_dpp v237, v237, v237 quad_perm:[2,3,0,1] row_mask:0xf bank_mask:0xf\n"
      "s_nop 1\n"
      "v_min_u32_dpp v237, v237, v237 row_half_mirror row_mask:0xf bank_mask:0xf\n"
      "s_nop 1\n"
      "v_min_u32_dpp v237, v237, v237 row_mirror row_mask:0xf bank_mask:0xf\n"
      "v_mov_b32 v238, v237\n"
      "s_nop 1\n"
      "v_permlane16_swap_b32 v237, v238\n"
      "v_min_u32 v237, v237, v238\n"
      "v_mov_b32 v238, v237\n"
      "s_nop 1\n"
      "v_permlane32_swap_b32 v237, v238\n"
      "v_min_u32 v237, v237, v238\n"
      "v_sub_u32 v237, s64, v237\n"
      "s_waitcnt vmcnt(3)\n"
      "v_mfma_f32_16x16x32_bf16 v[228:231], v[8:11], v[144:147], 0\n"
      "v_max3_u32 v244, v244, v144, v145\n"
      "v_max3_u32 v244, v244, v146, v147\n"
      "s_waitcnt vmcnt(2)\n"
      "v_mfma_f32_16x16x32_bf16 v[228:231], v[8:11], v[148:151], v[228:231]\n"
      "v_max3_u32 v244, v244, v148, v149\n"
      "v_max3_u32 v244, v244, v150, v151\n"
      "s_waitcnt vmcnt(1)\n"
      "v_mfma_f32_16x16x32_bf16 v[228:231], v[8:11], v[152:155], v[228:231]\n"
      "v_max3_u32 v244, v244, v152, v153\n"
      "v_max3_u32 v244, v244, v154, v155\n"
      "s_waitcnt vmcnt(0)\n"
      "v_mfma_f32_16x16x32_bf16 v[228:231], v[8:11], v[156:159], v[228:231]\n"
      "v_max3_u32 v244, v244, v156, v157\n"
      "v_max3_u32 v244, v244, v158, v159\n"
      "v_cmp_eq_u32_e64 vcc, -1, v244\n"
      "s_nop 4\n"
      "v_pk_mul_f32 v[228:229], v[228:229], v[224:225] op_sel:[0,1] op_sel_hi:[1,1]\n"
      "v_pk_mul_f32 v[230:231], v[230:231], v[224:225] op_sel:[0,1] op_sel_hi:[1,1]\n"
      "v_cvt_pk_bf16_f32 v234, v228, v229\n"
      "v_cvt_pk_bf16_f32 v235, v230, v231\n"
      "v_and_b32 v232, v12, v234\n"
      "v_and_b32 v233, v12, v235\n"
      "v_and_b32 v234, v13, v234\n"
      "v_and_b32_e64 v235, v13, v235\n"
      "s_nop 1\n"
      "v_mfma_f32_16x16x32_bf16 v[208:211], v[232:235], v[32:35], 0\n"
      "v_mfma_f32_16x16x32_bf16 v[212:215], v[232:235], v[36:39], 0\n"
      "v_mfma_f32_16x16x32_bf16 v[216:219], v[232:235], v[40:43], 0\n"
      "v_mfma_f32_16x16x32_bf16 v[220:223], v[232:235], v[44:47], 0\n"
      "s_cbranch_vccnz .Lsl_15\n"
      ".Lok_16:\n"
      "s_mov_b64 exec, 0x1\n"
      "ds_max_u32 v252, v237 offset:24408\n"
      "ds_add_u32 v252, v236 offset:24428\n"
      "s_mov_b64 exec, -1\n"
      "v_pk_add_f32 v[208:209], v[208:209], v[210:211]\n"
      "v_add_f32 v228, v208, v209\n"
      "v_pk_add_f32 v[212:213], v[212:213], v[214:215]\n"
      "v_add_f32 v229, v212, v213\n"
      "v_pk_add_f32 v[216:217], v[216:217], v[218:219]\n"
      "v_add_f32 v230, v216, v217\n"
      "v_pk_add_f32 v[220:221], v[220:221], v[222:223]\n"
      "v_add_f32_e64 v231, v220, v221\n"
      "ds_write_b128 v15, v[228:231] offset:24448\n"
      "s_mov_b64 exec, 0x1\n"
      "ds_add_rtn_u32 v232, v252, v254 offset:24356\n"
      "s_mov_b64 exec, -1\n"
      "ds_read_b32 v233, v252 offset:24428\n"
      "ds_read_b128 v[208:211], v14 offset:24448\n"
      "ds_read_b128 v[212:215], v14 offset:25472\n"
      "ds_read_b128 v[216:219], v14 offset:26496\n"
      "ds_read_b128 v[220:223], v14 offset:27520\n"
      "s_waitcnt lgkmcnt(4)\n"
      "v_readfirstlane_b32 s66, v232\n"
      "v_readfirstlane_b32 s75, v233\n"
      "s_cmp_gt_i32 s75, 0\n"
      "s_cselect_b32 s75, 0x2000, 0\n"
      "v_xor_b32_e64 v171, s75, v171\n"
      "s_waitcnt lgkmcnt(2)\n"
      "v_pk_add_f32 v[208:209], v[208:209], v[212:213]\n"
      "v_pk_add_f32 v[210:211], v[210:211], v[214:215]\n"
      "s_waitcnt lgkmcnt(0)\n"
      "v_pk_add_f32 v[216:217], v[216:217], v[220:221]\n"
      "v_pk_add_f32 v[218:219], v[218:219], v[222:223]\n"
      "v_pk_add_f32 v[208:209], v[208:209], v[216:217]\n"
      "v_pk_add_f32 v[210:211], v[210:211], v[218:219]\n"
      "s_cmp_lg_u32 s66, 3\n"
      "v_cvt_pk_bf16_f32 v208, v208, v209\n"
      "v_cvt_pk_bf16_f32 v209, v210, v211\n"
      "s_cbranch_scc1 .Lq2d_17\n"
      "buffer_store_dwordx2 v[208:209], v171, s[4:7], s94 offen sc1\n"
      ".Lq2d_17:\n"
      ".Lcw_18:\n"
      "ds_read_b32 v232, v252 offset:24356\n"
      "ds_read_b32 v233, v252 offset:24408\n"
      "ds_read_b32 v234, v252 offset:24428\n"
      "s_waitcnt lgkmcnt(0)\n"
      "v_cmp_gt_i32 vcc, 4, v232\n"
      "v_readfirstlane_b32 s61, v233\n"
      "s_cbranch_vccnz .Lcw_18\n"
      "v_readfirstlane_b32 s75, v234\n"
      "s_cmp_gt_i32 s75, 0\n"
      "s_cselect_b32 s75, 0x2000, 0\n"
      "s_xor_b32 s93, s93, s75\n"
      "s_xor_b32 s95, s95, s75\n"
      "s_xor_b32 s96, s96, s75\n"
      "s_xor_b32 s97, s97, s75\n"
      "s_xor_b32 s98, s98, s75\n"
      "s_xor_b32 s89, s89, s75\n"
      "s_or_b32 s66, s2, s48\n"
      "s_cbranch_scc1 .Lep_19\n"
      "s_add_u32 s66, s82, 1\n"
      "s_addc_u32 s67, s83, 0\n"
      "v_mov_b32 v234, s66\n"
      "v_mov_b32 v235, s67\n"
      "s_mov_b64 exec, 0x1\n"
      "buffer_store_dwordx2 v[234:235], off, s[4:7], 0 sc1\n"
      "s_mov_b64 exec, -1\n"
      ".Lep_19:\n"
      "v_mov_b32 v224, 0x3f803f80\n"
      "v_mov_b32 v225, 0x3f803f80\n"
      "v_mov_b32 v226, 0x3f803f80\n"
      "v_mov_b32 v227, 0x3f803f80\n"
      "s_nop 1\n"
      "v_mfma_f32_16x16x32_bf16 v[208:211], v[224:227], v[16:19], 0\n"
      "v_mfma_f32_16x16x32_bf16 v[212:215], v[224:227], v[20:23], 0\n"
      "v_mfma_f32_16x16x32_bf16 v[216:219], v[224:227], v[24:27], 0\n"
      "v_mfma_f32_16x16x32_bf16 v[220:223], v[224:227], v[28:31], 0\n"
      "v_lshrrev_b32 v253, 3, v1\n"
      "v_lshlrev_b32 v6, 10, v253\n"
      "v_and_b32 v253, 7, v1\n"
      "v_lshl_add_u32 v6, v253, 3, v6\n"
      "s_lshl_b32 s69, s55, 13\n"
      "s_lshl_b32 s71, s49, 11\n"
      "s_add_i32 s69, s69, s71\n"
      "s_lshl_b32 s71, s54, 6\n"
      "s_add_i32 s69, s69, s71\n"
      "s_add_i32 s69, s69, 0x20000\n"
      "v_cvt_pk_bf16_f32 v232, v208, v212\n"
      "v_cvt_pk_bf16_f32 v233, v216, v220\n"
      "s_mov_b64 exec, 0xffff\n"
      "buffer_store_dwordx2 v[232:233], v6, s[4:7], s69 offen sc1\n"
      "s_mov_b64 exec, -1\n"
      "s_mov_b32 s99, 470\n"
      ".Lwe_20:\n"
      "s_memrealtime s[64:65]\n"
      "v_mov_b32 v253, v254\n"
      "v_mov_b32 v253, v254\n"
      "v_mov_b32 v253, v254\n"
      "v_mov_b32 v253, v254\n"
      "v_mov_b32 v253, v254\n"
      "v_mov_b32 v253, v254\n"
      "v_mov_b32_e64 v253, v254\n"
      "v_mov_b32 v253, v254\n"
      "s_waitcnt lgkmcnt(0)\n"
      "s_sub_i32 s64, s64, s99\n"
      "s_sub_i32 s65, s61, s64\n"
      "s_sub_i32 s65, s3, s65\n"
      "s_max_i32 s65, s65, 0\n"
      "s_add_i32 s99, s99, s65\n"
      "s_add_i32 s62, s61, s99\n"
      ".Lwf_23:\n"
      "s_memrealtime s[64:65]\n"
      "v_mov_b32 v253, v254\n"
      "v_mov_b32 v253, v254\n"
      "v_mov_b32 v253, v254\n"
      "v_mov_b32 v253, v254\n"
      "v_mov_b32 v253, v254\n"
      "v_mov_b32 v253, v254\n"
      "v_mov_b32 v253, v254\n"
      "v_mov_b32_e64 v253, v254\n"
      "s_waitcnt lgkmcnt(0)\n"
      "s_sub_i32 s65, s62, s64\n"
      "s_cmp_gt_i32 s65, 3\n"
      "s_cbranch_scc1 .Lwf_23\n"
      "s_max_i32 s65, s65, 0\n"
      ".Lwd_24:\n"
      "s_sub_u32 s65, s65, 1\n"
      "s_cbranch_scc0 .Lwd_24\n"
      "buffer_load_dwordx4 v[208:211], v3, s[4:7], s95 offen sc1\n"
      "buffer_load_dwordx4 v[212:215], v3, s[4:7], s95 offen offset:1024 sc1\n"
      "buffer_load_dwordx4 v[216:219], v3, s[4:7], s95 offen offset:2048 sc1\n"
      "buffer_load_dwordx4 v[220:223], v3, s[4:7], s95 offen offset:3072 sc1\n"
      "v_lshrrev_b32 v253, 2, v1\n"
      "v_lshlrev_b32 v6, 0, v253\n"
      "v_lshl_add_u32 v6, v2, 2, v6\n"
      "v_lshrrev_b32 v253, 3, v6\n"
      "v_lshlrev_b32 v5, 0, v253\n"
      "v_and_b32 v253, 7, v6\n"
      "v_lshl_add_u32 v5, v253, 1, v5\n"
      "v_mul_u32_u24 v5, 1040, v5\n"
      "s_lshl_b32 s66, s48, 7\n"
      "v_and_b32_e64 v253, 3, v1\n"
      "v_lshl_add_u32 v5, v253, 3, v5\n"
      "v_add_u32_e64 v5, s66, v5\n"
      "v_add_u32 v6, 66560, v5\n"
      "v_mov_b32_e64 v224, 0\n"
      "s_waitcnt vmcnt(3)\n"
      "ds_write_b128 v7, v[208:211]\n"
      "v_max3_u32 v224, v224, v208, v209\n"
      "v_max3_u32 v224, v224, v210, v211\n"
      "s_waitcnt vmcnt(2)\n"
      "ds_write_b128 v7, v[212:215] offset:1040\n"
      "v_max3_u32 v224, v224, v212, v213\n"
      "v_max3_u32 v224, v224, v214, v215\n"
      "s_waitcnt vmcnt(1)\n"
      "ds_write_b128 v7, v[216:219] offset:2080\n"
      "v_max3_u32 v224, v224, v216, v217\n"
      "v_max3_u32 v224, v224, v218, v219\n"
      "s_waitcnt vmcnt(0)\n"
      "ds_write_b128 v7, v[220:223] offset:3120\n"
      "v_max3_u32 v224, v224, v220, v221\n"
      "v_max3_u32 v224, v224, v222, v223\n"
      "v_cmp_eq_u32 vcc, -1, v224\n"
      "s_cbranch_vccnz .Lsl_25\n"
      ".Lok_26:\n"
      "s_waitcnt lgkmcnt(0)\n"
      "s_barrier\n"
      "s_setprio 0\n"
      "s_cmp_lg_u32 s48, 2\n"
      "s_cbranch_scc1 .Lpt_27\n"
      "s_memrealtime s[64:65]\n"
      "s_mul_i32 s66, s51, 1792\n"
      "s_lshl_b32 s71, s50, 2\n"
      "s_add_i32 s66, s66, s71\n"
      "s_add_i32 s66, s66, 0x70000\n"
      "s_waitcnt lgkmcnt(0)\n"
      "s_bitset0_b32 s64, 0\n"
      "v_mov_b32_e64 v208, s64\n"
      "s_mov_b64 exec, 0x1\n"
      "buffer_store_dword v208, off, s[4:7], s66 sc1\n"
      "s_mov_b64 exec, -1\n"
      ".Lpt_27:\n"
      "s_mov_b64 s[86:87], s[34:35]\n"
      "s_cmp_lt_u32 s49, 2\n"
      "s_cselect_b64 s[84:85], s[32:33], s[36:37]\n"
      "v_lshrrev_b32_e64 v246, 2, v0\n"
      "v_mul_u32_u24 v246, 3584, v246\n"
      "v_and_b32_e64 v253, 3, v0\n"
      "v_lshl_add_u32 v246, v253, 7, v246\n"
      "s_mul_i32 s67, s50, 448\n"
      "s_and_b32 s67, s67, 0xffffff80\n"
      "s_mul_i32 s66, s51, 57344\n"
      "s_bfe_u32 s71, s49, 0x10000\n"
      "s_mul_i32 s71, s71, 28672\n"
      "s_add_i32 s66, s66, s71\n"
      "s_add_i32 s66, s66, s67\n"
      "s_mov_b32 exec_lo, 0xffffffff\n"
      "s_mov_b32 exec_hi, 0x0\n"
      "buffer_load_dword v247, v246, s[84:87], s66 offen\n"
      "s_mov_b64 exec, -1\n"
      "s_cmp_lg_u32 s48, 3\n"
      "s_cbranch_scc1 .Lcw_28\n"
      "s_getpc_b64 s[66:67]\n"
      ".Lch_29:\n"
      "s_sub_u32 s66, s66, .Lch_29-.Lpc_here\n"
      "s_subb_u32 s67, s67, 0\n"
      "s_lshl_b32 s71, s51, 7\n"
      "v_lshl_add_u32 v244, v0, 12, s71\n"
      "s_mov_b32 s71, .Lpc_end-.Lpc_here\n"
      "v_cmp_gt_u32 vcc, s71, v244\n"
      "s_and_b64 exec, exec, vcc\n"
      "s_cbranch_execz .Lcw_28\n"
      "global_load_dword v4, v244, s[66:67]\n"
      ".Lcw_28:\n"
      "s_mov_b64 exec, -1\n"
      "v_cmp_gt_u32 vcc, 8, v1\n"
      "v_mov_b32 v248, s36\n"
      "v_mov_b32 v249, s37\n"
      "v_mov_b32 v250, s32\n"
      "v_mov_b32 v251, s33\n"
      "v_cndmask_b32 v248, v248, v250, vcc\n"
      "v_cndmask_b32_e64 v249, v249, v251, vcc\n"
      "v_mul_u32_u24 v250, 28672, v2\n"
      "s_mul_i32 s66, s49, 458752\n"
      "s_lshl_b32 s71, s59, 6\n"
      "s_add_i32 s66, s66, s71\n"
      "v_and_b32_e64 v253, 7, v1\n"
      "v_lshl_add_u32 v250, v253, 3, v250\n"
      "v_add_u32 v250, s66, v250\n"
      "v_mov_b32 v251, 0\n"
      "v_lshl_add_u64 v[248:249], v[248:249], 0, v[250:251]\n"
      "ds_read_b64_tr_b16 v[144:145], v5 offset:0\n"
      "ds_read_b64_tr_b16 v[148:149], v5 offset:32\n"
      "ds_read_b64_tr_b16 v[152:153], v5 offset:64\n"
      "ds_read_b64_tr_b16 v[156:157], v5 offset:96\n"
      "ds_read_b64_tr_b16 v[146:147], v5 offset:16640\n"
      "ds_read_b64_tr_b16 v[150:151], v5 offset:16672\n"
      "ds_read_b64_tr_b16 v[154:155], v5 offset:16704\n"
      "ds_read_b64_tr_b16 v[158:159], v5 offset:16736\n"
      "ds_read_b64_tr_b16 v[160:161], v5 offset:33280\n"
      "ds_read_b64_tr_b16 v[164:165], v5 offset:33312\n"
      "ds_read_b64_tr_b16 v[168:169], v5 offset:33344\n"
      "ds_read_b64_tr_b16 v[172:173], v5 offset:33376\n"
      "ds_read_b64_tr_b16 v[162:163], v5 offset:49920\n"
      "ds_read_b64_tr_b16 v[166:167], v5 offset:49952\n"
      "ds_read_b64_tr_b16 v[170:171], v5 offset:49984\n"
      "ds_read_b64_tr_b16 v[174:175], v5 offset:50016\n"
      "ds_read_b64_tr_b16 v[176:177], v6 offset:0\n"
      "ds_read_b64_tr_b16 v[180:181], v6 offset:32\n"
      "ds_read_b64_tr_b16 v[184:185], v6 offset:64\n"
      "ds_read_b64_tr_b16 v[188:189], v6 offset:96\n"
      "ds_read_b64_tr_b16 v[178:179], v6 offset:16640\n"
      "ds_read_b64_tr_b16 v[182:183], v6 offset:16672\n"
      "ds_read_b64_tr_b16 v[186:187], v6 offset:16704\n"
      "ds_read_b64_tr_b16 v[190:191], v6 offset:16736\n"
      "ds_read_b64_tr_b16 v[192:193], v6 offset:33280\n"
      "ds_read_b64_tr_b16 v[196:197], v6 offset:33312\n"
      "ds_read_b64_tr_b16 v[200:201], v6 offset:33344\n"
      "ds_read_b64_tr_b16 v[204:205], v6 offset:33376\n"
      "ds_read_b64_tr_b16 v[194:195], v6 offset:49920\n"
      "ds_read_b64_tr_b16 v[198:199], v6 offset:49952\n"
      "ds_read_b64_tr_b16 v[202:203], v6 offset:49984\n"
      "ds_read_b64_tr_b16 v[206:207], v6 offset:50016\n"
      "v_lshlrev_b32_e64 v7, 13, v2\n"
      "v_lshl_add_u32 v7, v1, 4, v7\n"
      "s_lshl_b32 s69, s53, 19\n"
      "s_lshl_b32 s71, s52, 8\n"
      "s_add_i32 s69, s69, s71\n"
      "s_lshl_b32 s71, s49, 17\n"
      "s_add_i32 s69, s69, s71\n"
      "s_lshl_b32 s66, s49, 12\n"
      "s_add_i32 m0, s66, 133120\n"
      ".Lcw_30:\n"
      "ds_read_b32 v208, v252 offset:24392\n"
      "v_mov_b32 v253, v254\n"
      "v_mov_b32 v253, v254\n"
      "v_mov_b32 v253, v254\n"
      "v_mov_b32 v253, v254\n"
      "v_mov_b32 v253, v254\n"
      "v_mov_b32 v253, v254\n"
      "v_mov_b32 v253, v254\n"
      "v_mov_b32 v253, v254\n"
      "s_waitcnt lgkmcnt(0)\n"
      "v_cmp_gt_i32 vcc, 4, v208\n"
      "v_mov_b32 v253, v254\n"
      "v_mov_b32 v253, v254\n"
      "v_mov_b32 v253, v254\n"
      "v_mov_b32 v253, v254\n"
      "s_cbranch_vccnz .Lcw_30\n"
      "buffer_load_dwordx4 v7, s[24:27], s69 offen lds\n"
      "s_addk_i32 m0, 1024\n"
      "s_add_i32 s69, s69, 0x8000\n"
      "buffer_load_dwordx4 v7, s[24:27], s69 offen lds\n"
      "s_addk_i32 m0, 1024\n"
      "s_add_i32 s69, s69, 0x8000\n"
      "buffer_load_dwordx4 v7, s[24:27], s69 offen lds\n"
      "s_addk_i32 m0, 1024\n"
      "s_add_i32 s69, s69, 0x8000\n"
      "buffer_load_dwordx4 v7, s[24:27], s69 offen lds\n"
      "v_lshl_add_u32 v245, v1, 4, v252\n"
      "s_lshl_b32 s66, s58, 13\n"
      "s_xor_b32 s66, s66, s75\n"
      "v_lshlrev_b32 v5, 20, v2\n"
      "v_lshrrev_b32 v253, 1, v2\n"
      "v_lshl_add_u32 v5, v253, 13, v5\n"
      "v_lshl_add_u32 v5, v1, 3, v5\n"
      "v_xor_b32 v5, s66, v5\n"
      "v_lshrrev_b32 v253, 3, v1\n"
      "v_lshlrev_b32 v6, 14, v253\n"
      "v_bfe_u32 v253, v1, 2, 1\n"
      "v_lshl_add_u32 v6, v253, 12, v6\n"
      "v_and_b32 v253, 3, v1\n"
      "v_lshl_add_u32 v6, v253, 3, v6\n"
      ".Lcw_31:\n"
      "ds_read_b32 v208, v252 offset:24372\n"
      "v_mov_b32 v253, v254\n"
      "v_mov_b32 v253, v254\n"
      "v_mov_b32 v253, v254\n"
      "v_mov_b32 v253, v254\n"
      "v_mov_b32 v253, v254\n"
      "v_mov_b32 v253, v254\n"
      "v_mov_b32 v253, v254\n"
      "v_mov_b32 v253, v254\n"
      "s_waitcnt lgkmcnt(0)\n"
      "v_cmp_gt_i32 vcc, 4, v208\n"
      "v_mov_b32 v253, v254\n"
      "v_mov_b32 v253, v254\n"
      "v_mov_b32_e64 v253, v254\n"
      "v_mov_b32 v253, v254\n"
      "s_cbranch_vccnz .Lcw_31\n"
      "s_setprio 2\n"
      "s_barrier\n"
      "ds_read_b128 v[208:211], v14 offset:16640\n"
      "ds_read_b128 v[212:215], v14 offset:17664\n"
      "ds_read_b128 v[216:219], v14 offset:18688\n"
      "ds_read_b128 v[220:223], v14 offset:19712\n"
      "ds_read_b128 v[240:243], v245 offset:20736\n"
      "s_waitcnt lgkmcnt(4)\n"
      "v_mfma_f32_16x16x32_bf16 v[224:227], v[208:211], v[144:147], 0\n"
      "v_mfma_f32_16x16x32_bf16 v[228:231], v[208:211], v[148:151], 0\n"
      "v_mfma_f32_16x16x32_bf16 v[232:235], v[208:211], v[152:155], 0\n"
      "v_mfma_f32_16x16x32_bf16 v[236:239], v[208:211], v[156:159], 0\n"
      "s_waitcnt lgkmcnt(3)\n"
      "v_mfma_f32_16x16x32_bf16 v[224:227], v[212:215], v[160:163], v[224:227]\n"
      "v_mfma_f32_16x16x32_bf16 v[228:231], v[212:215], v[164:167], v[228:231]\n"
      "v_mfma_f32_16x16x32_bf16 v[232:235], v[212:215], v[168:171], v[232:235]\n"
      "v_mfma_f32_16x16x32_bf16 v[236:239], v[212:215], v[172:175], v[236:239]\n"
      "s_waitcnt lgkmcnt(2)\n"
      "v_mfma_f32_16x16x32_bf16 v[224:227], v[216:219], v[176:179], v[224:227]\n"
      "v_mfma_f32_16x16x32_bf16 v[228:231], v[216:219], v[180:183], v[228:231]\n"
      "v_mfma_f32_16x16x32_bf16 v[232:235], v[216:219], v[184:187], v[232:235]\n"
      "v_mfma_f32_16x16x32_bf16 v[236:239], v[216:219], v[188:191], v[236:239]\n"
      "s_waitcnt lgkmcnt(1)\n"
      "v_mfma_f32_16x16x32_bf16 v[224:227], v[220:223], v[192:195], v[224:227]\n"
      "v_mfma_f32_16x16x32_bf16 v[228:231], v[220:223], v[196:199], v[228:231]\n"
      "v_mfma_f32_16x16x32_bf16 v[232:235], v[220:223], v[200:203], v[232:235]\n"
      "v_mfma_f32_16x16x32_bf16 v[236:239], v[220:223], v[204:207], v[236:239]\n"
      "s_add_i32 s67, s88, 262144\n"
      "s_add_i32 s68, s88, 524288\n"
      "s_add_i32 s69, s88, 786432\n"
      "v_mov_b32 v160, 0x3f803f80\n"
      "v_mov_b32 v161, 0x3f803f80\n"
      "v_mov_b32 v162, 0x3f803f80\n"
      "v_mov_b32 v163, 0x3f803f80\n"
      "v_cvt_pk_bf16_f32 v144, v224, v228\n"
      "v_cvt_pk_bf16_f32 v146, v225, v229\n"
      "v_cvt_pk_bf16_f32 v145, v232, v236\n"
      "buffer_store_dwordx2 v[144:145], v5, s[4:7], s88 offen sc1\n"
      "v_cvt_pk_bf16_f32 v147, v233, v237\n"
      "buffer_store_dwordx2 v[146:147], v5, s[4:7], s67 offen sc1\n"
      "v_cvt_pk_bf16_f32 v148, v226, v230\n"
      "v_cvt_pk_bf16_f32 v149, v234, v238\n"
      "buffer_store_dwordx2 v[148:149], v5, s[4:7], s68 offen sc1\n"
      "v_cvt_pk_bf16_f32 v150, v227, v231\n"
      "v_cvt_pk_bf16_f32 v151, v235, v239\n"
      "buffer_store_dwordx2 v[150:151], v5, s[4:7], s69 offen sc1\n"
      "s_setprio 0\n"
      "v_mfma_f32_16x16x32_bf16 v[228:231], v[160:163], v[208:211], 0\n"
      "v_mfma_f32_16x16x32_bf16 v[228:231], v[160:163], v[212:215], v[228:231]\n"
      "v_mfma_f32_16x16x32_bf16 v[228:231], v[160:163], v[216:219], v[228:231]\n"
      "v_mfma_f32_16x16x32_bf16 v[228:231], v[160:163], v[220:223], v[228:231]\n"
      "s_waitcnt lgkmcnt(0)\n"
      "v_max3_f32 v244, v240, v241, v242\n"
      "v_max_f32 v244, v244, v243\n"
      "s_nop 4\n"
      "v_mov_b32 v245, v228\n"
      "s_cmp_gt_u32 s51, 27\n"
      "s_cbranch_scc1 .Lpa_32\n"
      "v_lshlrev_b32_e64 v246, 10, v0\n"
      "s_lshl_b32 s66, s54, 18\n"
      "s_bfe_u32 s71, s55, 0x10000\n"
      "s_lshl_b32 s71, s71, 17\n"
      "s_add_i32 s66, s66, s71\n"
      "s_lshl_b32 s71, s49, 15\n"
      "s_add_i32 s66, s66, s71\n"
      "s_bfe_u32 s71, s55, 0x30001\n"
      "s_lshl_b32 s71, s71, 7\n"
      "s_add_i32 s66, s66, s71\n"
      "s_mov_b32 exec_lo, 0xffffffff\n"
      "s_mov_b32 exec_hi, 0x0\n"
      "buffer_load_dword v247, v246, s[28:31], s66 offen\n"
      "s_mov_b64 exec, -1\n"
      "s_waitcnt vmcnt(1)\n"
      "s_branch .Lpa_33\n"
      ".Lpa_32:\n"
      "v_lshlrev_b32_e64 v246, 10, v0\n"
      "s_lshl_b32 s66, s54, 18\n"
      "s_bfe_u32 s71, s55, 0x10000\n"
      "s_lshl_b32 s71, s71, 17\n"
      "s_add_i32 s66, s66, s71\n"
      "s_lshl_b32 s71, s49, 15\n"
      "s_add_i32 s66, s66, s71\n"
      "s_bfe_u32 s71, s55, 0x30001\n"
      "s_lshl_b32 s71, s71, 7\n"
      "s_add_i32 s66, s66, s71\n"
      "s_mov_b32 exec_lo, 0xffffffff\n"
      "s_mov_b32 exec_hi, 0x0\n"
      "buffer_load_dword v247, v246, s[28:31], s66 offen\n"
      "s_mov_b64 exec, -1\n"
      "s_waitcnt vmcnt(1)\n"
      ".Lpa_33:\n"
      "s_mov_b64 exec, 0x1\n"
      "ds_add_rtn_u32 v240, v252, v254 offset:24404\n"
      "s_mov_b64 exec, -1\n"
      "s_waitcnt lgkmcnt(0)\n"
      "v_readfirstlane_b32 s66, v240\n"
      "s_cmp_lg_u32 s66, 7\n"
      "s_cbranch_scc1 .Lml_34\n"
      "s_mov_b64 exec, 0xffff\n"
      "buffer_store_dwordx2 v[244:245], v6, s[4:7], s89 offen sc1\n"
      "s_mov_b64 exec, -1\n"
      ".Lml_34:\n"
      "s_setprio 0\n"
      "v_lshlrev_b32_e64 v6, 13, v2\n"
      "v_lshl_add_u32 v6, v1, 4, v6\n"
      "v_lshrrev_b32 v253, 2, v0\n"
      "v_lshlrev_b32 v7, 10, v253\n"
      "v_and_b32 v253, 3, v0\n"
      "v_lshl_add_u32 v7, v253, 4, v7\n"
      "v_lshrrev_b32 v253, 2, v1\n"
      "v_lshlrev_b32 v160, 8, v253\n"
      "v_lshrrev_b32 v253, 1, v2\n"
      "v_lshl_add_u32 v160, v253, 7, v160\n"
      "v_and_b32 v253, 3, v1\n"
      "v_lshl_add_u32 v160, v253, 5, v160\n"
      "v_and_b32 v253, 1, v2\n"
      "v_lshl_add_u32 v160, v253, 4, v160\n"
      "s_lshl_b32 s66, s49, 2\n"
      "v_lshl_add_u32 v161, v1, 6, v252\n"
      "v_lshl_add_u32 v161, v2, 4, v161\n"
      "v_add_u32 v161, s66, v161\n"
      "v_lshrrev_b32 v253, 3, v1\n"
      "v_lshl_add_u32 v162, v253, 9, v252\n"
      "v_lshl_add_u32 v162, v2, 7, v162\n"
      "v_and_b32 v253, 7, v1\n"
      "v_lshl_add_u32 v162, v253, 4, v162\n"
      "v_lshrrev_b32 v253, 1, v2\n"
      "v_lshl_add_u32 v163, v253, 4, v252\n"
      "v_add_u32 v163, s66, v163\n"
      "v_lshrrev_b32 v253, 3, v1\n"
      "v_lshl_add_u32 v164, v253, 4, v252\n"
      "v_lshrrev_b32 v253, 2, v1\n"
      "v_lshlrev_b32 v165, 15, v253\n"
      "v_and_b32 v253, 3, v1\n"
      "v_lshl_add_u32 v165, v253, 4, v165\n"
      "v_mov_b32 v172, 0x39000000\n"
      "v_mov_b32 v173, 0x3b000000\n"
      "s_lshl_b32 s66, s49, 6\n"
      "v_lshl_add_u32 v166, v2, 4, v252\n"
      "v_and_b32 v253, 7, v1\n"
      "v_lshl_add_u32 v166, v253, 1, v166\n"
      "v_add_u32 v166, s66, v166\n"
      "v_lshrrev_b32 v253, 3, v1\n"
      "v_lshlrev_b32 v168, 12, v253\n"
      "v_lshl_add_u32 v168, v2, 6, v168\n"
      "v_and_b32 v253, 7, v1\n"
      "v_lshl_add_u32 v168, v253, 3, v168\n"
      "v_lshlrev_b32 v170, 7, v2\n"
      "v_and_b32 v253, 3, v1\n"
      "v_lshl_add_u32 v170, v253, 5, v170\n"
      "v_lshrrev_b32 v253, 2, v1\n"
      "v_lshl_add_u32 v170, v253, 3, v170\n"
      "v_lshrrev_b32 v253, 2, v1\n"
      "v_lshl_add_u32 v167, v253, 6, v252\n"
      "v_lshl_add_u32 v167, v2, 4, v167\n"
      "v_and_b32 v253, 1, v2\n"
      "v_lshlrev_b32 v171, 16, v253\n"
      "v_lshrrev_b32_e64 v253, 1, v2\n"
      "v_lshl_add_u32 v171, v253, 13, v171\n"
      "v_lshl_add_u32 v171, v1, 3, v171\n"
      "v_xor_b32 v171, s75, v171\n"
      "s_cmp_gt_u32 s51, 27\n"
      "s_cbranch_scc1 .Lsw_36\n"
      ".Lcw_37:\n"
      "ds_read_b32 v208, v252 offset:24404\n"
      "v_mov_b32 v253, v254\n"
      "v_mov_b32 v253, v254\n"
      "v_mov_b32 v253, v254\n"
      "v_mov_b32 v253, v254\n"
      "v_mov_b32 v253, v254\n"
      "v_mov_b32 v253, v254\n"
      "v_mov_b32 v253, v254\n"
      "v_mov_b32 v253, v254\n"
      "s_waitcnt lgkmcnt(0)\n"
      "v_cmp_gt_i32 vcc, 8, v208\n"
      "v_mov_b32 v253, v254\n"
      "v_mov_b32 v253, v254\n"
      "v_mov_b32 v253, v254\n"
      "v_mov_b32 v253, v254\n"
      "s_cbranch_vccnz .Lcw_37\n"
      "s_add_i32 s71, s61, -1714\n"
      ".Lpa_38:\n"
      "s_memrealtime s[64:65]\n"
      "v_mov_b32 v253, v254\n"
      "v_mov_b32 v253, v254\n"
      "v_mov_b32 v253, v254\n"
      "v_mov_b32 v253, v254\n"
      "v_mov_b32 v253, v254\n"
      "v_mov_b32 v253, v254\n"
      "v_mov_b32 v253, v254\n"
      "v_mov_b32 v253, v254\n"
      "s_waitcnt lgkmcnt(0)\n"
      "s_sub_i32 s65, s64, s71\n"
      "s_cmp_lt_i32 s65, 0\n"
      "s_cbranch_scc1 .Lpa_38\n"
      "s_mov_b32 s66, 7168\n"
      "s_mov_b32 s67, 0\n"
      "s_mov_b32 s84, 93184\n"
      "s_mov_b32 s85, 0\n"
      "global_load_dwordx2 v[64:65], v[248:249], off\n"
      "global_load_dwordx2 v[66:67], v[248:249], off offset:3584\n"
      "v_lshl_add_u64 v[248:249], v[248:249], 0, s[66:67]\n"
      "global_load_dwordx2 v[68:69], v[248:249], off\n"
      "global_load_dwordx2 v[70:71], v[248:249], off offset:3584\n"
      "v_lshl_add_u64 v[248:249], v[248:249], 0, s[66:67]\n"
      "global_load_dwordx2 v[72:73], v[248:249], off\n"
      "global_load_dwordx2 v[74:75], v[248:249], off offset:3584\n"
      "v_lshl_add_u64 v[248:249], v[248:249], 0, s[66:67]\n"
      "global_load_dwordx2 v[76:77], v[248:249], off\n"
      "global_load_dwordx2 v[78:79], v[248:249], off offset:3584\n"
      "v_lshl_add_u64 v[248:249], v[248:249], 0, s[84:85]\n"
      "s_add_i32 s71, s61, -1714\n"
      ".Lpa_39:\n"
      "s_memrealtime s[64:65]\n"
      "v_mov_b32 v253, v254\n"
      "v_mov_b32 v253, v254\n"
      "v_mov_b32 v253, v254\n"
      "v_mov_b32 v253, v254\n"
      "v_mov_b32 v253, v254\n"
      "v_mov_b32 v253, v254\n"
      "v_mov_b32_e64 v253, v254\n"
      "v_mov_b32_e64 v253, v254\n"
      "s_waitcnt lgkmcnt(0)\n"
      "s_sub_i32 s65, s64, s71\n"
      "s_cmp_lt_i32 s65, 0\n"
      "s_cbranch_scc1 .Lpa_39\n"
      "s_mov_b32 s66, 7168\n"
      "s_mov_b32 s67, 0\n"
      "s_mov_b32 s84, 93184\n"
      "s_mov_b32 s85, 0\n"
      "global_load_dwordx2 v[80:81], v[248:249], off\n"
      "global_load_dwordx2 v[82:83], v[248:249], off offset:3584\n"
      "v_lshl_add_u64 v[248:249], v[248:249], 0, s[66:67]\n"
      "global_load_dwordx2 v[84:85], v[248:249], off\n"
      "global_load_dwordx2 v[86:87], v[248:249], off offset:3584\n"
      "v_lshl_add_u64 v[248:249], v[248:249], 0, s[66:67]\n"
      "global_load_dwordx2 v[88:89], v[248:249], off\n"
      "global_load_dwordx2 v[90:91], v[248:249], off offset:3584\n"
      "v_lshl_add_u64 v[248:249], v[248:249], 0, s[66:67]\n"
      "global_load_dwordx2 v[92:93], v[248:249], off\n"
      "global_load_dwordx2 v[94:95], v[248:249], off offset:3584\n"
      "v_lshl_add_u64 v[248:249], v[248:249], 0, s[84:85]\n"
      "s_add_i32 s71, s61, -1714\n"
      ".Lpa_40:\n"
      "s_memrealtime s[64:65]\n"
      "v_mov_b32 v253, v254\n"
      "v_mov_b32 v253, v254\n"
      "v_mov_b32 v253, v254\n"
      "v_mov_b32 v253, v254\n"
      "v_mov_b32 v253, v254\n"
      "v_mov_b32 v253, v254\n"
      "v_mov_b32_e64 v253, v254\n"
      "v_mov_b32_e64 v253, v254\n"
      "s_waitcnt lgkmcnt(0)\n"
      "s_sub_i32 s65, s64, s71\n"
      "s_cmp_lt_i32 s65, 0\n"
      "s_cbranch_scc1 .Lpa_40\n"
      "s_mov_b32 s66, 7168\n"
      "s_mov_b32 s67, 0\n"
      "s_mov_b32 s84, 93184\n"
      "s_mov_b32 s85, 0\n"
      "global_load_dwordx2 v[96:97], v[248:249], off\n"
      "global_load_dwordx2 v[98:99], v[248:249], off offset:3584\n"
      "v_lshl_add_u64 v[248:249], v[248:249], 0, s[66:67]\n"
      "global_load_dwordx2 v[100:101], v[248:249], off\n"
      "global_load_dwordx2 v[102:103], v[248:249], off offset:3584\n"
      "v_lshl_add_u64 v[248:249], v[248:249], 0, s[66:67]\n"
      "global_load_dwordx2 v[104:105], v[248:249], off\n"
      "global_load_dwordx2 v[106:107], v[248:249], off offset:3584\n"
      "v_lshl_add_u64 v[248:249], v[248:249], 0, s[66:67]\n"
      "global_load_dwordx2 v[108:109], v[248:249], off\n"
      "global_load_dwordx2 v[110:111], v[248:249], off offset:3584\n"
      "v_lshl_add_u64 v[248:249], v[248:249], 0, s[84:85]\n"
      "s_add_i32 s71, s61, -1714\n"
      ".Lpa_41:\n"
      "s_memrealtime s[64:65]\n"
      "v_mov_b32 v253, v254\n"
      "v_mov_b32 v253, v254\n"
      "v_mov_b32 v253, v254\n"
      "v_mov_b32 v253, v254\n"
      "v_mov_b32 v253, v254\n"
      "v_mov_b32 v253, v254\n"
      "v_mov_b32_e64 v253, v254\n"
      "v_mov_b32_e64 v253, v254\n"
      "s_waitcnt lgkmcnt(0)\n"
      "s_sub_i32 s65, s64, s71\n"
      "s_cmp_lt_i32 s65, 0\n"
      "s_cbranch_scc1 .Lpa_41\n"
      "s_mov_b32 s66, 7168\n"
      "s_mov_b32 s67, 0\n"
      "s_mov_b32 s84, 93184\n"
      "s_mov_b32 s85, 0\n"
      "global_load_dwordx2 v[112:113], v[248:249], off\n"
      "global_load_dwordx2 v[114:115], v[248:249], off offset:3584\n"
      "v_lshl_add_u64 v[248:249], v[248:249], 0, s[66:67]\n"
      "global_load_dwordx2 v[116:117], v[248:249], off\n"
      "global_load_dwordx2 v[118:119], v[248:249], off offset:3584\n"
      "v_lshl_add_u64 v[248:249], v[248:249], 0, s[66:67]\n"
      "global_load_dwordx2 v[120:121], v[248:249], off\n"
      "global_load_dwordx2 v[122:123], v[248:249], off offset:3584\n"
      "v_lshl_add_u64 v[248:249], v[248:249], 0, s[66:67]\n"
      "global_load_dwordx2 v[124:125], v[248:249], off\n"
      "global_load_dwordx2 v[126:127], v[248:249], off offset:3584\n"
      "s_lshl_b32 s70, s54, 18\n"
      "s_lshl_b32 s71, s49, 16\n"
      "s_add_i32 s70, s70, s71\n"
      "s_lshl_b32 s71, s55, 6\n"
      "s_add_i32 s70, s70, s71\n"
      "s_lshl_b32 s66, s49, 12\n"
      "s_add_i32 m0, s66, 133120\n"
      ".Lcw_42:\n"
      "ds_read_b32 v244, v252 offset:24416\n"
      "v_mov_b32 v253, v254\n"
      "v_mov_b32 v253, v254\n"
      "v_mov_b32 v253, v254\n"
      "v_mov_b32 v253, v254\n"
      "v_mov_b32 v253, v254\n"
      "v_mov_b32 v253, v254\n"
      "v_mov_b32 v253, v254\n"
      "v_mov_b32 v253, v254\n"
      "s_waitcnt lgkmcnt(0)\n"
      "v_cmp_gt_i32 vcc, 4, v244\n"
      "v_mov_b32 v253, v254\n"
      "v_mov_b32 v253, v254\n"
      "v_mov_b32 v253, v254\n"
      "v_mov_b32 v253, v254\n"
      "s_cbranch_vccnz .Lcw_42\n"
      "s_add_i32 s71, s61, -1714\n"
      ".Lpa_43:\n"
      "s_memrealtime s[64:65]\n"
      "v_mov_b32 v253, v254\n"
      "v_mov_b32 v253, v254\n"
      "v_mov_b32 v253, v254\n"
      "v_mov_b32 v253, v254\n"
      "v_mov_b32 v253, v254\n"
      "v_mov_b32_e64 v253, v254\n"
      "v_mov_b32_e64 v253, v254\n"
      "v_mov_b32_e64 v253, v254\n"
      "s_waitcnt lgkmcnt(0)\n"
      "s_sub_i32 s65, s64, s71\n"
      "s_cmp_lt_i32 s65, 0\n"
      "s_cbranch_scc1 .Lpa_43\n"
      "buffer_load_dwordx4 v7, s[28:31], s70 offen lds\n"
      "s_addk_i32 m0, 1024\n"
      "s_add_i32 s70, s70, 0x4000\n"
      "buffer_load_dwordx4 v7, s[28:31], s70 offen lds\n"
      "s_addk_i32 m0, 1024\n"
      "s_add_i32 s70, s70, 0x4000\n"
      "buffer_load_dwordx4 v7, s[28:31], s70 offen lds\n"
      "s_addk_i32 m0, 1024\n"
      "s_add_i32 s70, s70, 0x4000\n"
      "buffer_load_dwordx4 v7, s[28:31], s70 offen lds\n"
      "s_lshl_b32 s69, s59, 15\n"
      "s_lshl_b32 s71, s49, 8\n"
      "s_add_i32 s69, s69, s71\n"
      "s_add_i32 s70, s69, 4096\n"
      "s_add_i32 s71, s61, -1714\n"
      ".Lpa_44:\n"
      "s_memrealtime s[64:65]\n"
      "v_mov_b32 v253, v254\n"
      "v_mov_b32 v253, v254\n"
      "v_mov_b32 v253, v254\n"
      "v_mov_b32 v253, v254\n"
      "v_mov_b32 v253, v254\n"
      "v_mov_b32 v253, v254\n"
      "v_mov_b32 v253, v254\n"
      "v_mov_b32 v253, v254\n"
      "s_waitcnt lgkmcnt(0)\n"
      "s_sub_i32 s65, s64, s71\n"
      "s_cmp_lt_i32 s65, 0\n"
      "s_cbranch_scc1 .Lpa_44\n"
      "buffer_load_dwordx4 v[176:179], v6, s[40:43], s69 offen\n"
      "buffer_load_dwordx4 v[180:183], v6, s[40:43], s69 offen offset:1024\n"
      "buffer_load_dwordx4 v[184:187], v6, s[40:43], s69 offen offset:2048\n"
      "buffer_load_dwordx4 v[188:191], v6, s[40:43], s69 offen offset:3072\n"
      "buffer_load_dwordx4 v[192:195], v6, s[40:43], s70 offen\n"
      "buffer_load_dwordx4 v[196:199], v6, s[40:43], s70 offen offset:1024\n"
      "buffer_load_dwordx4 v[200:203], v6, s[40:43], s70 offen offset:2048\n"
      "buffer_load_dwordx4 v[204:207], v6, s[40:43], s70 offen offset:3072\n"
      "s_waitcnt vmcnt(36)\n"
      "v_perm_b32 v48, v66, v64, s72\n"
      "v_perm_b32 v49, v70, v68, s72\n"
      "v_perm_b32 v50, v74, v72, s72\n"
      "v_perm_b32 v51, v78, v76, s72\n"
      "v_perm_b32 v52, v66, v64, s73\n"
      "v_perm_b32 v53, v70, v68, s73\n"
      "v_perm_b32 v54, v74, v72, s73\n"
      "v_perm_b32 v55, v78, v76, s73\n"
      "v_perm_b32 v56, v67, v65, s72\n"
      "v_perm_b32 v57, v71, v69, s72\n"
      "v_perm_b32 v58, v75, v73, s72\n"
      "v_perm_b32 v59, v79, v77, s72\n"
      "v_perm_b32 v60, v67, v65, s73\n"
      "v_perm_b32 v61, v71, v69, s73\n"
      "v_perm_b32 v62, v75, v73, s73\n"
      "v_perm_b32 v63, v79, v77, s73\n"
      "s_waitcnt vmcnt(28)\n"
      "v_perm_b32 v64, v82, v80, s72\n"
      "v_perm_b32 v65, v86, v84, s72\n"
      "v_perm_b32 v66, v90, v88, s72\n"
      "v_perm_b32 v67, v94, v92, s72\n"
      "v_perm_b32 v68, v82, v80, s73\n"
      "v_perm_b32 v69, v86, v84, s73\n"
      "v_perm_b32 v70, v90, v88, s73\n"
      "v_perm_b32 v71, v94, v92, s73\n"
      "v_perm_b32 v72, v83, v81, s72\n"
      "v_perm_b32 v73, v87, v85, s72\n"
      "v_perm_b32 v74, v91, v89, s72\n"
      "v_perm_b32 v75, v95, v93, s72\n"
      "v_perm_b32 v76, v83, v81, s73\n"
      "v_perm_b32 v77, v87, v85, s73\n"
      "v_perm_b32 v78, v91, v89, s73\n"
      "v_perm_b32 v79, v95, v93, s73\n"
      "s_waitcnt vmcnt(20)\n"
      "v_perm_b32 v80, v98, v96, s72\n"
      "v_perm_b32 v81, v102, v100, s72\n"
      "v_perm_b32 v82, v106, v104, s72\n"
      "v_perm_b32 v83, v110, v108, s72\n"
      "v_perm_b32 v84, v98, v96, s73\n"
      "v_perm_b32 v85, v102, v100, s73\n"
      "v_perm_b32 v86, v106, v104, s73\n"
      "v_perm_b32 v87, v110, v108, s73\n"
      "v_perm_b32 v88, v99, v97, s72\n"
      "v_perm_b32 v89, v103, v101, s72\n"
      "v_perm_b32 v90, v107, v105, s72\n"
      "v_perm_b32 v91, v111, v109, s72\n"
      "v_perm_b32 v92, v99, v97, s73\n"
      "v_perm_b32 v93, v103, v101, s73\n"
      "v_perm_b32 v94, v107, v105, s73\n"
      "v_perm_b32 v95, v111, v109, s73\n"
      "s_waitcnt vmcnt(12)\n"
      "v_perm_b32 v96, v114, v112, s72\n"
      "v_perm_b32 v97, v118, v116, s72\n"
      "v_perm_b32 v98, v122, v120, s72\n"
      "v_perm_b32 v99, v126, v124, s72\n"
      "v_perm_b32 v100, v114, v112, s73\n"
      "v_perm_b32 v101, v118, v116, s73\n"
      "v_perm_b32 v102, v122, v120, s73\n"
      "v_perm_b32 v103, v126, v124, s73\n"
      "v_perm_b32 v104, v115, v113, s72\n"
      "v_perm_b32 v105, v119, v117, s72\n"
      "v_perm_b32 v106, v123, v121, s72\n"
      "v_perm_b32 v107, v127, v125, s72\n"
      "v_perm_b32 v108, v115, v113, s73\n"
      "v_perm_b32 v109, v119, v117, s73\n"
      "v_perm_b32 v110, v123, v121, s73\n"
      "v_perm_b32 v111, v127, v125, s73\n"
      "v_mov_b32 v224, 0x3f803f80\n"
      "v_mov_b32 v225, 0x3f803f80\n"
      "v_mov_b32 v226, 0x3f803f80\n"
      "v_mov_b32 v227, 0x3f803f80\n"
      "s_nop 1\n"
      "v_mfma_f32_16x16x32_bf16 v[208:211], v[224:227], v[48:51], 0\n"
      "v_mfma_f32_16x16x32_bf16 v[212:215], v[224:227], v[52:55], 0\n"
      "v_mfma_f32_16x16x32_bf16 v[216:219], v[224:227], v[56:59], 0\n"
      "v_mfma_f32_16x16x32_bf16 v[220:223], v[224:227], v[60:63], 0\n"
      "v_mfma_f32_16x16x32_bf16 v[208:211], v[224:227], v[64:67], v[208:211]\n"
      "v_mfma_f32_16x16x32_bf16 v[212:215], v[224:227], v[68:71], v[212:215]\n"
      "v_mfma_f32_16x16x32_bf16 v[216:219], v[224:227], v[72:75], v[216:219]\n"
      "v_mfma_f32_16x16x32_bf16 v[220:223], v[224:227], v[76:79], v[220:223]\n"
      "v_mfma_f32_16x16x32_bf16 v[208:211], v[224:227], v[80:83], v[208:211]\n"
      "v_mfma_f32_16x16x32_bf16 v[212:215], v[224:227], v[84:87], v[212:215]\n"
      "v_mfma_f32_16x16x32_bf16 v[216:219], v[224:227], v[88:91], v[216:219]\n"
      "v_mfma_f32_16x16x32_bf16 v[220:223], v[224:227], v[92:95], v[220:223]\n"
      "v_mfma_f32_16x16x32_bf16 v[208:211], v[224:227], v[96:99], v[208:211]\n"
      "v_mfma_f32_16x16x32_bf16 v[212:215], v[224:227], v[100:103], v[212:215]\n"
      "v_mfma_f32_16x16x32_bf16 v[216:219], v[224:227], v[104:107], v[216:219]\n"
      "v_mfma_f32_16x16x32_bf16 v[220:223], v[224:227], v[108:111], v[220:223]\n"
      "s_mov_b64 exec, 0xffff\n"
      "s_nop 3\n"
      "ds_write_b32 v161, v208 offset:23296\n"
      "s_mov_b32 exec_lo, 0xffff0000\n"
      "s_mov_b32 exec_hi, 0x0\n"
      "ds_write_b32 v161, v212 offset:23296\n"
      "s_mov_b32 exec_lo, 0x0\n"
      "s_mov_b32 exec_hi, 0xffff\n"
      "ds_write_b32 v161, v216 offset:23296\n"
      "s_mov_b32 exec_lo, 0x0\n"
      "s_mov_b32 exec_hi, 0xffff0000\n"
      "ds_write_b32 v161, v220 offset:23296\n"
      "s_mov_b64 exec, 0x1\n"
      "ds_add_u32 v252, v254 offset:24364\n"
      "s_mov_b64 exec, -1\n"
      "s_waitcnt vmcnt(8)\n"
      "s_mov_b64 exec, 0x1\n"
      "ds_add_u32 v252, v254 offset:24420\n"
      "s_mov_b64 exec, -1\n"
      ".Lcw_45:\n"
      "ds_read_b32 v232, v252 offset:24364\n"
      "ds_read_b128 v[208:211], v162 offset:23296\n"
      "s_waitcnt lgkmcnt(0)\n"
      "v_cmp_gt_i32 vcc, 4, v232\n"
      "s_cbranch_vccnz .Lcw_45\n"
      "v_pk_add_f32 v[208:209], v[208:209], v[210:211]\n"
      "v_add_f32 v4, v208, v209\n"
      ".Lnw_35:\n"
      "s_add_i32 s94, s94, 0x400000\n"
      "s_add_i32 s95, s95, 0x400000\n"
      "s_add_i32 s89, s89, 0x400000\n"
      "s_bfe_u32 s71, s55, 0x30000\n"
      "s_lshl_b32 s90, s71, 16\n"
      "s_lshl_b32 s71, s49, 14\n"
      "s_add_i32 s90, s90, s71\n"
      "s_bfe_u32 s71, s55, 0x10003\n"
      "s_lshl_b32 s71, s71, 13\n"
      "s_add_i32 s90, s90, s71\n"
      "s_lshl_b32 s71, s54, 8\n"
      "s_add_i32 s90, s90, s71\n"
      "s_add_i32 s90, s90, 0x540000\n"
      "s_bfe_u32 s71, s52, 0x40000\n"
      "s_lshl_b32 s92, s71, 15\n"
      "s_bfe_u32 s71, s49, 0x10001\n"
      "s_lshl_b32 s71, s71, 14\n"
      "s_add_i32 s92, s92, s71\n"
      "s_bfe_u32 s71, s52, 0x10004\n"
      "s_lshl_b32 s71, s71, 13\n"
      "s_add_i32 s92, s92, s71\n"
      "s_bfe_u32 s71, s49, 0x10000\n"
      "s_lshl_b32 s71, s71, 12\n"
      "s_add_i32 s92, s92, s71\n"
      "s_add_i32 s92, s92, 0x540000\n"
      "s_xor_b32 s90, s90, s75\n"
      "s_xor_b32 s92, s92, s75\n"
      "s_add_i32 s88, s88, 0x400000\n"
      "s_mov_b32 s99, 602\n"
      "v_mov_b32 v232, 0x3f803f80\n"
      "v_mov_b32 v233, 0x3f803f80\n"
      "v_mov_b32 v234, 0x3f803f80\n"
      "v_mov_b32 v235, 0x3f803f80\n"
      ".Lcw_48:\n"
      "ds_read_b32 v208, v252 offset:24432\n"
      "ds_read_b32 v209, v252 offset:24408\n"
      "v_mov_b32 v253, v254\n"
      "v_mov_b32 v253, v254\n"
      "v_mov_b32 v253, v254\n"
      "v_mov_b32 v253, v254\n"
      "v_mov_b32 v253, v254\n"
      "v_mov_b32 v253, v254\n"
      "s_waitcnt lgkmcnt(0)\n"
      "v_cmp_gt_i32 vcc, 1, v208\n"
      "v_readfirstlane_b32 s61, v209\n"
      "s_cbranch_vccnz .Lcw_48\n"
      "s_cmp_gt_u32 s51, 27\n"
      "s_cbranch_scc1 .Lf1_49\n"
      "v_mov_b32_e64 v212, -1\n"
      "v_mov_b32_e64 v213, -1\n"
      "v_mov_b32_e64 v214, -1\n"
      "v_mov_b32 v215, -1\n"
      "s_mul_i32 s70, s50, 200704\n"
      "s_mul_i32 s71, s51, 7168\n"
      "s_add_i32 s70, s70, s71\n"
      "s_lshl_b32 s71, s49, 11\n"
      "s_add_i32 s70, s70, s71\n"
      "s_add_i32 s70, s70, 0x440000\n"
      "s_add_i32 s71, s61, 524\n"
      ".Lpa_50:\n"
      "s_memrealtime s[64:65]\n"
      "v_mov_b32 v253, v254\n"
      "v_mov_b32 v253, v254\n"
      "v_mov_b32 v253, v254\n"
      "v_mov_b32 v253, v254\n"
      "v_mov_b32 v253, v254\n"
      "v_mov_b32 v253, v254\n"
      "v_mov_b32 v253, v254\n"
      "v_mov_b32 v253, v254\n"
      "s_waitcnt lgkmcnt(0)\n"
      "s_sub_i32 s65, s64, s71\n"
      "s_cmp_lt_i32 s65, 0\n"
      "s_cbranch_scc1 .Lpa_50\n"
      "buffer_store_dwordx4 v[212:215], v3, s[4:7], s70 offen sc1\n"
      "s_cmp_eq_u32 s49, 3\n"
      "s_cbranch_scc1 .Lf1_49\n"
      "buffer_store_dwordx4 v[212:215], v3, s[4:7], s70 offen offset:1024 sc1\n"
      ".Lf1_49:\n"
      "s_waitcnt vmcnt(0)\n"
      "s_mov_b64 exec, 0x1\n"
      "ds_add_u32 v252, v254 offset:24392\n"
      "s_mov_b64 exec, -1\n"
      ".Lcw_51:\n"
      "ds_read_b32 v208, v252 offset:24384\n"
      "v_mov_b32 v253, v254\n"
      "v_mov_b32 v253, v254\n"
      "v_mov_b32 v253, v254\n"
      "v_mov_b32 v253, v254\n"
      "v_mov_b32 v253, v254\n"
      "v_mov_b32 v253, v254\n"
      "v_mov_b32 v253, v254\n"
      "v_mov_b32 v253, v254\n"
      "s_waitcnt lgkmcnt(0)\n"
      "v_cmp_gt_i32 vcc, 1, v208\n"
      "v_mov_b32 v253, v254\n"
      "v_mov_b32 v253, v254\n"
      "v_mov_b32 v253, v254\n"
      "v_mov_b32 v253, v254\n"
      "s_cbranch_vccnz .Lcw_51\n"
      "s_setprio 2\n"
      "s_cmp_gt_u32 s51, 27\n"
      "s_cbranch_scc1 .Lnf_46\n"
      ".Lwe_52:\n"
      "s_memrealtime s[64:65]\n"
      "v_mov_b32 v253, v254\n"
      "v_mov_b32 v253, v254\n"
      "v_mov_b32 v253, v254\n"
      "v_mov_b32 v253, v254\n"
      "v_mov_b32 v253, v254\n"
      "v_mov_b32 v253, v254\n"
      "v_mov_b32 v253, v254\n"
      "v_mov_b32 v253, v254\n"
      "s_waitcnt lgkmcnt(0)\n"
      "s_sub_i32 s64, s64, s99\n"
      "s_sub_i32 s65, s61, s64\n"
      "s_sub_i32 s65, s3, s65\n"
      "s_max_i32 s65, s65, 0\n"
      "s_add_i32 s99, s99, s65\n"
      "s_add_i32 s62, s61, s99\n"
      ".Lwf_55:\n"
      "s_memrealtime s[64:65]\n"
      "v_mov_b32 v253, v254\n"
      "v_mov_b32 v253, v254\n"
      "v_mov_b32 v253, v254\n"
      "v_mov_b32 v253, v254\n"
      "v_mov_b32 v253, v254\n"
      "v_mov_b32 v253, v254\n"
      "v_mov_b32 v253, v254\n"
      "v_mov_b32 v253, v254\n"
      "s_waitcnt lgkmcnt(0)\n"
      "s_sub_i32 s65, s62, s64\n"
      "s_cmp_gt_i32 s65, 3\n"
      "s_cbranch_scc1 .Lwf_55\n"
      "s_max_i32 s65, s65, 0\n"
      ".Lwd_56:\n"
      "s_sub_u32 s65, s65, 1\n"
      "s_cbranch_scc0 .Lwd_56\n"
      "buffer_load_dwordx4 v[144:147], v160, s[4:7], s96 offen sc1\n"
      "v_mov_b32 v253, v254\n"
      "v_mov_b32 v253, v254\n"
      "v_mov_b32 v253, v254\n"
      "buffer_load_dwordx4 v[148:151], v160, s[4:7], s96 offen offset:1024 sc1\n"
      "v_mov_b32 v253, v254\n"
      "v_mov_b32 v253, v254\n"
      "v_mov_b32 v253, v254\n"
      "buffer_load_dwordx4 v[152:155], v160, s[4:7], s96 offen offset:2048 sc1\n"
      "v_mov_b32 v253, v254\n"
      "v_mov_b32 v253, v254\n"
      "v_mov_b32 v253, v254\n"
      "buffer_load_dwordx4 v[156:159], v160, s[4:7], s96 offen offset:3072 sc1\n"
      "v_mov_b32_e64 v243, 0\n"
      "v_perm_b32 v112, v180, v176, s72\n"
      "v_perm_b32 v113, v188, v184, s72\n"
      "v_perm_b32 v114, v196, v192, s72\n"
      "v_perm_b32 v115, v204, v200, s72\n"
      "v_perm_b32 v116, v180, v176, s73\n"
      "v_perm_b32 v117, v188, v184, s73\n"
      "v_perm_b32 v118, v196, v192, s73\n"
      "v_perm_b32 v119, v204, v200, s73\n"
      "v_perm_b32 v120, v181, v177, s72\n"
      "v_perm_b32 v121, v189, v185, s72\n"
      "v_perm_b32 v122, v197, v193, s72\n"
      "v_perm_b32 v123, v205, v201, s72\n"
      "v_perm_b32 v124, v181, v177, s73\n"
      "v_perm_b32 v125, v189, v185, s73\n"
      "v_perm_b32 v126, v197, v193, s73\n"
      "v_perm_b32 v127, v205, v201, s73\n"
      "v_perm_b32 v128, v182, v178, s72\n"
      "v_perm_b32 v129, v190, v186, s72\n"
      "v_perm_b32 v130, v198, v194, s72\n"
      "v_perm_b32 v131, v206, v202, s72\n"
      "v_perm_b32 v132, v182, v178, s73\n"
      "v_perm_b32 v133, v190, v186, s73\n"
      "v_perm_b32 v134, v198, v194, s73\n"
      "v_perm_b32 v135, v206, v202, s73\n"
      "v_perm_b32 v136, v183, v179, s72\n"
      "v_perm_b32 v137, v191, v187, s72\n"
      "v_perm_b32 v138, v199, v195, s72\n"
      "v_perm_b32 v139, v207, v203, s72\n"
      "v_perm_b32 v140, v183, v179, s73\n"
      "v_perm_b32 v141, v191, v187, s73\n"
      "v_perm_b32 v142, v199, v195, s73\n"
      "v_perm_b32 v143, v207, v203, s73\n"
      "s_waitcnt vmcnt(3)\n"
      "v_mfma_f32_16x16x32_bf16 v[224:227], v[144:147], v[144:147], 0\n"
      "v_mfma_f32_16x16x32_bf16 v[228:231], v[144:147], v[232:235], 0\n"
      "v_mfma_f32_16x16x32_bf16 v[208:211], v[144:147], v[48:51], 0\n"
      "v_max3_u32 v243, v243, v144, v145\n"
      "v_max3_u32 v243, v243, v146, v147\n"
      "v_mfma_f32_16x16x32_bf16 v[212:215], v[144:147], v[52:55], 0\n"
      "v_mfma_f32_16x16x32_bf16 v[216:219], v[144:147], v[56:59], 0\n"
      "v_mfma_f32_16x16x32_bf16 v[220:223], v[144:147], v[60:63], 0\n"
      "s_waitcnt vmcnt(2)\n"
      "v_mfma_f32_16x16x32_bf16 v[224:227], v[148:151], v[148:151], v[224:227]\n"
      "v_mfma_f32_16x16x32_bf16 v[228:231], v[148:151], v[232:235], v[228:231]\n"
      "v_mfma_f32_16x16x32_bf16 v[208:211], v[148:151], v[64:67], v[208:211]\n"
      "v_max3_u32 v243, v243, v148, v149\n"
      "v_max3_u32 v243, v243, v150, v151\n"
      "v_mfma_f32_16x16x32_bf16 v[212:215], v[148:151], v[68:71], v[212:215]\n"
      "v_mfma_f32_16x16x32_bf16 v[216:219], v[148:151], v[72:75], v[216:219]\n"
      "v_mfma_f32_16x16x32_bf16 v[220:223], v[148:151], v[76:79], v[220:223]\n"
      "s_waitcnt vmcnt(1)\n"
      "v_mfma_f32_16x16x32_bf16 v[224:227], v[152:155], v[152:155], v[224:227]\n"
      "v_mfma_f32_16x16x32_bf16 v[228:231], v[152:155], v[232:235], v[228:231]\n"
      "v_mfma_f32_16x16x32_bf16 v[208:211], v[152:155], v[80:83], v[208:211]\n"
      "v_max3_u32 v243, v243, v152, v153\n"
      "v_max3_u32 v243, v243, v154, v155\n"
      "v_mfma_f32_16x16x32_bf16 v[212:215], v[152:155], v[84:87], v[212:215]\n"
      "v_mfma_f32_16x16x32_bf16 v[216:219], v[152:155], v[88:91], v[216:219]\n"
      "v_mfma_f32_16x16x32_bf16 v[220:223], v[152:155], v[92:95], v[220:223]\n"
      "s_waitcnt vmcnt(0)\n"
      "v_mfma_f32_16x16x32_bf16 v[224:227], v[156:159], v[156:159], v[224:227]\n"
      "v_mfma_f32_16x16x32_bf16 v[228:231], v[156:159], v[232:235], v[228:231]\n"
      "v_mfma_f32_16x16x32_bf16 v[208:211], v[156:159], v[96:99], v[208:211]\n"
      "v_max3_u32 v243, v243, v156, v157\n"
      "v_max3_u32 v243, v243, v158, v159\n"
      "v_mfma_f32_16x16x32_bf16 v[212:215], v[156:159], v[100:103], v[212:215]\n"
      "v_cmp_eq_u32_e64 vcc, -1, v243\n"
      "v_mfma_f32_16x16x32_bf16 v[216:219], v[156:159], v[104:107], v[216:219]\n"
      "s_nop 0\n"
      "v_pk_add_f32 v[224:225], v[224:225], v[226:227]\n"
      "v_mfma_f32_16x16x32_bf16 v[220:223], v[156:159], v[108:111], v[220:223]\n"
      "v_add_f32 v224, v224, v225\n"
      "v_pk_add_f32 v[228:229], v[228:229], v[230:231]\n"
      "v_add_f32 v228, v228, v229\n"
      "s_cbranch_vccnz .Lsl_57\n"
      ".Lok_58:\n"
      "v_pk_add_f32 v[208:209], v[208:209], v[210:211]\n"
      "v_add_f32 v208, v208, v209\n"
      "v_permlane32_swap_b32 v228, v224\n"
      "v_add_f32 v228, v228, v224\n"
      "v_pk_add_f32 v[216:217], v[216:217], v[218:219]\n"
      "v_add_f32 v216, v216, v217\n"
      "v_mov_b32 v224, v228\n"
      "v_pk_add_f32 v[212:213], v[212:213], v[214:215]\n"
      "v_add_f32 v212, v212, v213\n"
      "v_permlane16_swap_b32 v228, v224\n"
      "v_add_f32 v228, v228, v224\n"
      "v_pk_add_f32 v[220:221], v[220:221], v[222:223]\n"
      "v_add_f32 v220, v220, v221\n"
      "v_add_f32_dpp v228, v228, v228 quad_perm:[1,0,3,2] row_mask:0xf bank_mask:0xf\n"
      "v_permlane32_swap_b32 v208, v216\n"
      "v_add_f32 v208, v208, v216\n"
      "v_add_f32_dpp v228, v228, v228 quad_perm:[2,3,0,1] row_mask:0xf bank_mask:0xf\n"
      "v_permlane32_swap_b32 v212, v220\n"
      "v_add_f32 v212, v212, v220\n"
      "v_add_f32_dpp v228, v228, v228 row_half_mirror row_mask:0xf bank_mask:0xf\n"
      "s_nop 0\n"
      "v_permlane16_swap_b32 v208, v212\n"
      "v_add_f32 v208, v208, v212\n"
      "v_add_f32_dpp v228, v228, v228 row_mirror row_mask:0xf bank_mask:0xf\n"
      "ds_write_b32 v161, v208 offset:22272\n"
      "s_mov_b32 exec_lo, 0x1\n"
      "s_mov_b32 exec_hi, 0x1\n"
      "ds_write_b32 v163, v228 offset:24320\n"
      "s_mov_b64 exec, 0x1\n"
      "ds_add_u32 v252, v254 offset:24360\n"
      "s_mov_b64 exec, -1\n"
      "v_mov_b32 v253, v254\n"
      "v_mov_b32 v253, v254\n"
      "v_mov_b32 v253, v254\n"
      "v_mov_b32 v253, v254\n"
      ".Lcw_59:\n"
      "ds_read_b32 v216, v252 offset:24360\n"
      "ds_read_b128 v[212:215], v164 offset:24320\n"
      "ds_read_b128 v[208:211], v162 offset:22272\n"
      "s_waitcnt lgkmcnt(0)\n"
      "v_cmp_gt_i32 vcc, 4, v216\n"
      "v_pk_add_f32 v[212:213], v[212:213], v[214:215]\n"
      "v_add_f32 v212, v212, v213\n"
      "v_pk_add_f32 v[208:209], v[208:209], v[210:211]\n"
      "v_add_f32 v208, v208, v209\n"
      "v_mul_f32_dpp v213, v212, v172 row_newbcast:0 row_mask:0xf bank_mask:0xf\n"
      "v_mul_f32_dpp v214, v212, v173 row_newbcast:8 row_mask:0xf bank_mask:0xf\n"
      "s_cbranch_vccnz .Lcw_59\n"
      "v_fma_f32 v214, -v213, v213, v214\n"
      "v_max_f32 v214, 0, v214\n"
      "v_add_f32 v214, 0x3727c5ac, v214\n"
      "v_rsq_f32 v214, v214\n"
      "v_fma_f32 v208, -v213, v4, v208\n"
      "v_mul_f32 v208, v208, v214\n"
      "v_mul_f32 v210, 0xbfb8aa3b, v208\n"
      "v_exp_f32_e64 v210, v210\n"
      "v_mul_f32_dpp v209, v208, v208 row_ror:8 row_mask:0xf bank_mask:0xf\n"
      "v_add_f32_e64 v210, 1.0, v210\n"
      "v_rcp_f32_e64 v210, v210\n"
      "s_nop 0\n"
      "v_mul_f32_e64 v209, v209, v210\n"
      "s_nop 1\n"
      "v_mov_b32_dpp v210, v209 row_shl:1 row_mask:0xf bank_mask:0xf bound_ctrl:1\n"
      "v_cvt_pk_bf16_f32 v240, v209, v210\n"
      "s_nop 1\n"
      "v_mov_b32_dpp v241, v240 row_shl:2 row_mask:0xf bank_mask:0xf bound_ctrl:1\n"
      "v_mov_b32_dpp v242, v240 row_shl:4 row_mask:0xf bank_mask:0xf bound_ctrl:1\n"
      "v_mov_b32_dpp v243, v240 row_shl:6 row_mask:0xf bank_mask:0xf bound_ctrl:1\n"
      "s_nop 1\n"
      "v_mfma_f32_16x16x32_bf16 v[208:211], v[240:243], v[112:115], 0\n"
      "v_mfma_f32_16x16x32_bf16 v[212:215], v[240:243], v[116:119], 0\n"
      "v_mfma_f32_16x16x32_bf16 v[216:219], v[240:243], v[120:123], 0\n"
      "v_mfma_f32_16x16x32_bf16 v[220:223], v[240:243], v[124:127], 0\n"
      "v_mfma_f32_16x16x32_bf16 v[224:227], v[240:243], v[128:131], 0\n"
      "v_mfma_f32_16x16x32_bf16 v[228:231], v[240:243], v[132:135], 0\n"
      "v_mfma_f32_16x16x32_bf16 v[232:235], v[240:243], v[136:139], 0\n"
      "v_mfma_f32_16x16x32_bf16 v[236:239], v[240:243], v[140:143], 0\n"
      "s_mov_b64 exec, 0xffff\n"
      "s_nop 0\n"
      "v_cvt_pk_bf16_f32 v144, v208, v212\n"
      "s_nop 0\n"
      "v_cvt_pk_bf16_f32 v145, v216, v220\n"
      "s_nop 0\n"
      "v_cvt_pk_bf16_f32 v146, v224, v228\n"
      "s_nop 0\n"
      "v_cvt_pk_bf16_f32 v147, v232, v236\n"
      "buffer_store_dwordx4 v[144:147], v165, s[4:7], s97 offen sc1\n"
      "s_mov_b64 exec, -1\n"
      ".Lq1a_47:\n"
      "s_mov_b32 s99, 122\n"
      ".Lcw_60:\n"
      "ds_read_b32 v240, v252 offset:24432\n"
      "s_waitcnt lgkmcnt(0)\n"
      "v_cmp_gt_i32 vcc, 2, v240\n"
      "s_cbranch_vccnz .Lcw_60\n"
      "v_mov_b32 v156, 0\n"
      "v_mov_b32 v157, 0\n"
      "v_mov_b32 v158, 0\n"
      "v_mov_b32 v159, 0\n"
      ".Lwe_61:\n"
      ".Lwt_62:\n"
      "s_memrealtime s[64:65]\n"
      "ds_read_b32 v253, v252 offset:24408\n"
      "v_mov_b32 v254, 1\n"
      "v_mov_b32 v254, 1\n"
      "v_mov_b32 v254, 1\n"
      "v_mov_b32 v254, 1\n"
      "v_mov_b32 v254, 1\n"
      "v_mov_b32 v254, 1\n"
      "v_mov_b32 v254, 1\n"
      "v_mov_b32 v254, 1\n"
      "s_waitcnt lgkmcnt(0)\n"
      "v_readfirstlane_b32 s61, v253\n"
      "s_sub_i32 s64, s64, s99\n"
      "s_sub_i32 s65, s61, s64\n"
      "s_cmp_gt_i32 s65, 16\n"
      "s_cbranch_scc1 .Lwt_62\n"
      "s_add_i32 s62, s61, s99\n"
      "s_cmp_lt_i32 s65, s78\n"
      "s_cbranch_scc1 .Lws_63\n"
      ".Lwf_64:\n"
      "s_memrealtime s[64:65]\n"
      "v_mov_b32 v253, v254\n"
      "v_mov_b32 v253, v254\n"
      "v_mov_b32 v253, v254\n"
      "v_mov_b32 v253, v254\n"
      "v_mov_b32 v253, v254\n"
      "v_mov_b32 v253, v254\n"
      "v_mov_b32 v253, v254\n"
      "v_mov_b32 v253, v254\n"
      "s_waitcnt lgkmcnt(0)\n"
      "s_sub_i32 s65, s62, s64\n"
      "s_cmp_gt_i32 s65, 3\n"
      "s_cbranch_scc1 .Lwf_64\n"
      "s_max_i32 s65, s65, 0\n"
      ".Lwd_65:\n"
      "s_sub_u32 s65, s65, 1\n"
      "s_cbranch_scc0 .Lwd_65\n"
      "buffer_load_dwordx4 v[144:147], v3, s[4:7], s98 offen sc1\n"
      "buffer_load_dwordx4 v[148:151], v3, s[4:7], s98 offen offset:1024 sc1\n"
      "buffer_load_dwordx4 v[152:155], v3, s[4:7], s98 offen offset:2048 sc1\n"
      "s_mov_b32 exec_hi, 15\n"
      "buffer_load_dwordx4 v[156:159], v3, s[4:7], s98 offen offset:3072 sc1\n"
      "s_mov_b64 exec, -1\n"
      "v_mov_b32 v243, 0\n"
      "s_lshl_b32 s70, s52, 12\n"
      "s_lshl_b32 s71, s49, 10\n"
      "s_add_i32 s70, s70, s71\n"
      "s_add_i32 s70, s70, 0x20000\n"
      "s_waitcnt vmcnt(3)\n"
      "v_mfma_f32_16x16x32_bf16 v[208:211], v[144:147], v[8:11], 0\n"
      "v_max3_u32 v243, v243, v144, v145\n"
      "v_max3_u32 v243, v243, v146, v147\n"
      "s_waitcnt vmcnt(2)\n"
      "v_mfma_f32_16x16x32_bf16 v[208:211], v[148:151], v[8:11], v[208:211]\n"
      "v_max3_u32 v243, v243, v148, v149\n"
      "v_max3_u32 v243, v243, v150, v151\n"
      "s_waitcnt vmcnt(1)\n"
      "v_mfma_f32_16x16x32_bf16 v[208:211], v[152:155], v[8:11], v[208:211]\n"
      "v_max3_u32 v243, v243, v152, v153\n"
      "v_max3_u32 v243, v243, v154, v155\n"
      "s_waitcnt vmcnt(0)\n"
      "v_mfma_f32_16x16x32_bf16 v[208:211], v[156:159], v[8:11], v[208:211]\n"
      "v_max3_u32 v243, v243, v156, v157\n"
      "v_max3_u32 v243, v243, v158, v159\n"
      "v_cmp_eq_u32 vcc, -1, v243\n"
      "s_nop 4\n"
      "v_permlane32_swap_b32 v208, v210\n"
      "v_add_f32 v208, v208, v210\n"
      "v_permlane32_swap_b32 v209, v211\n"
      "v_add_f32 v209, v209, v211\n"
      "s_cbranch_vccnz .Lsl_66\n"
      ".Lok_67:\n"
      "s_nop 0\n"
      "v_permlane16_swap_b32_e64 v208, v209\n"
      "v_add_f32 v208, v208, v209\n"
      "v_cvt_pk_bf16_f32 v230, v208, v208\n"
      "ds_write_b16 v166, v230 offset:22016\n"
      "s_mov_b64 exec, 0x1\n"
      "ds_add_u32 v252, v254 offset:24352\n"
      "s_mov_b64 exec, -1\n"
      "buffer_load_dwordx4 v[240:243], v3, s[4:7], s70 offen sc1\n"
      ".Lcw_68:\n"
      "ds_read_b32 v236, v252 offset:24352\n"
      "ds_read_b128 v[224:227], v167 offset:22016\n"
      "s_waitcnt lgkmcnt(0)\n"
      "v_cmp_gt_i32 vcc, 8, v236\n"
      "v_mfma_f32_16x16x32_bf16 v[208:211], v[224:227], v[16:19], 0\n"
      "v_mfma_f32_16x16x32_bf16 v[212:215], v[224:227], v[20:23], 0\n"
      "s_cbranch_vccnz .Lcw_68\n"
      "v_mfma_f32_16x16x32_bf16 v[216:219], v[224:227], v[24:27], 0\n"
      "v_mfma_f32_16x16x32_bf16 v[220:223], v[224:227], v[28:31], 0\n"
      "s_mov_b32 s99, 204\n"
      "s_nop 3\n"
      "v_cvt_pk_bf16_f32 v228, v208, v212\n"
      "s_nop 1\n"
      "v_cvt_pk_bf16_f32 v229, v216, v220\n"
      "buffer_store_dwordx2 v[228:229], v168, s[4:7], s90 offen sc1\n"
      ".Lwe_69:\n"
      ".Lwt_70:\n"
      "s_memrealtime s[64:65]\n"
      "ds_read_b32 v253, v252 offset:24408\n"
      "v_mov_b32 v254, 1\n"
      "v_mov_b32 v254, 1\n"
      "v_mov_b32 v254, 1\n"
      "v_mov_b32 v254, 1\n"
      "v_mov_b32 v254, 1\n"
      "v_mov_b32 v254, 1\n"
      "v_mov_b32 v254, 1\n"
      "v_mov_b32 v254, 1\n"
      "s_waitcnt lgkmcnt(0)\n"
      "v_readfirstlane_b32 s61, v253\n"
      "s_sub_i32 s64, s64, s99\n"
      "s_sub_i32 s65, s61, s64\n"
      "s_cmp_gt_i32 s65, 16\n"
      "s_cbranch_scc1 .Lwt_70\n"
      "s_add_i32 s62, s61, s99\n"
      "s_cmp_lt_i32 s65, s78\n"
      "s_cbranch_scc1 .Lws_71\n"
      ".Lwf_72:\n"
      "s_memrealtime s[64:65]\n"
      "v_mov_b32 v253, v254\n"
      "v_mov_b32 v253, v254\n"
      "v_mov_b32 v253, v254\n"
      "v_mov_b32 v253, v254\n"
      "v_mov_b32 v253, v254\n"
      "v_mov_b32 v253, v254\n"
      "v_mov_b32 v253, v254\n"
      "v_mov_b32 v253, v254\n"
      "s_waitcnt lgkmcnt(0)\n"
      "s_sub_i32 s65, s62, s64\n"
      "s_cmp_gt_i32 s65, 3\n"
      "s_cbranch_scc1 .Lwf_72\n"
      "s_max_i32 s65, s65, 0\n"
      ".Lwd_73:\n"
      "s_sub_u32 s65, s65, 1\n"
      "s_cbranch_scc0 .Lwd_73\n"
      "buffer_load_dwordx2 v[224:225], v170, s[4:7], s93 offen sc1\n"
      "v_mov_b32 v253, v254\n"
      "v_mov_b32 v253, v254\n"
      "v_mov_b32 v253, v254\n"
      "v_mov_b32 v253, v254\n"
      "v_mov_b32 v253, v254\n"
      "v_mov_b32 v253, v254\n"
      "v_mov_b32 v253, v254\n"
      "v_mov_b32 v253, v254\n"
      "v_mov_b32 v253, v254\n"
      "v_mov_b32 v253, v254\n"
      "v_mov_b32 v253, v254\n"
      "v_mov_b32 v253, v254\n"
      "buffer_load_dwordx4 v[144:147], v3, s[4:7], s92 offen sc1\n"
      "buffer_load_dwordx4 v[148:151], v3, s[4:7], s92 offen offset:1024 sc1\n"
      "buffer_load_dwordx4 v[152:155], v3, s[4:7], s92 offen offset:2048 sc1\n"
      "buffer_load_dwordx4 v[156:159], v3, s[4:7], s92 offen offset:3072 sc1\n"
      "s_mul_i32 s66, s49, 4160\n"
      "v_add_u32 v7, s66, v14\n"
      ".Lcw_74:\n"
      "ds_read_b32 v236, v252 offset:24400\n"
      "v_mov_b32 v253, v254\n"
      "v_mov_b32 v253, v254\n"
      "v_mov_b32 v253, v254\n"
      "v_mov_b32 v253, v254\n"
      "v_mov_b32 v253, v254\n"
      "v_mov_b32 v253, v254\n"
      "v_mov_b32 v253, v254\n"
      "v_mov_b32 v253, v254\n"
      "s_waitcnt lgkmcnt(0)\n"
      "v_cmp_gt_i32 vcc, 4, v236\n"
      "v_mov_b32 v253, v254\n"
      "v_mov_b32 v253, v254\n"
      "v_mov_b32 v253, v254\n"
      "v_mov_b32 v253, v254\n"
      "s_cbranch_vccnz .Lcw_74\n"
      "s_waitcnt vmcnt(6)\n"
      "v_mfma_f32_16x16x32_bf16 v[248:251], v[8:11], v[240:243], 0\n"
      "s_nop 7\n"
      "v_add_f32_dpp v248, v248, v248 row_ror:4 row_mask:0xf bank_mask:0xf\n"
      "v_add_f32_dpp v249, v249, v249 row_ror:4 row_mask:0xf bank_mask:0xf\n"
      "v_add_f32_dpp v250, v250, v250 row_ror:4 row_mask:0xf bank_mask:0xf\n"
      "v_add_f32_dpp v251, v251, v251 row_ror:4 row_mask:0xf bank_mask:0xf\n"
      "v_add_f32_dpp v248, v248, v248 row_ror:8 row_mask:0xf bank_mask:0xf\n"
      "v_add_f32_dpp v249, v249, v249 row_ror:8 row_mask:0xf bank_mask:0xf\n"
      "v_add_f32_dpp v250, v250, v250 row_ror:8 row_mask:0xf bank_mask:0xf\n"
      "v_add_f32_dpp v251, v251, v251 row_ror:8 row_mask:0xf bank_mask:0xf\n"
      "s_waitcnt vmcnt(4)\n"
      "v_max_u32_e64 v244, v224, v225\n"
      "v_add_f32_dpp v224, v224, v224 quad_perm:[1,0,3,2] row_mask:0xf bank_mask:0xf\n"
      "v_add_f32_dpp v225, v225, v225 quad_perm:[1,0,3,2] row_mask:0xf bank_mask:0xf\n"
      "s_nop 0\n"
      "v_add_f32_dpp v224, v224, v224 quad_perm:[2,3,0,1] row_mask:0xf bank_mask:0xf\n"
      "v_add_f32_dpp v225, v225, v225 quad_perm:[2,3,0,1] row_mask:0xf bank_mask:0xf\n"
      "s_nop 1\n"
      "v_permlane16_swap_b32 v224, v225\n"
      "v_add_f32_e64 v224, v224, v225\n"
      "v_mov_b32 v225, v224\n"
      "s_nop 1\n"
      "v_permlane32_swap_b32_e64 v224, v225\n"
      "v_add_f32_e64 v224, v224, v225\n"
      "v_mul_f32_e64 v224, v173, v224\n"
      "v_mov_b32 v225, v224\n"
      "s_nop 1\n"
      "v_permlane16_swap_b32_e64 v224, v225\n"
      "v_fma_f32 v225, -v224, v224, v225\n"
      "v_max_f32_e64 v225, 0, v225\n"
      "v_add_f32 v225, 0x3727c5ac, v225\n"
      "v_rsq_f32 v225, v225\n"
      "s_nop 0\n"
      "v_pk_mul_f32 v[228:229], v[248:249], v[224:225] op_sel_hi:[1,0] neg_lo:[1,0] neg_hi:[1,0]\n"
      "v_pk_mul_f32 v[230:231], v[250:251], v[224:225] op_sel_hi:[1,0] neg_lo:[1,0] neg_hi:[1,0]\n"
      "v_mul_f32 v225, 0x3e0293ee, v225\n"
      "s_waitcnt vmcnt(3)\n"
      "v_mfma_f32_16x16x32_bf16 v[228:231], v[8:11], v[144:147], v[228:231]\n"
      "v_max3_u32 v244, v244, v144, v145\n"
      "v_max3_u32 v244, v244, v146, v147\n"
      "s_waitcnt vmcnt(2)\n"
      "v_mfma_f32_16x16x32_bf16 v[228:231], v[8:11], v[148:151], v[228:231]\n"
      "v_max3_u32 v244, v244, v148, v149\n"
      "v_max3_u32 v244, v244, v150, v151\n"
      "s_waitcnt vmcnt(1)\n"
      "v_mfma_f32_16x16x32_bf16 v[228:231], v[8:11], v[152:155], v[228:231]\n"
      "v_max3_u32 v244, v244, v152, v153\n"
      "v_max3_u32 v244, v244, v154, v155\n"
      "s_waitcnt vmcnt(0)\n"
      "v_mfma_f32_16x16x32_bf16 v[228:231], v[8:11], v[156:159], v[228:231]\n"
      "v_max3_u32 v244, v244, v156, v157\n"
      "v_max3_u32 v244, v244, v158, v159\n"
      "v_cmp_eq_u32 vcc, -1, v244\n"
      "s_nop 4\n"
      "v_pk_mul_f32 v[228:229], v[228:229], v[224:225] op_sel:[0,1] op_sel_hi:[1,1]\n"
      "v_pk_mul_f32 v[230:231], v[230:231], v[224:225] op_sel:[0,1] op_sel_hi:[1,1]\n"
      "v_cvt_pk_bf16_f32 v234, v228, v229\n"
      "v_cvt_pk_bf16_f32 v235, v230, v231\n"
      "v_and_b32 v232, v12, v234\n"
      "v_and_b32_e64 v233, v12, v235\n"
      "v_and_b32_e64 v234, v13, v234\n"
      "v_and_b32_e64 v235, v13, v235\n"
      "s_nop 1\n"
      "v_mfma_f32_16x16x32_bf16 v[208:211], v[232:235], v[32:35], 0\n"
      "v_mfma_f32_16x16x32_bf16 v[212:215], v[232:235], v[36:39], 0\n"
      "v_mfma_f32_16x16x32_bf16 v[216:219], v[232:235], v[40:43], 0\n"
      "v_mfma_f32_16x16x32_bf16 v[220:223], v[232:235], v[44:47], 0\n"
      "s_cbranch_vccnz .Lsl_75\n"
      ".Lok_76:\n"
      "s_mov_b32 s99, 288\n"
      "s_nop 2\n"
      "v_pk_add_f32 v[208:209], v[208:209], v[210:211]\n"
      "v_add_f32_e64 v228, v208, v209\n"
      "v_pk_add_f32 v[212:213], v[212:213], v[214:215]\n"
      "v_add_f32 v229, v212, v213\n"
      "v_pk_add_f32 v[216:217], v[216:217], v[218:219]\n"
      "v_add_f32 v230, v216, v217\n"
      "v_pk_add_f32 v[220:221], v[220:221], v[222:223]\n"
      "v_add_f32_e64 v231, v220, v221\n"
      "ds_write_b128 v15, v[228:231] offset:24448\n"
      "s_mov_b64 exec, 0x1\n"
      "ds_add_rtn_u32 v232, v252, v254 offset:24356\n"
      "s_mov_b64 exec, -1\n"
      "ds_read_b128 v[208:211], v14 offset:24448\n"
      "ds_read_b128 v[212:215], v14 offset:25472\n"
      "ds_read_b128 v[216:219], v14 offset:26496\n"
      "ds_read_b128 v[220:223], v14 offset:27520\n"
      "s_waitcnt lgkmcnt(4)\n"
      "v_readfirstlane_b32 s66, v232\n"
      "s_waitcnt lgkmcnt(2)\n"
      "v_pk_add_f32 v[208:209], v[208:209], v[212:213]\n"
      "v_pk_add_f32 v[210:211], v[210:211], v[214:215]\n"
      "s_waitcnt lgkmcnt(0)\n"
      "v_pk_add_f32 v[216:217], v[216:217], v[220:221]\n"
      "v_pk_add_f32 v[218:219], v[218:219], v[222:223]\n"
      "v_pk_add_f32 v[208:209], v[208:209], v[216:217]\n"
      "v_pk_add_f32 v[210:211], v[210:211], v[218:219]\n"
      "s_cmp_lg_u32 s66, 7\n"
      "v_cvt_pk_bf16_f32 v208, v208, v209\n"
      "v_cvt_pk_bf16_f32 v209, v210, v211\n"
      "s_cbranch_scc1 .Lq2d_77\n"
      "buffer_store_dwordx2 v[208:209], v171, s[4:7], s94 offen sc1\n"
      ".Lq2d_77:\n"
      ".Lwe_78:\n"
      ".Lwt_79:\n"
      "s_memrealtime s[64:65]\n"
      "ds_read_b32 v253, v252 offset:24408\n"
      "v_mov_b32 v254, 1\n"
      "v_mov_b32 v254, 1\n"
      "v_mov_b32 v254, 1\n"
      "v_mov_b32 v254, 1\n"
      "v_mov_b32 v254, 1\n"
      "v_mov_b32 v254, 1\n"
      "v_mov_b32 v254, 1\n"
      "v_mov_b32 v254, 1\n"
      "s_waitcnt lgkmcnt(0)\n"
      "v_readfirstlane_b32 s61, v253\n"
      "s_sub_i32 s64, s64, s99\n"
      "s_sub_i32 s65, s61, s64\n"
      "s_cmp_gt_i32 s65, 16\n"
      "s_cbranch_scc1 .Lwt_79\n"
      "s_add_i32 s62, s61, s99\n"
      "s_cmp_lt_i32 s65, s78\n"
      "s_cbranch_scc1 .Lws_80\n"
      ".Lwf_81:\n"
      "s_memrealtime s[64:65]\n"
      "v_mov_b32 v253, v254\n"
      "v_mov_b32 v253, v254\n"
      "v_mov_b32 v253, v254\n"
      "v_mov_b32 v253, v254\n"
      "v_mov_b32 v253, v254\n"
      "v_mov_b32 v253, v254\n"
      "v_mov_b32 v253, v254\n"
      "v_mov_b32 v253, v254\n"
      "s_waitcnt lgkmcnt(0)\n"
      "s_sub_i32 s65, s62, s64\n"
      "s_cmp_gt_i32 s65, 3\n"
      "s_cbranch_scc1 .Lwf_81\n"
      "s_max_i32 s65, s65, 0\n"
      ".Lwd_82:\n"
      "s_sub_u32 s65, s65, 1\n"
      "s_cbranch_scc0 .Lwd_82\n"
      "buffer_load_dwordx4 v[208:211], v3, s[4:7], s95 offen sc1\n"
      "buffer_load_dwordx4 v[212:215], v3, s[4:7], s95 offen offset:1024 sc1\n"
      "buffer_load_dwordx4 v[216:219], v3, s[4:7], s95 offen offset:2048 sc1\n"
      "buffer_load_dwordx4 v[220:223], v3, s[4:7], s95 offen offset:3072 sc1\n"
      "v_lshrrev_b32 v253, 2, v1\n"
      "v_lshlrev_b32 v6, 0, v253\n"
      "v_lshl_add_u32 v6, v2, 2, v6\n"
      "v_lshrrev_b32 v253, 3, v6\n"
      "v_lshlrev_b32 v5, 0, v253\n"
      "v_and_b32_e64 v253, 7, v6\n"
      "v_lshl_add_u32 v5, v253, 1, v5\n"
      "v_mul_u32_u24 v5, 1040, v5\n"
      "s_lshl_b32 s66, s48, 7\n"
      "v_and_b32 v253, 3, v1\n"
      "v_lshl_add_u32 v5, v253, 3, v5\n"
      "v_add_u32_e64 v5, s66, v5\n"
      "v_add_u32 v6, 66560, v5\n"
      "ds_read_b64_tr_b16 v[144:145], v5 offset:0\n"
      "ds_read_b64_tr_b16 v[148:149], v5 offset:32\n"
      "ds_read_b64_tr_b16 v[152:153], v5 offset:64\n"
      "ds_read_b64_tr_b16 v[156:157], v5 offset:96\n"
      "ds_read_b64_tr_b16 v[146:147], v5 offset:16640\n"
      "ds_read_b64_tr_b16 v[150:151], v5 offset:16672\n"
      "ds_read_b64_tr_b16 v[154:155], v5 offset:16704\n"
      "ds_read_b64_tr_b16 v[158:159], v5 offset:16736\n"
      "ds_read_b64_tr_b16 v[160:161], v5 offset:33280\n"
      "ds_read_b64_tr_b16 v[164:165], v5 offset:33312\n"
      "ds_read_b64_tr_b16 v[168:169], v5 offset:33344\n"
      "ds_read_b64_tr_b16 v[172:173], v5 offset:33376\n"
      "ds_read_b64_tr_b16 v[162:163], v5 offset:49920\n"
      "ds_read_b64_tr_b16 v[166:167], v5 offset:49952\n"
      "ds_read_b64_tr_b16 v[170:171], v5 offset:49984\n"
      "ds_read_b64_tr_b16 v[174:175], v5 offset:50016\n"
      "ds_read_b64_tr_b16 v[176:177], v6 offset:0\n"
      "ds_read_b64_tr_b16 v[180:181], v6 offset:32\n"
      "ds_read_b64_tr_b16 v[184:185], v6 offset:64\n"
      "ds_read_b64_tr_b16 v[188:189], v6 offset:96\n"
      "ds_read_b64_tr_b16 v[178:179], v6 offset:16640\n"
      "ds_read_b64_tr_b16 v[182:183], v6 offset:16672\n"
      "ds_read_b64_tr_b16 v[186:187], v6 offset:16704\n"
      "ds_read_b64_tr_b16 v[190:191], v6 offset:16736\n"
      "ds_read_b64_tr_b16 v[192:193], v6 offset:33280\n"
      "ds_read_b64_tr_b16 v[196:197], v6 offset:33312\n"
      "ds_read_b64_tr_b16 v[200:201], v6 offset:33344\n"
      "ds_read_b64_tr_b16 v[204:205], v6 offset:33376\n"
      "ds_read_b64_tr_b16 v[194:195], v6 offset:49920\n"
      "ds_read_b64_tr_b16 v[198:199], v6 offset:49952\n"
      "ds_read_b64_tr_b16 v[202:203], v6 offset:49984\n"
      "ds_read_b64_tr_b16 v[206:207], v6 offset:50016\n"
      "v_mov_b32 v224, 0\n"
      "s_waitcnt vmcnt(3)\n"
      "ds_write_b128 v7, v[208:211]\n"
      "v_max3_u32 v224, v224, v208, v209\n"
      "v_max3_u32 v224, v224, v210, v211\n"
      "s_waitcnt vmcnt(2)\n"
      "ds_write_b128 v7, v[212:215] offset:1040\n"
      "v_max3_u32 v224, v224, v212, v213\n"
      "v_max3_u32 v224, v224, v214, v215\n"
      "s_waitcnt vmcnt(1)\n"
      "ds_write_b128 v7, v[216:219] offset:2080\n"
      "v_max3_u32 v224, v224, v216, v217\n"
      "v_max3_u32 v224, v224, v218, v219\n"
      "s_waitcnt vmcnt(0)\n"
      "ds_write_b128 v7, v[220:223] offset:3120\n"
      "v_max3_u32 v224, v224, v220, v221\n"
      "v_max3_u32 v224, v224, v222, v223\n"
      "v_cmp_eq_u32 vcc, -1, v224\n"
      "s_cbranch_vccnz .Lsl_83\n"
      ".Lok_84:\n"
      "s_waitcnt lgkmcnt(0)\n"
      "s_barrier\n"
      "s_setprio 0\n"
      "s_lshl_b32 s66, s58, 13\n"
      "s_xor_b32 s66, s66, s75\n"
      "v_lshlrev_b32 v5, 20, v2\n"
      "v_lshrrev_b32 v253, 1, v2\n"
      "v_lshl_add_u32 v5, v253, 13, v5\n"
      "v_lshl_add_u32 v5, v1, 3, v5\n"
      "v_xor_b32 v5, s66, v5\n"
      "v_lshrrev_b32 v253, 3, v1\n"
      "v_lshlrev_b32_e64 v6, 14, v253\n"
      "v_bfe_u32 v253, v1, 2, 1\n"
      "v_lshl_add_u32 v6, v253, 12, v6\n"
      "v_and_b32_e64 v253, 3, v1\n"
      "v_lshl_add_u32 v6, v253, 3, v6\n"
      ".Lcw_85:\n"
      "ds_read_b32 v208, v252 offset:24372\n"
      "v_mov_b32 v253, v254\n"
      "v_mov_b32 v253, v254\n"
      "v_mov_b32 v253, v254\n"
      "v_mov_b32 v253, v254\n"
      "v_mov_b32 v253, v254\n"
      "v_mov_b32 v253, v254\n"
      "v_mov_b32 v253, v254\n"
      "v_mov_b32 v253, v254\n"
      "s_waitcnt lgkmcnt(0)\n"
      "v_cmp_gt_i32 vcc, 8, v208\n"
      "v_mov_b32 v253, v254\n"
      "v_mov_b32 v253, v254\n"
      "v_mov_b32_e64 v253, v254\n"
      "v_mov_b32_e64 v253, v254\n"
      "s_cbranch_vccnz .Lcw_85\n"
      "s_setprio 2\n"
      "s_barrier\n"
      "ds_read_b128 v[208:211], v14 offset:16640\n"
      "ds_read_b128 v[212:215], v14 offset:17664\n"
      "ds_read_b128 v[216:219], v14 offset:18688\n"
      "ds_read_b128 v[220:223], v14 offset:19712\n"
      "s_waitcnt lgkmcnt(3)\n"
      "v_mfma_f32_16x16x32_bf16 v[224:227], v[208:211], v[144:147], 0\n"
      "v_mfma_f32_16x16x32_bf16 v[228:231], v[208:211], v[148:151], 0\n"
      "v_mfma_f32_16x16x32_bf16 v[232:235], v[208:211], v[152:155], 0\n"
      "v_mfma_f32_16x16x32_bf16 v[236:239], v[208:211], v[156:159], 0\n"
      "s_waitcnt lgkmcnt(2)\n"
      "v_mfma_f32_16x16x32_bf16 v[224:227], v[212:215], v[160:163], v[224:227]\n"
      "v_mfma_f32_16x16x32_bf16 v[228:231], v[212:215], v[164:167], v[228:231]\n"
      "v_mfma_f32_16x16x32_bf16 v[232:235], v[212:215], v[168:171], v[232:235]\n"
      "v_mfma_f32_16x16x32_bf16 v[236:239], v[212:215], v[172:175], v[236:239]\n"
      "s_waitcnt lgkmcnt(1)\n"
      "v_mfma_f32_16x16x32_bf16 v[224:227], v[216:219], v[176:179], v[224:227]\n"
      "v_mfma_f32_16x16x32_bf16 v[228:231], v[216:219], v[180:183], v[228:231]\n"
      "v_mfma_f32_16x16x32_bf16 v[232:235], v[216:219], v[184:187], v[232:235]\n"
      "v_mfma_f32_16x16x32_bf16 v[236:239], v[216:219], v[188:191], v[236:239]\n"
      "s_waitcnt lgkmcnt(0)\n"
      "v_mfma_f32_16x16x32_bf16 v[224:227], v[220:223], v[192:195], v[224:227]\n"
      "v_mfma_f32_16x16x32_bf16 v[228:231], v[220:223], v[196:199], v[228:231]\n"
      "v_mfma_f32_16x16x32_bf16 v[232:235], v[220:223], v[200:203], v[232:235]\n"
      "v_mfma_f32_16x16x32_bf16 v[236:239], v[220:223], v[204:207], v[236:239]\n"
      "s_add_i32 s67, s88, 262144\n"
      "s_add_i32 s68, s88, 524288\n"
      "s_add_i32 s69, s88, 786432\n"
      "s_nop 2\n"
      "v_cvt_pk_bf16_f32 v144, v224, v228\n"
      "v_cvt_pk_bf16_f32 v146, v225, v229\n"
      "v_cvt_pk_bf16_f32 v145, v232, v236\n"
      "buffer_store_dwordx2 v[144:145], v5, s[4:7], s88 offen sc1\n"
      "v_cvt_pk_bf16_f32 v147, v233, v237\n"
      "buffer_store_dwordx2 v[146:147], v5, s[4:7], s67 offen sc1\n"
      "v_cvt_pk_bf16_f32 v148, v226, v230\n"
      "v_cvt_pk_bf16_f32 v149, v234, v238\n"
      "buffer_store_dwordx2 v[148:149], v5, s[4:7], s68 offen sc1\n"
      "v_cvt_pk_bf16_f32 v150, v227, v231\n"
      "v_cvt_pk_bf16_f32 v151, v235, v239\n"
      "buffer_store_dwordx2 v[150:151], v5, s[4:7], s69 offen sc1\n"
      "s_setprio 0\n"
      "v_lshrrev_b32 v253, 2, v1\n"
      "v_lshlrev_b32 v160, 8, v253\n"
      "v_lshrrev_b32 v253, 1, v2\n"
      "v_lshl_add_u32 v160, v253, 7, v160\n"
      "v_and_b32_e64 v253, 3, v1\n"
      "v_lshl_add_u32 v160, v253, 5, v160\n"
      "v_and_b32 v253, 1, v2\n"
      "v_lshl_add_u32 v160, v253, 4, v160\n"
      "s_lshl_b32 s66, s49, 2\n"
      "v_lshl_add_u32 v161, v1, 6, v252\n"
      "v_lshl_add_u32 v161, v2, 4, v161\n"
      "v_add_u32 v161, s66, v161\n"
      "v_lshrrev_b32 v253, 3, v1\n"
      "v_lshl_add_u32 v162, v253, 9, v252\n"
      "v_lshl_add_u32 v162, v2, 7, v162\n"
      "v_and_b32 v253, 7, v1\n"
      "v_lshl_add_u32 v162, v253, 4, v162\n"
      "v_lshrrev_b32 v253, 1, v2\n"
      "v_lshl_add_u32 v163, v253, 4, v252\n"
      "v_add_u32 v163, s66, v163\n"
      "v_lshrrev_b32 v253, 3, v1\n"
      "v_lshl_add_u32 v164, v253, 4, v252\n"
      "v_lshrrev_b32 v253, 2, v1\n"
      "v_lshlrev_b32 v165, 15, v253\n"
      "v_and_b32_e64 v253, 3, v1\n"
      "v_lshl_add_u32 v165, v253, 4, v165\n"
      "v_mov_b32 v172, 0x39000000\n"
      "v_mov_b32 v173, 0x3b000000\n"
      "s_lshl_b32 s66, s49, 6\n"
      "v_lshl_add_u32 v166, v2, 4, v252\n"
      "v_and_b32 v253, 7, v1\n"
      "v_lshl_add_u32 v166, v253, 1, v166\n"
      "v_add_u32 v166, s66, v166\n"
      "v_lshrrev_b32 v253, 3, v1\n"
      "v_lshlrev_b32 v168, 12, v253\n"
      "v_lshl_add_u32 v168, v2, 6, v168\n"
      "v_and_b32 v253, 7, v1\n"
      "v_lshl_add_u32 v168, v253, 3, v168\n"
      "v_lshlrev_b32 v170, 7, v2\n"
      "v_and_b32 v253, 3, v1\n"
      "v_lshl_add_u32 v170, v253, 5, v170\n"
      "v_lshrrev_b32_e64 v253, 2, v1\n"
      "v_lshl_add_u32 v170, v253, 3, v170\n"
      "v_lshrrev_b32 v253, 2, v1\n"
      "v_lshl_add_u32 v167, v253, 6, v252\n"
      "v_lshl_add_u32 v167, v2, 4, v167\n"
      "v_and_b32 v253, 1, v2\n"
      "v_lshlrev_b32 v171, 16, v253\n"
      "v_lshrrev_b32 v253, 1, v2\n"
      "v_lshl_add_u32 v171, v253, 13, v171\n"
      "v_lshl_add_u32 v171, v1, 3, v171\n"
      "v_xor_b32_e64 v171, s75, v171\n"
      "s_add_i32 s96, s96, 0x400000\n"
      "s_add_i32 s97, s97, 0x400000\n"
      "s_add_i32 s98, s98, 0x400000\n"
      "v_lshlrev_b32_e64 v166, 4, v2\n"
      "v_and_b32 v253, 7, v1\n"
      "v_lshl_add_u32 v166, v253, 1, v166\n"
      "s_lshl_b32 s69, s49, 10\n"
      "s_lshl_b32 s71, s54, 6\n"
      "s_add_i32 s69, s69, s71\n"
      "s_mov_b32 s99, 630\n"
      "v_mov_b32 v232, 0x3f803f80\n"
      "v_mov_b32 v233, 0x3f803f80\n"
      "v_mov_b32 v234, 0x3f803f80\n"
      "v_mov_b32 v235, 0x3f803f80\n"
      ".Lcw_88:\n"
      "ds_read_b32 v208, v252 offset:24384\n"
      "v_mov_b32 v253, v254\n"
      "v_mov_b32 v253, v254\n"
      "v_mov_b32 v253, v254\n"
      "v_mov_b32 v253, v254\n"
      "v_mov_b32 v253, v254\n"
      "v_mov_b32 v253, v254\n"
      "v_mov_b32 v253, v254\n"
      "v_mov_b32 v253, v254\n"
      "s_waitcnt lgkmcnt(0)\n"
      "v_cmp_gt_i32 vcc, 2, v208\n"
      "v_mov_b32 v253, v254\n"
      "v_mov_b32 v253, v254\n"
      "v_mov_b32 v253, v254\n"
      "v_mov_b32 v253, v254\n"
      "s_cbranch_vccnz .Lcw_88\n"
      "s_setprio 2\n"
      "s_cmp_gt_u32 s51, 27\n"
      "s_cbranch_scc1 .Lnf_86\n"
      ".Lwe_89:\n"
      ".Lwt_90:\n"
      "s_memrealtime s[64:65]\n"
      "ds_read_b32 v253, v252 offset:24408\n"
      "v_mov_b32 v254, 1\n"
      "v_mov_b32 v254, 1\n"
      "v_mov_b32 v254, 1\n"
      "v_mov_b32 v254, 1\n"
      "v_mov_b32 v254, 1\n"
      "v_mov_b32 v254, 1\n"
      "v_mov_b32 v254, 1\n"
      "v_mov_b32 v254, 1\n"
      "s_waitcnt lgkmcnt(0)\n"
      "v_readfirstlane_b32 s61, v253\n"
      "s_sub_i32 s64, s64, s99\n"
      "s_sub_i32 s65, s61, s64\n"
      "s_cmp_gt_i32 s65, 16\n"
      "s_cbranch_scc1 .Lwt_90\n"
      "s_add_i32 s62, s61, s99\n"
      "s_cmp_lt_i32 s65, s78\n"
      "s_cbranch_scc1 .Lws_91\n"
      ".Lwf_92:\n"
      "s_memrealtime s[64:65]\n"
      "v_mov_b32 v253, v254\n"
      "v_mov_b32 v253, v254\n"
      "v_mov_b32 v253, v254\n"
      "v_mov_b32 v253, v254\n"
      "v_mov_b32 v253, v254\n"
      "v_mov_b32 v253, v254\n"
      "v_mov_b32 v253, v254\n"
      "v_mov_b32 v253, v254\n"
      "s_waitcnt lgkmcnt(0)\n"
      "s_sub_i32 s65, s62, s64\n"
      "s_cmp_gt_i32 s65, 3\n"
      "s_cbranch_scc1 .Lwf_92\n"
      "s_max_i32 s65, s65, 0\n"
      ".Lwd_93:\n"
      "s_sub_u32 s65, s65, 1\n"
      "s_cbranch_scc0 .Lwd_93\n"
      "buffer_load_dwordx4 v[144:147], v160, s[4:7], s96 offen sc1\n"
      "v_mov_b32 v253, v254\n"
      "v_mov_b32 v253, v254\n"
      "v_mov_b32_e64 v253, v254\n"
      "buffer_load_dwordx4 v[148:151], v160, s[4:7], s96 offen offset:1024 sc1\n"
      "v_mov_b32 v253, v254\n"
      "v_mov_b32 v253, v254\n"
      "v_mov_b32 v253, v254\n"
      "buffer_load_dwordx4 v[152:155], v160, s[4:7], s96 offen offset:2048 sc1\n"
      "v_mov_b32 v253, v254\n"
      "v_mov_b32 v253, v254\n"
      "v_mov_b32 v253, v254\n"
      "buffer_load_dwordx4 v[156:159], v160, s[4:7], s96 offen offset:3072 sc1\n"
      "v_mov_b32 v243, 0\n"
      "s_waitcnt vmcnt(3)\n"
      "v_mfma_f32_16x16x32_bf16 v[224:227], v[144:147], v[144:147], 0\n"
      "v_mfma_f32_16x16x32_bf16 v[228:231], v[144:147], v[232:235], 0\n"
      "v_mfma_f32_16x16x32_bf16 v[208:211], v[144:147], v[48:51], 0\n"
      "v_max3_u32 v243, v243, v144, v145\n"
      "v_max3_u32 v243, v243, v146, v147\n"
      "v_mfma_f32_16x16x32_bf16 v[212:215], v[144:147], v[52:55], 0\n"
      "v_mfma_f32_16x16x32_bf16 v[216:219], v[144:147], v[56:59], 0\n"
      "v_mfma_f32_16x16x32_bf16 v[220:223], v[144:147], v[60:63], 0\n"
      "s_waitcnt vmcnt(2)\n"
      "v_mfma_f32_16x16x32_bf16 v[224:227], v[148:151], v[148:151], v[224:227]\n"
      "v_mfma_f32_16x16x32_bf16 v[228:231], v[148:151], v[232:235], v[228:231]\n"
      "v_mfma_f32_16x16x32_bf16 v[208:211], v[148:151], v[64:67], v[208:211]\n"
      "v_max3_u32 v243, v243, v148, v149\n"
      "v_max3_u32 v243, v243, v150, v151\n"
      "v_mfma_f32_16x16x32_bf16 v[212:215], v[148:151], v[68:71], v[212:215]\n"
      "v_mfma_f32_16x16x32_bf16 v[216:219], v[148:151], v[72:75], v[216:219]\n"
      "v_mfma_f32_16x16x32_bf16 v[220:223], v[148:151], v[76:79], v[220:223]\n"
      "s_waitcnt vmcnt(1)\n"
      "v_mfma_f32_16x16x32_bf16 v[224:227], v[152:155], v[152:155], v[224:227]\n"
      "v_mfma_f32_16x16x32_bf16 v[228:231], v[152:155], v[232:235], v[228:231]\n"
      "v_mfma_f32_16x16x32_bf16 v[208:211], v[152:155], v[80:83], v[208:211]\n"
      "v_max3_u32 v243, v243, v152, v153\n"
      "v_max3_u32 v243, v243, v154, v155\n"
      "v_mfma_f32_16x16x32_bf16 v[212:215], v[152:155], v[84:87], v[212:215]\n"
      "v_mfma_f32_16x16x32_bf16 v[216:219], v[152:155], v[88:91], v[216:219]\n"
      "v_mfma_f32_16x16x32_bf16 v[220:223], v[152:155], v[92:95], v[220:223]\n"
      "s_waitcnt vmcnt(0)\n"
      "v_mfma_f32_16x16x32_bf16 v[224:227], v[156:159], v[156:159], v[224:227]\n"
      "v_mfma_f32_16x16x32_bf16 v[228:231], v[156:159], v[232:235], v[228:231]\n"
      "v_mfma_f32_16x16x32_bf16 v[208:211], v[156:159], v[96:99], v[208:211]\n"
      "v_max3_u32 v243, v243, v156, v157\n"
      "v_max3_u32 v243, v243, v158, v159\n"
      "v_mfma_f32_16x16x32_bf16 v[212:215], v[156:159], v[100:103], v[212:215]\n"
      "v_cmp_eq_u32 vcc, -1, v243\n"
      "v_mfma_f32_16x16x32_bf16 v[216:219], v[156:159], v[104:107], v[216:219]\n"
      "s_nop 0\n"
      "v_pk_add_f32 v[224:225], v[224:225], v[226:227]\n"
      "v_mfma_f32_16x16x32_bf16 v[220:223], v[156:159], v[108:111], v[220:223]\n"
      "v_add_f32 v224, v224, v225\n"
      "v_pk_add_f32 v[228:229], v[228:229], v[230:231]\n"
      "v_add_f32 v228, v228, v229\n"
      "s_cbranch_vccnz .Lsl_94\n"
      ".Lok_95:\n"
      "v_pk_add_f32 v[208:209], v[208:209], v[210:211]\n"
      "v_add_f32 v208, v208, v209\n"
      "v_permlane32_swap_b32 v228, v224\n"
      "v_add_f32_e64 v228, v228, v224\n"
      "v_pk_add_f32 v[216:217], v[216:217], v[218:219]\n"
      "v_add_f32 v216, v216, v217\n"
      "v_mov_b32 v224, v228\n"
      "v_pk_add_f32 v[212:213], v[212:213], v[214:215]\n"
      "v_add_f32 v212, v212, v213\n"
      "v_permlane16_swap_b32 v228, v224\n"
      "v_add_f32 v228, v228, v224\n"
      "v_pk_add_f32 v[220:221], v[220:221], v[222:223]\n"
      "v_add_f32 v220, v220, v221\n"
      "v_add_f32_dpp v228, v228, v228 quad_perm:[1,0,3,2] row_mask:0xf bank_mask:0xf\n"
      "v_permlane32_swap_b32 v208, v216\n"
      "v_add_f32 v208, v208, v216\n"
      "v_add_f32_dpp v228, v228, v228 quad_perm:[2,3,0,1] row_mask:0xf bank_mask:0xf\n"
      "v_permlane32_swap_b32 v212, v220\n"
      "v_add_f32 v212, v212, v220\n"
      "v_add_f32_dpp v228, v228, v228 row_half_mirror row_mask:0xf bank_mask:0xf\n"
      "s_nop 0\n"
      "v_permlane16_swap_b32 v208, v212\n"
      "v_add_f32_e64 v208, v208, v212\n"
      "v_add_f32_dpp v228, v228, v228 row_mirror row_mask:0xf bank_mask:0xf\n"
      "ds_write_b32 v161, v208 offset:22272\n"
      "s_mov_b32 exec_lo, 0x1\n"
      "s_mov_b32 exec_hi, 0x1\n"
      "ds_write_b32 v163, v228 offset:24320\n"
      "s_mov_b64 exec, 0x1\n"
      "ds_add_u32 v252, v254 offset:24360\n"
      "s_mov_b64 exec, -1\n"
      "v_mov_b32 v253, v254\n"
      "v_mov_b32 v253, v254\n"
      "v_mov_b32 v253, v254\n"
      "v_mov_b32 v253, v254\n"
      ".Lcw_96:\n"
      "ds_read_b32 v216, v252 offset:24360\n"
      "ds_read_b128 v[212:215], v164 offset:24320\n"
      "ds_read_b128 v[208:211], v162 offset:22272\n"
      "s_waitcnt lgkmcnt(0)\n"
      "v_cmp_gt_i32 vcc, 8, v216\n"
      "v_pk_add_f32 v[212:213], v[212:213], v[214:215]\n"
      "v_add_f32 v212, v212, v213\n"
      "v_pk_add_f32 v[208:209], v[208:209], v[210:211]\n"
      "v_add_f32 v208, v208, v209\n"
      "v_mul_f32_dpp v213, v212, v172 row_newbcast:0 row_mask:0xf bank_mask:0xf\n"
      "v_mul_f32_dpp v214, v212, v173 row_newbcast:8 row_mask:0xf bank_mask:0xf\n"
      "s_cbranch_vccnz .Lcw_96\n"
      "v_fma_f32 v214, -v213, v213, v214\n"
      "v_max_f32 v214, 0, v214\n"
      "v_add_f32 v214, 0x3727c5ac, v214\n"
      "v_rsq_f32 v214, v214\n"
      "v_fma_f32 v208, -v213, v4, v208\n"
      "v_mul_f32 v208, v208, v214\n"
      "v_mul_f32 v210, 0xbfb8aa3b, v208\n"
      "v_exp_f32_e64 v210, v210\n"
      "v_mul_f32_dpp v209, v208, v208 row_ror:8 row_mask:0xf bank_mask:0xf\n"
      "v_add_f32_e64 v210, 1.0, v210\n"
      "v_rcp_f32_e64 v210, v210\n"
      "s_nop 0\n"
      "v_mul_f32_e64 v209, v209, v210\n"
      "s_nop 1\n"
      "v_mov_b32_dpp v210, v209 row_shl:1 row_mask:0xf bank_mask:0xf bound_ctrl:1\n"
      "v_cvt_pk_bf16_f32 v240, v209, v210\n"
      "s_nop 1\n"
      "v_mov_b32_dpp v241, v240 row_shl:2 row_mask:0xf bank_mask:0xf bound_ctrl:1\n"
      "v_mov_b32_dpp v242, v240 row_shl:4 row_mask:0xf bank_mask:0xf bound_ctrl:1\n"
      "v_mov_b32_dpp v243, v240 row_shl:6 row_mask:0xf bank_mask:0xf bound_ctrl:1\n"
      "s_nop 1\n"
      "v_mfma_f32_16x16x32_bf16 v[208:211], v[240:243], v[112:115], 0\n"
      "v_mfma_f32_16x16x32_bf16 v[212:215], v[240:243], v[116:119], 0\n"
      "v_mfma_f32_16x16x32_bf16 v[216:219], v[240:243], v[120:123], 0\n"
      "v_mfma_f32_16x16x32_bf16 v[220:223], v[240:243], v[124:127], 0\n"
      "v_mfma_f32_16x16x32_bf16 v[224:227], v[240:243], v[128:131], 0\n"
      "v_mfma_f32_16x16x32_bf16 v[228:231], v[240:243], v[132:135], 0\n"
      "v_mfma_f32_16x16x32_bf16 v[232:235], v[240:243], v[136:139], 0\n"
      "v_mfma_f32_16x16x32_bf16 v[236:239], v[240:243], v[140:143], 0\n"
      "s_mov_b64 exec, 0xffff\n"
      "s_nop 0\n"
      "v_cvt_pk_bf16_f32 v144, v208, v212\n"
      "s_nop 0\n"
      "v_cvt_pk_bf16_f32 v145, v216, v220\n"
      "s_nop 0\n"
      "v_cvt_pk_bf16_f32 v146, v224, v228\n"
      "s_nop 0\n"
      "v_cvt_pk_bf16_f32 v147, v232, v236\n"
      "buffer_store_dwordx4 v[144:147], v165, s[4:7], s97 offen sc1\n"
      "s_mov_b64 exec, -1\n"
      ".Lq1a_87:\n"
      "s_cmp_lg_u32 s54, s55\n"
      "s_cbranch_scc1 .Lend\n"
      "s_mov_b32 s99, 728\n"
      "v_mov_b32 v156, 0\n"
      "v_mov_b32 v157, 0\n"
      "v_mov_b32 v158, 0\n"
      "v_mov_b32 v159, 0\n"
      ".Lwe_97:\n"
      ".Lwt_98:\n"
      "s_memrealtime s[64:65]\n"
      "ds_read_b32 v253, v252 offset:24408\n"
      "v_mov_b32 v254, 1\n"
      "v_mov_b32 v254, 1\n"
      "v_mov_b32 v254, 1\n"
      "v_mov_b32 v254, 1\n"
      "v_mov_b32 v254, 1\n"
      "v_mov_b32 v254, 1\n"
      "v_mov_b32 v254, 1\n"
      "v_mov_b32 v254, 1\n"
      "s_waitcnt lgkmcnt(0)\n"
      "v_readfirstlane_b32 s61, v253\n"
      "s_sub_i32 s64, s64, s99\n"
      "s_sub_i32 s65, s61, s64\n"
      "s_cmp_gt_i32 s65, 16\n"
      "s_cbranch_scc1 .Lwt_98\n"
      "s_add_i32 s62, s61, s99\n"
      "s_cmp_lt_i32 s65, s78\n"
      "s_cbranch_scc1 .Lws_99\n"
      ".Lwf_100:\n"
      "s_memrealtime s[64:65]\n"
      "v_mov_b32 v253, v254\n"
      "v_mov_b32 v253, v254\n"
      "v_mov_b32_e64 v253, v254\n"
      "v_mov_b32_e64 v253, v254\n"
      "v_mov_b32_e64 v253, v254\n"
      "v_mov_b32 v253, v254\n"
      "v_mov_b32_e64 v253, v254\n"
      "v_mov_b32_e64 v253, v254\n"
      "s_waitcnt lgkmcnt(0)\n"
      "s_sub_i32 s65, s62, s64\n"
      "s_cmp_gt_i32 s65, 3\n"
      "s_cbranch_scc1 .Lwf_100\n"
      "s_max_i32 s65, s65, 0\n"
      ".Lwd_101:\n"
      "s_sub_u32 s65, s65, 1\n"
      "s_cbranch_scc0 .Lwd_101\n"
      "buffer_load_dwordx4 v[144:147], v3, s[4:7], s98 offen sc1\n"
      "buffer_load_dwordx4 v[148:151], v3, s[4:7], s98 offen offset:1024 sc1\n"
      "buffer_load_dwordx4 v[152:155], v3, s[4:7], s98 offen offset:2048 sc1\n"
      "s_mov_b32 exec_hi, 15\n"
      "buffer_load_dwordx4 v[156:159], v3, s[4:7], s98 offen offset:3072 sc1\n"
      "s_mov_b64 exec, -1\n"
      "v_mov_b32_e64 v243, 0\n"
      "s_waitcnt vmcnt(3)\n"
      "v_mfma_f32_16x16x32_bf16 v[208:211], v[144:147], v[8:11], 0\n"
      "v_max3_u32 v243, v243, v144, v145\n"
      "v_max3_u32 v243, v243, v146, v147\n"
      "s_waitcnt vmcnt(2)\n"
      "v_mfma_f32_16x16x32_bf16 v[208:211], v[148:151], v[8:11], v[208:211]\n"
      "v_max3_u32 v243, v243, v148, v149\n"
      "v_max3_u32 v243, v243, v150, v151\n"
      "s_waitcnt vmcnt(1)\n"
      "v_mfma_f32_16x16x32_bf16 v[208:211], v[152:155], v[8:11], v[208:211]\n"
      "v_max3_u32 v243, v243, v152, v153\n"
      "v_max3_u32 v243, v243, v154, v155\n"
      "s_waitcnt vmcnt(0)\n"
      "v_mfma_f32_16x16x32_bf16 v[208:211], v[156:159], v[8:11], v[208:211]\n"
      "v_max3_u32 v243, v243, v156, v157\n"
      "v_max3_u32 v243, v243, v158, v159\n"
      "v_cmp_eq_u32 vcc, -1, v243\n"
      "s_nop 4\n"
      "v_permlane32_swap_b32 v208, v210\n"
      "v_add_f32 v208, v208, v210\n"
      "v_permlane32_swap_b32 v209, v211\n"
      "v_add_f32 v209, v209, v211\n"
      "s_cbranch_vccnz .Lsl_102\n"
      ".Lok_103:\n"
      "s_nop 0\n"
      "v_permlane16_swap_b32 v208, v209\n"
      "v_add_f32 v208, v208, v209\n"
      "v_cvt_pk_bf16_f32 v230, v208, v208\n"
      "s_mov_b32 exec_lo, 0xff00ff\n"
      "s_mov_b32 exec_hi, 0xff00ff\n"
      "buffer_store_short v230, v166, s[44:47], s69 offen\n"
      "s_mov_b64 exec, -1\n"
      "s_branch .Lend\n"
      ".Lsl_7:\n"
      "s_add_u32 s74, s74, 1\n"
      "s_cmp_gt_u32 s74, 0x186a0\n"
      "s_cbranch_scc1 .Lok_8\n"
      "s_sleep 2\n"
      "s_nop 9\n"
      "s_branch .Lwe_4\n"
      ".Lsl_15:\n"
      "s_add_u32 s74, s74, 1\n"
      "s_cmp_gt_u32 s74, 0x186a0\n"
      "s_cbranch_scc1 .Lok_16\n"
      "s_sleep 2\n"
      "s_nop 9\n"
      "s_branch .Lwe_10\n"
      ".Lsl_25:\n"
      "s_add_u32 s74, s74, 1\n"
      "s_cmp_gt_u32 s74, 0x186a0\n"
      "s_cbranch_scc1 .Lok_26\n"
      "s_sleep 2\n"
      "s_nop 9\n"
      "s_branch .Lwe_20\n"
      ".Lsw_36:\n"
      "s_lshl_b32 s70, s54, 18\n"
      "s_lshl_b32 s71, s49, 16\n"
      "s_add_i32 s70, s70, s71\n"
      "s_lshl_b32 s71, s55, 6\n"
      "s_add_i32 s70, s70, s71\n"
      "s_lshl_b32 s66, s49, 12\n"
      "s_add_i32 m0, s66, 133120\n"
      ".Lcw_104:\n"
      "ds_read_b32 v244, v252 offset:24416\n"
      "v_mov_b32 v253, v254\n"
      "v_mov_b32 v253, v254\n"
      "v_mov_b32 v253, v254\n"
      "v_mov_b32 v253, v254\n"
      "v_mov_b32 v253, v254\n"
      "v_mov_b32 v253, v254\n"
      "v_mov_b32 v253, v254\n"
      "v_mov_b32 v253, v254\n"
      "s_waitcnt lgkmcnt(0)\n"
      "v_cmp_gt_i32 vcc, 4, v244\n"
      "v_mov_b32 v253, v254\n"
      "v_mov_b32 v253, v254\n"
      "v_mov_b32 v253, v254\n"
      "v_mov_b32 v253, v254\n"
      "s_cbranch_vccnz .Lcw_104\n"
      "s_add_i32 s71, s61, -1714\n"
      ".Lpa_105:\n"
      "s_memrealtime s[64:65]\n"
      "v_mov_b32 v253, v254\n"
      "v_mov_b32 v253, v254\n"
      "v_mov_b32 v253, v254\n"
      "v_mov_b32 v253, v254\n"
      "v_mov_b32 v253, v254\n"
      "v_mov_b32 v253, v254\n"
      "v_mov_b32 v253, v254\n"
      "v_mov_b32 v253, v254\n"
      "s_waitcnt lgkmcnt(0)\n"
      "s_sub_i32 s65, s64, s71\n"
      "s_cmp_lt_i32 s65, 0\n"
      "s_cbranch_scc1 .Lpa_105\n"
      "buffer_load_dwordx4 v7, s[28:31], s70 offen lds\n"
      "s_addk_i32 m0, 1024\n"
      "s_add_i32 s70, s70, 0x4000\n"
      "buffer_load_dwordx4 v7, s[28:31], s70 offen lds\n"
      "s_addk_i32 m0, 1024\n"
      "s_add_i32 s70, s70, 0x4000\n"
      "buffer_load_dwordx4 v7, s[28:31], s70 offen lds\n"
      "s_addk_i32 m0, 1024\n"
      "s_add_i32 s70, s70, 0x4000\n"
      "buffer_load_dwordx4 v7, s[28:31], s70 offen lds\n"
      "s_waitcnt vmcnt(0)\n"
      "s_mov_b64 exec, 0x1\n"
      "ds_add_u32 v252, v254 offset:24420\n"
      "s_mov_b64 exec, -1\n"
      "s_branch .Lnw_35\n"
      ".Lsl_57:\n"
      "s_add_u32 s74, s74, 1\n"
      "s_cmp_gt_u32 s74, 0x186a0\n"
      "s_cbranch_scc1 .Lok_58\n"
      "s_sleep 2\n"
      "s_nop 9\n"
      "s_branch .Lwe_52\n"
      ".Lnf_46:\n"
      "s_cmp_gt_u32 s49, 1\n"
      "s_cbranch_scc1 .Lq1a_47\n"
      "s_bfe_u32 s71, s56, 0x10000\n"
      "s_lshl_b32 s70, s71, 17\n"
      "s_lshl_b32 s71, s60, 15\n"
      "s_add_i32 s70, s70, s71\n"
      "s_bfe_u32 s71, s50, 0x10002\n"
      "s_mul_i32 s71, s71, 24576\n"
      "s_add_i32 s70, s70, s71\n"
      "s_bfe_u32 s71, s50, 0x20000\n"
      "s_lshl_b32 s71, s71, 11\n"
      "s_add_i32 s70, s70, s71\n"
      "s_lshl_b32 s71, s49, 10\n"
      "s_add_i32 s70, s70, s71\n"
      "s_add_i32 s70, s70, 0xc0000\n"
      "s_bfe_u32 s71, s50, 0x20000\n"
      "s_lshl_b32 s68, s71, 16\n"
      "s_lshl_b32 s71, s49, 15\n"
      "s_add_i32 s68, s68, s71\n"
      "s_bfe_u32 s71, s60, 0x10001\n"
      "s_lshl_b32 s71, s71, 14\n"
      "s_add_i32 s68, s68, s71\n"
      "s_bfe_u32 s71, s50, 0x10002\n"
      "s_lshl_b32 s71, s71, 13\n"
      "s_add_i32 s68, s68, s71\n"
      "s_bfe_u32 s71, s60, 0x10000\n"
      "s_lshl_b32 s71, s71, 12\n"
      "s_add_i32 s68, s68, s71\n"
      "s_add_i32 s68, s68, 0x100e00\n"
      "s_xor_b32 s70, s70, s75\n"
      "s_xor_b32 s68, s68, s75\n"
      "v_lshlrev_b32 v6, 1, v1\n"
      "v_lshl_add_u32 v6, v2, 4, v6\n"
      ".Lwe_106:\n"
      "s_memrealtime s[64:65]\n"
      "v_mov_b32 v253, v254\n"
      "v_mov_b32 v253, v254\n"
      "v_mov_b32 v253, v254\n"
      "v_mov_b32 v253, v254\n"
      "v_mov_b32 v253, v254\n"
      "v_mov_b32 v253, v254\n"
      "v_mov_b32 v253, v254\n"
      "v_mov_b32 v253, v254\n"
      "s_waitcnt lgkmcnt(0)\n"
      "s_sub_i32 s64, s64, s99\n"
      "s_sub_i32 s65, s61, s64\n"
      "s_sub_i32 s65, s3, s65\n"
      "s_max_i32 s65, s65, 0\n"
      "s_add_i32 s99, s99, s65\n"
      "s_add_i32 s62, s61, s99\n"
      ".Lwf_109:\n"
      "s_memrealtime s[64:65]\n"
      "v_mov_b32 v253, v254\n"
      "v_mov_b32 v253, v254\n"
      "v_mov_b32 v253, v254\n"
      "v_mov_b32 v253, v254\n"
      "v_mov_b32 v253, v254\n"
      "v_mov_b32 v253, v254\n"
      "v_mov_b32 v253, v254\n"
      "v_mov_b32 v253, v254\n"
      "s_waitcnt lgkmcnt(0)\n"
      "s_sub_i32 s65, s62, s64\n"
      "s_cmp_gt_i32 s65, 3\n"
      "s_cbranch_scc1 .Lwf_109\n"
      "s_max_i32 s65, s65, 0\n"
      ".Lwd_110:\n"
      "s_sub_u32 s65, s65, 1\n"
      "s_cbranch_scc0 .Lwd_110\n"
      "buffer_load_dwordx4 v[144:147], v3, s[4:7], s70 offen sc1\n"
      "v_mov_b32 v216, 0\n"
      "s_waitcnt vmcnt(0)\n"
      "v_mfma_f32_16x16x32_bf16 v[208:211], v[144:147], v[8:11], 0\n"
      "v_max3_u32 v216, v216, v144, v145\n"
      "v_max3_u32 v216, v216, v146, v147\n"
      "v_cmp_eq_u32_e64 vcc, -1, v216\n"
      "s_cbranch_vccnz .Lsl_111\n"
      ".Lok_112:\n"
      "s_nop 3\n"
      "v_pk_add_f32 v[208:209], v[208:209], v[210:211]\n"
      "s_nop 1\n"
      "v_permlane16_swap_b32 v208, v209\n"
      "v_add_f32 v208, v208, v209\n"
      "s_nop 1\n"
      "v_mov_b32_dpp v209, v208 row_shl:1 row_mask:0xf bank_mask:0xf bound_ctrl:1\n"
      "v_cvt_pk_bf16_f32 v208, v208, v209\n"
      "s_mov_b32 exec_lo, 0x550055\n"
      "s_mov_b32 exec_hi, 0x550055\n"
      "buffer_store_dword v208, v6, s[4:7], s68 offen sc1\n"
      "s_mov_b64 exec, -1\n"
      "s_branch .Lq1a_47\n"
      ".Lws_63:\n"
      ".Lwg_113:\n"
      "s_add_i32 s61, s61, s79\n"
      "s_add_i32 s65, s65, s79\n"
      "s_cmp_lt_i32 s65, s78\n"
      "s_cbranch_scc1 .Lwg_113\n"
      "v_mov_b32 v253, s61\n"
      "s_mov_b64 exec, 0x1\n"
      "ds_max_u32 v252, v253 offset:24408\n"
      "s_mov_b64 exec, -1\n"
      "s_branch .Lwt_62\n"
      ".Lsl_66:\n"
      "s_add_u32 s74, s74, 1\n"
      "s_cmp_gt_u32 s74, 0x186a0\n"
      "s_cbranch_scc1 .Lok_67\n"
      "s_sleep 2\n"
      "s_nop 9\n"
      "s_branch .Lwe_61\n"
      ".Lws_71:\n"
      ".Lwg_114:\n"
      "s_add_i32 s61, s61, s79\n"
      "s_add_i32 s65, s65, s79\n"
      "s_cmp_lt_i32 s65, s78\n"
      "s_cbranch_scc1 .Lwg_114\n"
      "v_mov_b32 v253, s61\n"
      "s_mov_b64 exec, 0x1\n"
      "ds_max_u32 v252, v253 offset:24408\n"
      "s_mov_b64 exec, -1\n"
      "s_branch .Lwt_70\n"
      ".Lsl_75:\n"
      "s_add_u32 s74, s74, 1\n"
      "s_cmp_gt_u32 s74, 0x186a0\n"
      "s_cbranch_scc1 .Lok_76\n"
      "s_sleep 2\n"
      "s_nop 9\n"
      "s_branch .Lwe_69\n"
      ".Lws_80:\n"
      ".Lwg_115:\n"
      "s_add_i32 s61, s61, s79\n"
      "s_add_i32 s65, s65, s79\n"
      "s_cmp_lt_i32 s65, s78\n"
      "s_cbranch_scc1 .Lwg_115\n"
      "v_mov_b32_e64 v253, s61\n"
      "s_mov_b64 exec, 0x1\n"
      "ds_max_u32 v252, v253 offset:24408\n"
      "s_mov_b64 exec, -1\n"
      "s_branch .Lwt_79\n"
      ".Lsl_83:\n"
      "s_add_u32 s74, s74, 1\n"
      "s_cmp_gt_u32 s74, 0x186a0\n"
      "s_cbranch_scc1 .Lok_84\n"
      "s_sleep 2\n"
      "s_nop 9\n"
      "s_branch .Lwe_78\n"
      ".Lws_91:\n"
      ".Lwg_116:\n"
      "s_add_i32 s61, s61, s79\n"
      "s_add_i32 s65, s65, s79\n"
      "s_cmp_lt_i32 s65, s78\n"
      "s_cbranch_scc1 .Lwg_116\n"
      "v_mov_b32 v253, s61\n"
      "s_mov_b64 exec, 0x1\n"
      "ds_max_u32 v252, v253 offset:24408\n"
      "s_mov_b64 exec, -1\n"
      "s_branch .Lwt_90\n"
      ".Lsl_94:\n"
      "s_add_u32 s74, s74, 1\n"
      "s_cmp_gt_u32 s74, 0x186a0\n"
      "s_cbranch_scc1 .Lok_95\n"
      "s_sleep 2\n"
      "s_nop 9\n"
      "s_branch .Lwe_89\n"
      ".Lnf_86:\n"
      "s_cmp_gt_u32 s49, 1\n"
      "s_cbranch_scc1 .Lq1a_87\n"
      "s_bfe_u32 s71, s56, 0x10000\n"
      "s_lshl_b32 s70, s71, 17\n"
      "s_lshl_b32 s71, s60, 15\n"
      "s_add_i32 s70, s70, s71\n"
      "s_bfe_u32 s71, s50, 0x10002\n"
      "s_mul_i32 s71, s71, 24576\n"
      "s_add_i32 s70, s70, s71\n"
      "s_bfe_u32 s71, s50, 0x20000\n"
      "s_lshl_b32 s71, s71, 11\n"
      "s_add_i32 s70, s70, s71\n"
      "s_lshl_b32 s71, s49, 10\n"
      "s_add_i32 s70, s70, s71\n"
      "s_add_i32 s70, s70, 0x4c0000\n"
      "s_bfe_u32 s71, s50, 0x20000\n"
      "s_lshl_b32 s68, s71, 16\n"
      "s_lshl_b32 s71, s49, 15\n"
      "s_add_i32 s68, s68, s71\n"
      "s_bfe_u32 s71, s60, 0x10001\n"
      "s_lshl_b32 s71, s71, 14\n"
      "s_add_i32 s68, s68, s71\n"
      "s_bfe_u32 s71, s50, 0x10002\n"
      "s_lshl_b32 s71, s71, 13\n"
      "s_add_i32 s68, s68, s71\n"
      "s_bfe_u32 s71, s60, 0x10000\n"
      "s_lshl_b32 s71, s71, 12\n"
      "s_add_i32 s68, s68, s71\n"
      "s_add_i32 s68, s68, 0x500e00\n"
      "s_xor_b32 s70, s70, s75\n"
      "s_xor_b32 s68, s68, s75\n"
      "v_lshlrev_b32 v6, 1, v1\n"
      "v_lshl_add_u32 v6, v2, 4, v6\n"
      ".Lwe_117:\n"
      ".Lwt_118:\n"
      "s_memrealtime s[64:65]\n"
      "ds_read_b32 v253, v252 offset:24408\n"
      "v_mov_b32 v254, 1\n"
      "v_mov_b32 v254, 1\n"
      "v_mov_b32 v254, 1\n"
      "v_mov_b32 v254, 1\n"
      "v_mov_b32 v254, 1\n"
      "v_mov_b32 v254, 1\n"
      "v_mov_b32 v254, 1\n"
      "v_mov_b32 v254, 1\n"
      "s_waitcnt lgkmcnt(0)\n"
      "v_readfirstlane_b32 s61, v253\n"
      "s_sub_i32 s64, s64, s99\n"
      "s_sub_i32 s65, s61, s64\n"
      "s_cmp_gt_i32 s65, 16\n"
      "s_cbranch_scc1 .Lwt_118\n"
      "s_add_i32 s62, s61, s99\n"
      "s_cmp_lt_i32 s65, s78\n"
      "s_cbranch_scc1 .Lws_119\n"
      ".Lwf_120:\n"
      "s_memrealtime s[64:65]\n"
      "v_mov_b32 v253, v254\n"
      "v_mov_b32 v253, v254\n"
      "v_mov_b32 v253, v254\n"
      "v_mov_b32 v253, v254\n"
      "v_mov_b32 v253, v254\n"
      "v_mov_b32 v253, v254\n"
      "v_mov_b32 v253, v254\n"
      "v_mov_b32 v253, v254\n"
      "s_waitcnt lgkmcnt(0)\n"
      "s_sub_i32 s65, s62, s64\n"
      "s_cmp_gt_i32 s65, 3\n"
      "s_cbranch_scc1 .Lwf_120\n"
      "s_max_i32 s65, s65, 0\n"
      ".Lwd_121:\n"
      "s_sub_u32 s65, s65, 1\n"
      "s_cbranch_scc0 .Lwd_121\n"
      "buffer_load_dwordx4 v[144:147], v3, s[4:7], s70 offen sc1\n"
      "v_mov_b32_e64 v216, 0\n"
      "s_waitcnt vmcnt(0)\n"
      "v_mfma_f32_16x16x32_bf16 v[208:211], v[144:147], v[8:11], 0\n"
      "v_max3_u32 v216, v216, v144, v145\n"
      "v_max3_u32 v216, v216, v146, v147\n"
      "v_cmp_eq_u32_e64 vcc, -1, v216\n"
      "s_cbranch_vccnz .Lsl_122\n"
      ".Lok_123:\n"
      "s_nop 3\n"
      "v_pk_add_f32 v[208:209], v[208:209], v[210:211]\n"
      "s_nop 1\n"
      "v_permlane16_swap_b32 v208, v209\n"
      "v_add_f32 v208, v208, v209\n"
      "s_nop 1\n"
      "v_mov_b32_dpp v209, v208 row_shl:1 row_mask:0xf bank_mask:0xf bound_ctrl:1\n"
      "v_cvt_pk_bf16_f32 v208, v208, v209\n"
      "s_mov_b32 exec_lo, 0x550055\n"
      "s_mov_b32 exec_hi, 0x550055\n"
      "buffer_store_dword v208, v6, s[4:7], s68 offen sc1\n"
      "s_mov_b64 exec, -1\n"
      "s_branch .Lq1a_87\n"
      ".Lws_99:\n"
      ".Lwg_124:\n"
      "s_add_i32 s61, s61, s79\n"
      "s_add_i32 s65, s65, s79\n"
      "s_cmp_lt_i32 s65, s78\n"
      "s_cbranch_scc1 .Lwg_124\n"
      "v_mov_b32 v253, s61\n"
      "s_mov_b64 exec, 0x1\n"
      "ds_max_u32 v252, v253 offset:24408\n"
      "s_mov_b64 exec, -1\n"
      "s_branch .Lwt_98\n"
      ".Lsl_102:\n"
      "s_add_u32 s74, s74, 1\n"
      "s_cmp_gt_u32 s74, 0x186a0\n"
      "s_cbranch_scc1 .Lok_103\n"
      "s_sleep 2\n"
      "s_nop 9\n"
      "s_branch .Lwe_97\n"
      ".Lsl_111:\n"
      "s_add_u32 s74, s74, 1\n"
      "s_cmp_gt_u32 s74, 0x186a0\n"
      "s_cbranch_scc1 .Lok_112\n"
      "s_sleep 2\n"
      "s_nop 9\n"
      "s_branch .Lwe_106\n"
      ".Lws_119:\n"
      ".Lwg_125:\n"
      "s_add_i32 s61, s61, s79\n"
      "s_add_i32 s65, s65, s79\n"
      "s_cmp_lt_i32 s65, s78\n"
      "s_cbranch_scc1 .Lwg_125\n"
      "v_mov_b32 v253, s61\n"
      "s_mov_b64 exec, 0x1\n"
      "ds_max_u32 v252, v253 offset:24408\n"
      "s_mov_b64 exec, -1\n"
      "s_branch .Lwt_118\n"
      ".Lsl_122:\n"
      "s_add_u32 s74, s74, 1\n"
      "s_cmp_gt_u32 s74, 0x186a0\n"
      "s_cbranch_scc1 .Lok_123\n"
      "s_sleep 2\n"
      "s_nop 9\n"
      "s_branch .Lwe_117\n"
      ".Lhi:\n"
      "s_waitcnt lgkmcnt(0)\n"
      "s_sub_i32 s66, s82, s84\n"
      "s_min_i32 s66, s66, 2000\n"
      "s_sub_i32 s67, 0, s66\n"
      "s_lshr_b32 s69, s50, 2\n"
      "s_xor_b32 s69, s69, s49\n"
      "s_bitcmp1_b32 s69, 0\n"
      "s_cselect_b32 s66, s67, s66\n"
      "v_mov_b32 v5, s66\n"
      "s_mov_b64 exec, 0x1\n"
      "ds_add_u32 v252, v5 offset:24424\n"
      "s_mov_b64 exec, -1\n"
      ".Lpr_126:\n"
      "s_movk_i32 s78, -8\n"
      "s_movk_i32 s79, 88\n"
      "s_movk_i32 s3, 0\n"
      "s_and_b32 s58, s51, 1\n"
      "s_mul_i32 s59, s50, 7\n"
      "s_bfe_u32 s71, s51, 0x30002\n"
      "s_add_i32 s59, s59, s71\n"
      "s_and_b32 s60, s51, 3\n"
      "s_mov_b32 s72, 0x05040100\n"
      "s_mov_b32 s73, 0x07060302\n"
      "s_mov_b32 s74, 0\n"
      "v_and_b32 v253, 1, v1\n"
      "v_lshlrev_b32 v5, 4, v253\n"
      "v_mov_b32 v6, 0xffff\n"
      "v_lshlrev_b32 v5, v5, v6\n"
      "v_bfe_u32 v6, v1, 1, 2\n"
      "v_cmp_eq_u32_e64 vcc, 0, v6\n"
      "s_nop 1\n"
      "v_cndmask_b32_e64 v12, 0, v5, vcc\n"
      "v_and_b32 v8, 0x3f803f80, v12\n"
      "v_cmp_eq_u32_e64 vcc, 1, v6\n"
      "s_nop 1\n"
      "v_cndmask_b32 v13, 0, v5, vcc\n"
      "v_and_b32 v9, 0x3f803f80, v13\n"
      "v_cmp_eq_u32_e64 vcc, 2, v6\n"
      "s_nop 1\n"
      "v_cndmask_b32_e64 v14, 0, v5, vcc\n"
      "v_and_b32 v10, 0x3f803f80, v14\n"
      "v_cmp_eq_u32_e64 vcc, 3, v6\n"
      "s_nop 1\n"
      "v_cndmask_b32_e64 v15, 0, v5, vcc\n"
      "v_and_b32 v11, 0x3f803f80, v15\n"
      "s_lshl_b32 s88, s48, 15\n"
      "s_bfe_u32 s71, s56, 0x10001\n"
      "s_lshl_b32 s71, s71, 14\n"
      "s_add_i32 s88, s88, s71\n"
      "s_bfe_u32 s71, s56, 0x10000\n"
      "s_lshl_b32 s71, s71, 12\n"
      "s_add_i32 s88, s88, s71\n"
      "s_lshl_b32 s71, s57, 7\n"
      "s_add_i32 s88, s88, s71\n"
      "s_add_i32 s88, s88, 0x800000\n"
      "s_lshl_b32 s89, s58, 13\n"
      "s_lshl_b32 s71, s56, 10\n"
      "s_add_i32 s89, s89, s71\n"
      "s_bfe_u32 s71, s57, 0x30000\n"
      "s_lshl_b32 s71, s71, 7\n"
      "s_add_i32 s89, s89, s71\n"
      "s_bfe_u32 s71, s57, 0x20003\n"
      "s_lshl_b32 s71, s71, 5\n"
      "s_add_i32 s89, s89, s71\n"
      "s_add_i32 s89, s89, 0x60000\n"
      "s_bfe_u32 s71, s52, 0x40000\n"
      "s_lshl_b32 s90, s71, 18\n"
      "s_lshl_b32 s71, s53, 15\n"
      "s_add_i32 s90, s90, s71\n"
      "s_bfe_u32 s71, s49, 0x10001\n"
      "s_lshl_b32 s71, s71, 14\n"
      "s_add_i32 s90, s90, s71\n"
      "s_bfe_u32 s71, s52, 0x10004\n"
      "s_lshl_b32 s71, s71, 13\n"
      "s_add_i32 s90, s90, s71\n"
      "s_bfe_u32 s71, s49, 0x10000\n"
      "s_lshl_b32 s71, s71, 12\n"
      "s_add_i32 s90, s90, s71\n"
      "s_add_i32 s90, s90, 0x800000\n"
      "s_bfe_u32 s71, s52, 0x10003\n"
      "s_lshl_b32 s66, s71, 13\n"
      "s_xor_b32 s90, s90, s66\n"
      "s_bfe_u32 s71, s50, 0x10001\n"
      "s_lshl_b32 s91, s71, 14\n"
      "s_bfe_u32 s71, s50, 0x10002\n"
      "s_lshl_b32 s71, s71, 13\n"
      "s_add_i32 s91, s91, s71\n"
      "s_bfe_u32 s71, s50, 0x10000\n"
      "s_lshl_b32 s71, s71, 12\n"
      "s_add_i32 s91, s91, s71\n"
      "s_lshl_b32 s71, s49, 10\n"
      "s_add_i32 s91, s91, s71\n"
      "s_bfe_u32 s71, s52, 0x20000\n"
      "s_lshl_b32 s71, s71, 3\n"
      "s_add_i32 s91, s91, s71\n"
      "s_add_i32 s91, s91, 0x60000\n"
      "s_bfe_u32 s71, s52, 0x40000\n"
      "s_lshl_b32 s92, s71, 14\n"
      "s_bfe_u32 s71, s52, 0x10004\n"
      "s_lshl_b32 s71, s71, 13\n"
      "s_add_i32 s92, s92, s71\n"
      "s_lshl_b32 s71, s49, 11\n"
      "s_add_i32 s92, s92, s71\n"
      "s_lshl_b32 s71, s53, 8\n"
      "s_add_i32 s92, s92, s71\n"
      "s_add_i32 s92, s92, 0x80000\n"
      "s_bfe_u32 s71, s54, 0x30000\n"
      "s_lshl_b32 s93, s71, 15\n"
      "s_bfe_u32 s71, s54, 0x10003\n"
      "s_lshl_b32 s71, s71, 13\n"
      "s_add_i32 s93, s93, s71\n"
      "s_lshl_b32 s71, s49, 11\n"
      "s_add_i32 s93, s93, s71\n"
      "s_add_i32 s93, s93, 0x80000\n"
      "s_add_i32 s94, s93, 16384\n"
      "s_bfe_u32 s71, s55, 0x10003\n"
      "s_lshl_b32 s95, s71, 14\n"
      "s_bfe_u32 s71, s55, 0x30000\n"
      "s_lshl_b32 s71, s71, 10\n"
      "s_add_i32 s95, s95, s71\n"
      "s_bfe_u32 s71, s54, 0x20002\n"
      "s_lshl_b32 s71, s71, 8\n"
      "s_add_i32 s95, s95, s71\n"
      "s_bfe_u32 s71, s54, 0x20000\n"
      "s_lshl_b32 s71, s71, 5\n"
      "s_add_i32 s95, s95, s71\n"
      "s_add_i32 s95, s95, 0xc0000\n"
      "s_waitcnt vmcnt(6)\n"
      "buffer_load_dwordx4 v3, s[20:23], s68 offen lds\n"
      "s_addk_i32 m0, 1040\n"
      "s_addk_i32 s68, 1024\n"
      "s_waitcnt vmcnt(6)\n"
      "buffer_load_dwordx4 v3, s[20:23], s68 offen lds\n"
      "s_addk_i32 m0, 1040\n"
      "s_addk_i32 s68, 1024\n"
      "s_waitcnt vmcnt(6)\n"
      "buffer_load_dwordx4 v3, s[20:23], s68 offen lds\n"
      "s_addk_i32 m0, 1040\n"
      "s_addk_i32 s68, 1024\n"
      "s_waitcnt vmcnt(6)\n"
      "s_mov_b64 exec, 0x1\n"
      "ds_add_u32 v252, v254 offset:24388\n"
      "s_mov_b64 exec, -1\n"
      "buffer_load_dwordx4 v3, s[20:23], s68 offen lds\n"
      "s_addk_i32 m0, 1040\n"
      "s_addk_i32 s68, 1024\n"
      "s_waitcnt vmcnt(6)\n"
      "buffer_load_dwordx4 v3, s[20:23], s68 offen lds\n"
      "s_addk_i32 m0, 1040\n"
      "s_addk_i32 s68, 1024\n"
      "s_waitcnt vmcnt(6)\n"
      "buffer_load_dwordx4 v3, s[20:23], s68 offen lds\n"
      "s_addk_i32 m0, 1040\n"
      "s_addk_i32 s68, 1024\n"
      "s_waitcnt vmcnt(6)\n"
      "buffer_load_dwordx4 v3, s[20:23], s68 offen lds\n"
      "s_addk_i32 m0, 1040\n"
      "s_addk_i32 s68, 1024\n"
      "s_waitcnt vmcnt(6)\n"
      "buffer_load_dwordx4 v3, s[20:23], s68 offen lds\n"
      "s_addk_i32 m0, 1040\n"
      "s_addk_i32 s68, 1024\n"
      "s_waitcnt vmcnt(6)\n"
      "buffer_load_dwordx4 v3, s[20:23], s68 offen lds\n"
      "s_addk_i32 m0, 1040\n"
      "s_addk_i32 s68, 1024\n"
      "s_waitcnt vmcnt(6)\n"
      "buffer_load_dwordx4 v3, s[20:23], s68 offen lds\n"
      "s_addk_i32 m0, 1040\n"
      "s_addk_i32 s68, 1024\n"
      "s_waitcnt vmcnt(6)\n"
      "buffer_load_dwordx4 v3, s[20:23], s68 offen lds\n"
      "s_addk_i32 m0, 1040\n"
      "s_addk_i32 s68, 1024\n"
      "s_waitcnt vmcnt(6)\n"
      "buffer_load_dwordx4 v3, s[20:23], s68 offen lds\n"
      "s_addk_i32 m0, 1040\n"
      "s_addk_i32 s68, 1024\n"
      "s_waitcnt vmcnt(6)\n"
      "buffer_load_dwordx4 v3, s[20:23], s68 offen lds\n"
      "s_addk_i32 m0, 1040\n"
      "s_addk_i32 s68, 1024\n"
      "s_waitcnt vmcnt(6)\n"
      "buffer_load_dwordx4 v3, s[20:23], s68 offen lds\n"
      "s_addk_i32 m0, 1040\n"
      "s_addk_i32 s68, 1024\n"
      "s_waitcnt vmcnt(6)\n"
      "buffer_load_dwordx4 v3, s[20:23], s68 offen lds\n"
      "s_addk_i32 m0, 1040\n"
      "s_addk_i32 s68, 1024\n"
      "s_waitcnt vmcnt(6)\n"
      "buffer_load_dwordx4 v3, s[20:23], s68 offen lds\n"
      "s_addk_i32 m0, 1040\n"
      "s_addk_i32 s68, 1024\n"
      "s_waitcnt vmcnt(6)\n"
      "buffer_load_dwordx4 v3, s[20:23], s68 offen lds\n"
      "s_addk_i32 m0, 1040\n"
      "s_addk_i32 s68, 1024\n"
      "s_waitcnt vmcnt(6)\n"
      "buffer_load_dwordx4 v3, s[20:23], s68 offen lds\n"
      "s_addk_i32 m0, 1040\n"
      "s_addk_i32 s68, 1024\n"
      "s_waitcnt vmcnt(6)\n"
      "buffer_load_dwordx4 v3, s[20:23], s68 offen lds\n"
      "s_addk_i32 m0, 1040\n"
      "s_addk_i32 s68, 1024\n"
      "s_waitcnt vmcnt(6)\n"
      "buffer_load_dwordx4 v3, s[20:23], s68 offen lds\n"
      "s_addk_i32 m0, 1040\n"
      "s_addk_i32 s68, 1024\n"
      "s_waitcnt vmcnt(6)\n"
      "buffer_load_dwordx4 v3, s[20:23], s68 offen lds\n"
      "s_addk_i32 m0, 1040\n"
      "s_addk_i32 s68, 1024\n"
      "s_waitcnt vmcnt(6)\n"
      "buffer_load_dwordx4 v3, s[20:23], s68 offen lds\n"
      "s_addk_i32 m0, 1040\n"
      "s_addk_i32 s68, 1024\n"
      "s_waitcnt vmcnt(6)\n"
      "buffer_load_dwordx4 v3, s[20:23], s68 offen lds\n"
      "s_addk_i32 m0, 1040\n"
      "s_addk_i32 s68, 1024\n"
      "s_waitcnt vmcnt(6)\n"
      "buffer_load_dwordx4 v3, s[20:23], s68 offen lds\n"
      "s_addk_i32 m0, 1040\n"
      "s_addk_i32 s68, 1024\n"
      "s_waitcnt vmcnt(6)\n"
      "buffer_load_dwordx4 v3, s[20:23], s68 offen lds\n"
      "s_addk_i32 m0, 1040\n"
      "s_addk_i32 s68, 1024\n"
      "s_waitcnt vmcnt(6)\n"
      "buffer_load_dwordx4 v3, s[20:23], s68 offen lds\n"
      "s_addk_i32 m0, 1040\n"
      "s_addk_i32 s68, 1024\n"
      "s_waitcnt vmcnt(6)\n"
      "buffer_load_dwordx4 v3, s[20:23], s68 offen lds\n"
      "s_addk_i32 m0, 1040\n"
      "s_addk_i32 s68, 1024\n"
      "s_waitcnt vmcnt(6)\n"
      "buffer_load_dwordx4 v3, s[20:23], s68 offen lds\n"
      "s_addk_i32 m0, 1040\n"
      "s_addk_i32 s68, 1024\n"
      "s_waitcnt vmcnt(6)\n"
      "buffer_load_dwordx4 v3, s[20:23], s68 offen lds\n"
      "v_lshrrev_b32 v253, 3, v1\n"
      "v_lshlrev_b32 v248, 0, v253\n"
      "v_and_b32 v253, 7, v1\n"
      "v_lshl_add_u32 v248, v253, 1, v248\n"
      "v_mul_u32_u24 v248, 1040, v248\n"
      "s_mul_i32 s66, s49, 33280\n"
      "v_lshl_add_u32 v248, v2, 8, v248\n"
      "v_add_u32 v248, s66, v248\n"
      "s_waitcnt vmcnt(16)\n"
      "ds_read_b128 v[48:51], v248\n"
      "ds_read_b128 v[52:55], v248 offset:16\n"
      "ds_read_b128 v[56:59], v248 offset:32\n"
      "ds_read_b128 v[60:63], v248 offset:48\n"
      "ds_read_b128 v[64:67], v248 offset:64\n"
      "ds_read_b128 v[68:71], v248 offset:80\n"
      "ds_read_b128 v[72:75], v248 offset:96\n"
      "ds_read_b128 v[76:79], v248 offset:112\n"
      "ds_read_b128 v[80:83], v248 offset:128\n"
      "ds_read_b128 v[84:87], v248 offset:144\n"
      "ds_read_b128 v[88:91], v248 offset:160\n"
      "ds_read_b128 v[92:95], v248 offset:176\n"
      "ds_read_b128 v[96:99], v248 offset:192\n"
      "ds_read_b128 v[100:103], v248 offset:208\n"
      "ds_read_b128 v[104:107], v248 offset:224\n"
      "ds_read_b128 v[108:111], v248 offset:240\n"
      "v_lshrrev_b32 v253, 2, v1\n"
      "v_lshlrev_b32 v6, 0, v253\n"
      "v_lshl_add_u32 v6, v2, 2, v6\n"
      "v_lshrrev_b32 v253, 3, v6\n"
      "v_lshlrev_b32 v5, 0, v253\n"
      "v_and_b32_e64 v253, 7, v6\n"
      "v_lshl_add_u32 v5, v253, 1, v5\n"
      "v_mul_u32_u24 v5, 1040, v5\n"
      "s_lshl_b32 s66, s48, 7\n"
      "v_and_b32 v253, 3, v1\n"
      "v_lshl_add_u32 v5, v253, 3, v5\n"
      "v_add_u32 v5, s66, v5\n"
      "v_add_u32 v6, 66560, v5\n"
      "v_mul_u32_u24 v7, 1040, v1\n"
      "v_lshl_add_u32 v7, v2, 8, v7\n"
      "v_add_u32 v7, v7, v252\n"
      "v_lshl_add_u32 v251, v1, 4, v252\n"
      "s_lshl_b32 s66, s49, 2\n"
      "v_add_u32_e64 v250, s66, v251\n"
      "v_add_u32_e64 v4, v3, v252\n"
      "s_lshl_b32 s66, s49, 10\n"
      "v_add_u32 v249, s66, v4\n"
      ".Lcw_127:\n"
      "ds_read_b32 v240, v252 offset:24356\n"
      "ds_read_b32 v244, v252 offset:24428\n"
      "s_waitcnt lgkmcnt(0)\n"
      "v_cmp_gt_i32_e64 vcc, 4, v240\n"
      "s_cbranch_vccnz .Lcw_127\n"
      "v_readfirstlane_b32 s75, v244\n"
      "s_cmp_gt_i32 s75, 0\n"
      "s_cselect_b32 s75, 0x2000, 0\n"
      "s_xor_b32 s89, s89, s75\n"
      "s_xor_b32 s90, s90, s75\n"
      "s_xor_b32 s91, s91, s75\n"
      "s_xor_b32 s92, s92, s75\n"
      "s_xor_b32 s93, s93, s75\n"
      "s_xor_b32 s94, s94, s75\n"
      "s_xor_b32 s95, s95, s75\n"
      "s_setprio 2\n"
      "s_waitcnt vmcnt(0)\n"
      "ds_read_b128 v[112:115], v248 offset:16640\n"
      "ds_read_b128 v[116:119], v248 offset:16656\n"
      "ds_read_b128 v[120:123], v248 offset:16672\n"
      "ds_read_b128 v[124:127], v248 offset:16688\n"
      "ds_read_b128 v[128:131], v248 offset:16704\n"
      "ds_read_b128 v[132:135], v248 offset:16720\n"
      "ds_read_b128 v[136:139], v248 offset:16736\n"
      "ds_read_b128 v[140:143], v248 offset:16752\n"
      "ds_read_b128 v[144:147], v248 offset:16768\n"
      "ds_read_b128 v[148:151], v248 offset:16784\n"
      "ds_read_b128 v[152:155], v248 offset:16800\n"
      "ds_read_b128 v[156:159], v248 offset:16816\n"
      "ds_read_b128 v[160:163], v248 offset:16832\n"
      "ds_read_b128 v[164:167], v248 offset:16848\n"
      "ds_read_b128 v[168:171], v248 offset:16864\n"
      "ds_read_b128 v[172:175], v248 offset:16880\n"
      "s_waitcnt lgkmcnt(0)\n"
      "s_barrier\n"
      "ds_read_b128 v[176:179], v7\n"
      "ds_read_b128 v[180:183], v7 offset:16\n"
      "ds_read_b128 v[184:187], v7 offset:32\n"
      "ds_read_b128 v[188:191], v7 offset:48\n"
      "ds_read_b128 v[192:195], v7 offset:64\n"
      "ds_read_b128 v[196:199], v7 offset:80\n"
      "ds_read_b128 v[200:203], v7 offset:96\n"
      "ds_read_b128 v[204:207], v7 offset:112\n"
      "s_waitcnt lgkmcnt(7)\n"
      "v_mfma_f32_16x16x32_bf16 v[240:243], v[48:51], v[176:179], 0\n"
      "ds_read_b128 v[208:211], v7 offset:128\n"
      "v_mfma_f32_16x16x32_bf16 v[244:247], v[112:115], v[176:179], 0\n"
      "s_waitcnt lgkmcnt(7)\n"
      "v_mfma_f32_16x16x32_bf16 v[240:243], v[52:55], v[180:183], v[240:243]\n"
      "ds_read_b128 v[212:215], v7 offset:144\n"
      "v_mfma_f32_16x16x32_bf16 v[244:247], v[116:119], v[180:183], v[244:247]\n"
      "s_waitcnt lgkmcnt(7)\n"
      "v_mfma_f32_16x16x32_bf16 v[240:243], v[56:59], v[184:187], v[240:243]\n"
      "ds_read_b128 v[216:219], v7 offset:160\n"
      "v_mfma_f32_16x16x32_bf16 v[244:247], v[120:123], v[184:187], v[244:247]\n"
      "s_waitcnt lgkmcnt(7)\n"
      "v_mfma_f32_16x16x32_bf16 v[240:243], v[60:63], v[188:191], v[240:243]\n"
      "ds_read_b128 v[220:223], v7 offset:176\n"
      "v_mfma_f32_16x16x32_bf16 v[244:247], v[124:127], v[188:191], v[244:247]\n"
      "s_waitcnt lgkmcnt(7)\n"
      "v_mfma_f32_16x16x32_bf16 v[240:243], v[64:67], v[192:195], v[240:243]\n"
      "ds_read_b128 v[224:227], v7 offset:192\n"
      "v_mfma_f32_16x16x32_bf16 v[244:247], v[128:131], v[192:195], v[244:247]\n"
      "s_waitcnt lgkmcnt(7)\n"
      "v_mfma_f32_16x16x32_bf16 v[240:243], v[68:71], v[196:199], v[240:243]\n"
      "ds_read_b128 v[228:231], v7 offset:208\n"
      "v_mfma_f32_16x16x32_bf16 v[244:247], v[132:135], v[196:199], v[244:247]\n"
      "s_waitcnt lgkmcnt(7)\n"
      "v_mfma_f32_16x16x32_bf16 v[240:243], v[72:75], v[200:203], v[240:243]\n"
      "ds_read_b128 v[232:235], v7 offset:224\n"
      "v_mfma_f32_16x16x32_bf16 v[244:247], v[136:139], v[200:203], v[244:247]\n"
      "s_waitcnt lgkmcnt(7)\n"
      "v_mfma_f32_16x16x32_bf16 v[240:243], v[76:79], v[204:207], v[240:243]\n"
      "ds_read_b128 v[236:239], v7 offset:240\n"
      "v_mfma_f32_16x16x32_bf16 v[244:247], v[140:143], v[204:207], v[244:247]\n"
      "s_mov_b64 exec, 0x1\n"
      "ds_add_u32 v252, v254 offset:24392\n"
      "s_mov_b64 exec, -1\n"
      "s_waitcnt lgkmcnt(7)\n"
      "v_mfma_f32_16x16x32_bf16 v[240:243], v[80:83], v[208:211], v[240:243]\n"
      "v_mfma_f32_16x16x32_bf16 v[244:247], v[144:147], v[208:211], v[244:247]\n"
      "s_waitcnt lgkmcnt(6)\n"
      "v_mfma_f32_16x16x32_bf16 v[240:243], v[84:87], v[212:215], v[240:243]\n"
      "v_mfma_f32_16x16x32_bf16 v[244:247], v[148:151], v[212:215], v[244:247]\n"
      "s_waitcnt lgkmcnt(5)\n"
      "v_mfma_f32_16x16x32_bf16 v[240:243], v[88:91], v[216:219], v[240:243]\n"
      "v_mfma_f32_16x16x32_bf16 v[244:247], v[152:155], v[216:219], v[244:247]\n"
      "s_waitcnt lgkmcnt(4)\n"
      "v_mfma_f32_16x16x32_bf16 v[240:243], v[92:95], v[220:223], v[240:243]\n"
      "v_mfma_f32_16x16x32_bf16 v[244:247], v[156:159], v[220:223], v[244:247]\n"
      "s_waitcnt lgkmcnt(3)\n"
      "v_mfma_f32_16x16x32_bf16 v[240:243], v[96:99], v[224:227], v[240:243]\n"
      "v_mfma_f32_16x16x32_bf16 v[244:247], v[160:163], v[224:227], v[244:247]\n"
      "s_waitcnt lgkmcnt(2)\n"
      "v_mfma_f32_16x16x32_bf16 v[240:243], v[100:103], v[228:231], v[240:243]\n"
      "v_mfma_f32_16x16x32_bf16 v[244:247], v[164:167], v[228:231], v[244:247]\n"
      "s_waitcnt lgkmcnt(1)\n"
      "v_mfma_f32_16x16x32_bf16 v[240:243], v[104:107], v[232:235], v[240:243]\n"
      "v_mfma_f32_16x16x32_bf16 v[244:247], v[168:171], v[232:235], v[244:247]\n"
      "s_waitcnt lgkmcnt(0)\n"
      "v_mfma_f32_16x16x32_bf16 v[240:243], v[108:111], v[236:239], v[240:243]\n"
      "v_mfma_f32_16x16x32_bf16 v[244:247], v[172:175], v[236:239], v[244:247]\n"
      "s_nop 6\n"
      "v_max3_f32 v248, v240, v241, v242\n"
      "v_max3_f32 v248, v248, v243, v244\n"
      "v_max3_f32 v248, v248, v245, v246\n"
      "v_max_f32 v248, v248, v247\n"
      "v_mov_b32 v253, v248\n"
      "s_nop 1\n"
      "v_permlane16_swap_b32_e64 v248, v253\n"
      "v_max_f32 v248, v248, v253\n"
      "v_mov_b32_e64 v253, v248\n"
      "s_nop 1\n"
      "v_permlane32_swap_b32_e64 v248, v253\n"
      "v_max_f32_e64 v248, v248, v253\n"
      "s_mov_b64 exec, 0xffff\n"
      "ds_write_b32 v250, v248 offset:20736\n"
      "s_mov_b64 exec, 0x1\n"
      "ds_add_u32 v252, v254 offset:24372\n"
      "s_mov_b64 exec, -1\n"
      "s_nop 8\n"
      ".Lcw_128:\n"
      "ds_read_b32 v208, v252 offset:24372\n"
      "ds_read_b128 v[216:219], v251 offset:20736\n"
      "s_waitcnt lgkmcnt(0)\n"
      "v_cmp_gt_i32_e64 vcc, 4, v208\n"
      "v_max3_f32 v212, v216, v217, v218\n"
      "v_max_f32_e64 v212, v212, v219\n"
      "v_pk_add_f32 v[220:221], v[240:241], v[212:213] op_sel_hi:[1,0] neg_lo:[0,1] neg_hi:[0,1]\n"
      "v_pk_add_f32 v[222:223], v[242:243], v[212:213] op_sel_hi:[1,0] neg_lo:[0,1] neg_hi:[0,1]\n"
      "v_pk_add_f32 v[224:225], v[244:245], v[212:213] op_sel_hi:[1,0] neg_lo:[0,1] neg_hi:[0,1]\n"
      "v_pk_add_f32 v[226:227], v[246:247], v[212:213] op_sel_hi:[1,0] neg_lo:[0,1] neg_hi:[0,1]\n"
      "s_cbranch_vccnz .Lcw_128\n"
      "v_exp_f32 v220, v220\n"
      "v_exp_f32 v221, v221\n"
      "v_exp_f32 v222, v222\n"
      "v_exp_f32 v223, v223\n"
      "v_exp_f32 v224, v224\n"
      "v_exp_f32 v225, v225\n"
      "v_exp_f32 v226, v226\n"
      "v_exp_f32 v227, v227\n"
      "v_cvt_pk_bf16_f32 v220, v220, v221\n"
      "v_cvt_pk_bf16_f32 v221, v222, v223\n"
      "v_cvt_pk_bf16_f32 v222, v224, v225\n"
      "v_cvt_pk_bf16_f32 v223, v226, v227\n"
      "ds_write_b128 v249, v[220:223] offset:16640\n"
      "v_mov_b32 v216, 0x3f803f80\n"
      "v_mov_b32 v217, 0x3f803f80\n"
      "v_mov_b32 v218, 0x3f803f80\n"
      "v_mov_b32 v219, 0x3f803f80\n"
      "s_setprio 1\n"
      "s_waitcnt lgkmcnt(0)\n"
      "s_barrier\n"
      "ds_read_b128 v[176:179], v4 offset:16640\n"
      "ds_read_b128 v[180:183], v4 offset:17664\n"
      "ds_read_b128 v[184:187], v4 offset:18688\n"
      "ds_read_b128 v[188:191], v4 offset:19712\n"
      "ds_read_b64_tr_b16 v[48:49], v5 offset:0\n"
      "ds_read_b64_tr_b16 v[52:53], v5 offset:32\n"
      "ds_read_b64_tr_b16 v[56:57], v5 offset:64\n"
      "ds_read_b64_tr_b16 v[60:61], v5 offset:96\n"
      "ds_read_b64_tr_b16 v[50:51], v5 offset:16640\n"
      "ds_read_b64_tr_b16 v[54:55], v5 offset:16672\n"
      "ds_read_b64_tr_b16 v[58:59], v5 offset:16704\n"
      "ds_read_b64_tr_b16 v[62:63], v5 offset:16736\n"
      "ds_read_b64_tr_b16 v[64:65], v5 offset:33280\n"
      "ds_read_b64_tr_b16 v[68:69], v5 offset:33312\n"
      "ds_read_b64_tr_b16 v[72:73], v5 offset:33344\n"
      "ds_read_b64_tr_b16 v[76:77], v5 offset:33376\n"
      "ds_read_b64_tr_b16 v[66:67], v5 offset:49920\n"
      "ds_read_b64_tr_b16 v[70:71], v5 offset:49952\n"
      "ds_read_b64_tr_b16 v[74:75], v5 offset:49984\n"
      "ds_read_b64_tr_b16 v[78:79], v5 offset:50016\n"
      "ds_read_b64_tr_b16 v[80:81], v6 offset:0\n"
      "ds_read_b64_tr_b16 v[84:85], v6 offset:32\n"
      "ds_read_b64_tr_b16 v[88:89], v6 offset:64\n"
      "ds_read_b64_tr_b16 v[92:93], v6 offset:96\n"
      "ds_read_b64_tr_b16 v[82:83], v6 offset:16640\n"
      "ds_read_b64_tr_b16 v[86:87], v6 offset:16672\n"
      "ds_read_b64_tr_b16 v[90:91], v6 offset:16704\n"
      "ds_read_b64_tr_b16 v[94:95], v6 offset:16736\n"
      "ds_read_b64_tr_b16 v[96:97], v6 offset:33280\n"
      "ds_read_b64_tr_b16 v[100:101], v6 offset:33312\n"
      "ds_read_b64_tr_b16 v[104:105], v6 offset:33344\n"
      "ds_read_b64_tr_b16 v[108:109], v6 offset:33376\n"
      "ds_read_b64_tr_b16 v[98:99], v6 offset:49920\n"
      "ds_read_b64_tr_b16 v[102:103], v6 offset:49952\n"
      "ds_read_b64_tr_b16 v[106:107], v6 offset:49984\n"
      "ds_read_b64_tr_b16 v[110:111], v6 offset:50016\n"
      "s_lshl_b32 s66, s58, 13\n"
      "s_xor_b32 s66, s66, s75\n"
      "v_lshlrev_b32 v5, 20, v2\n"
      "v_lshrrev_b32 v253, 1, v2\n"
      "v_lshl_add_u32 v5, v253, 13, v5\n"
      "v_lshl_add_u32 v5, v1, 3, v5\n"
      "v_xor_b32 v5, s66, v5\n"
      "v_lshrrev_b32 v253, 3, v1\n"
      "v_lshlrev_b32_e64 v6, 14, v253\n"
      "v_bfe_u32 v253, v1, 2, 1\n"
      "v_lshl_add_u32 v6, v253, 12, v6\n"
      "v_and_b32_e64 v253, 3, v1\n"
      "v_lshl_add_u32 v6, v253, 3, v6\n"
      "s_waitcnt lgkmcnt(14)\n"
      "v_mfma_f32_16x16x32_bf16 v[192:195], v[176:179], v[48:51], 0\n"
      "v_mfma_f32_16x16x32_bf16 v[196:199], v[176:179], v[52:55], 0\n"
      "v_mfma_f32_16x16x32_bf16 v[200:203], v[176:179], v[56:59], 0\n"
      "v_mfma_f32_16x16x32_bf16 v[204:207], v[176:179], v[60:63], 0\n"
      "s_waitcnt lgkmcnt(14)\n"
      "v_mfma_f32_16x16x32_bf16 v[192:195], v[180:183], v[64:67], v[192:195]\n"
      "v_mfma_f32_16x16x32_bf16 v[196:199], v[180:183], v[68:71], v[196:199]\n"
      "v_mfma_f32_16x16x32_bf16 v[200:203], v[180:183], v[72:75], v[200:203]\n"
      "v_mfma_f32_16x16x32_bf16 v[204:207], v[180:183], v[76:79], v[204:207]\n"
      "s_waitcnt lgkmcnt(8)\n"
      "v_mfma_f32_16x16x32_bf16 v[192:195], v[184:187], v[80:83], v[192:195]\n"
      "v_mfma_f32_16x16x32_bf16 v[196:199], v[184:187], v[84:87], v[196:199]\n"
      "v_mfma_f32_16x16x32_bf16 v[200:203], v[184:187], v[88:91], v[200:203]\n"
      "v_mfma_f32_16x16x32_bf16 v[204:207], v[184:187], v[92:95], v[204:207]\n"
      "s_waitcnt lgkmcnt(0)\n"
      "v_mfma_f32_16x16x32_bf16 v[192:195], v[188:191], v[96:99], v[192:195]\n"
      "v_mfma_f32_16x16x32_bf16 v[196:199], v[188:191], v[100:103], v[196:199]\n"
      "v_mfma_f32_16x16x32_bf16 v[200:203], v[188:191], v[104:107], v[200:203]\n"
      "v_mfma_f32_16x16x32_bf16 v[204:207], v[188:191], v[108:111], v[204:207]\n"
      "s_add_i32 s67, s88, 262144\n"
      "s_add_i32 s68, s88, 524288\n"
      "s_add_i32 s69, s88, 786432\n"
      "v_mov_b32 v64, 0x3f803f80\n"
      "v_mov_b32 v65, 0x3f803f80\n"
      "v_mov_b32 v66, 0x3f803f80\n"
      "v_mov_b32 v67, 0x3f803f80\n"
      "v_cvt_pk_bf16_f32 v48, v192, v196\n"
      "v_cvt_pk_bf16_f32 v50, v193, v197\n"
      "v_cvt_pk_bf16_f32 v49, v200, v204\n"
      "buffer_store_dwordx2 v[48:49], v5, s[4:7], s88 offen sc1\n"
      "v_cvt_pk_bf16_f32 v51, v201, v205\n"
      "buffer_store_dwordx2 v[50:51], v5, s[4:7], s67 offen sc1\n"
      "v_cvt_pk_bf16_f32 v52, v194, v198\n"
      "v_cvt_pk_bf16_f32 v53, v202, v206\n"
      "buffer_store_dwordx2 v[52:53], v5, s[4:7], s68 offen sc1\n"
      "v_cvt_pk_bf16_f32 v54, v195, v199\n"
      "v_cvt_pk_bf16_f32 v55, v203, v207\n"
      "buffer_store_dwordx2 v[54:55], v5, s[4:7], s69 offen sc1\n"
      "s_cmp_lg_u32 s48, 6\n"
      "s_cbranch_scc1 .Lge_129\n"
      "s_mov_b32 s67, 0x70000\n"
      "v_lshrrev_b32 v117, 1, v0\n"
      "v_mul_u32_u24 v117, 1792, v117\n"
      "v_and_b32 v253, 1, v0\n"
      "v_lshl_add_u32 v117, v253, 4, v117\n"
      "buffer_load_dwordx4 v[112:115], v117, s[4:7], s67 offen sc1\n"
      ".Lge_129:\n"
      "v_mfma_f32_16x16x32_bf16 v[196:199], v[64:67], v[176:179], 0\n"
      "v_mfma_f32_16x16x32_bf16 v[196:199], v[64:67], v[180:183], v[196:199]\n"
      "v_mfma_f32_16x16x32_bf16 v[196:199], v[64:67], v[184:187], v[196:199]\n"
      "v_mfma_f32_16x16x32_bf16 v[196:199], v[64:67], v[188:191], v[196:199]\n"
      "s_nop 7\n"
      "v_mov_b32 v213, v196\n"
      "s_waitcnt vmcnt(1)\n"
      "s_cmp_eq_u32 s48, 6\n"
      "s_cbranch_scc1 .Lpa_130\n"
      "s_waitcnt vmcnt(0)\n"
      ".Lpa_130:\n"
      "s_mov_b64 exec, 0x1\n"
      "ds_add_rtn_u32 v208, v252, v254 offset:24404\n"
      "s_mov_b64 exec, -1\n"
      "s_waitcnt lgkmcnt(0)\n"
      "v_readfirstlane_b32 s66, v208\n"
      "s_cmp_lg_u32 s66, 7\n"
      "s_cbranch_scc1 .Lml_131\n"
      "s_mov_b64 exec, 0xffff\n"
      "buffer_store_dwordx2 v[212:213], v6, s[4:7], s89 offen sc1\n"
      "s_mov_b64 exec, -1\n"
      ".Lml_131:\n"
      "s_setprio 2\n"
      "s_cmp_lg_u32 s48, 6\n"
      "s_cbranch_scc1 .Lgt_132\n"
      "s_mov_b32 s67, 0x70000\n"
      "v_lshrrev_b32 v117, 1, v0\n"
      "v_mul_u32_u24 v117, 1792, v117\n"
      "v_and_b32 v253, 1, v0\n"
      "v_lshl_add_u32 v117, v253, 4, v117\n"
      "s_waitcnt vmcnt(0)\n"
      "s_branch .Lgl_134\n"
      ".Lgr_133:\n"
      "buffer_load_dwordx4 v[112:115], v117, s[4:7], s67 offen sc1\n"
      "s_waitcnt vmcnt(0)\n"
      ".Lgl_134:\n"
      "v_mov_b32 v116, 0\n"
      "s_memrealtime s[64:65]\n"
      "v_max3_u32 v116, v116, v112, v113\n"
      "v_max3_u32 v116, v116, v114, v115\n"
      "v_cmp_eq_u32 vcc, -1, v116\n"
      "s_waitcnt lgkmcnt(0)\n"
      "s_cbranch_vccnz .Lsl_135\n"
      ".Lok_136:\n"
      "v_sub_u32 v112, s64, v112\n"
      "v_sub_u32 v113, s64, v113\n"
      "v_sub_u32 v114, s64, v114\n"
      "v_sub_u32_e64 v115, s64, v115\n"
      "v_min3_i32 v112, v112, v113, v114\n"
      "v_min_i32 v112, v112, v115\n"
      "s_nop 1\n"
      "v_min_i32_dpp v112, v112, v112 quad_perm:[1,0,3,2] row_mask:0xf bank_mask:0xf\n"
      "s_nop 1\n"
      "v_min_i32_dpp v112, v112, v112 quad_perm:[2,3,0,1] row_mask:0xf bank_mask:0xf\n"
      "s_nop 1\n"
      "v_min_i32_dpp v112, v112, v112 row_half_mirror row_mask:0xf bank_mask:0xf\n"
      "s_nop 1\n"
      "v_min_i32_dpp v112, v112, v112 row_mirror row_mask:0xf bank_mask:0xf\n"
      "v_mov_b32 v113, v112\n"
      "s_nop 1\n"
      "v_permlane16_swap_b32 v112, v113\n"
      "v_min_i32 v112, v112, v113\n"
      "v_mov_b32 v113, v112\n"
      "s_nop 1\n"
      "v_permlane32_swap_b32 v112, v113\n"
      "v_min_i32 v112, v112, v113\n"
      "s_nop 0\n"
      "v_readfirstlane_b32 s66, v112\n"
      "s_max_i32 s66, s66, 0\n"
      "s_min_i32 s66, s66, 0x1000\n"
      "s_sub_i32 s66, s64, s66\n"
      "v_mov_b32 v112, s66\n"
      "s_mov_b64 exec, 0x1\n"
      "ds_write_b32 v252, v112 offset:24408\n"
      "s_mov_b64 exec, 0x1\n"
      "ds_add_u32 v252, v254 offset:24432\n"
      "s_mov_b64 exec, -1\n"
      ".Lgt_132:\n"
      "v_lshlrev_b32_e64 v5, 10, v2\n"
      "v_lshl_add_u32 v5, v1, 2, v5\n"
      "s_lshl_b32 s66, s55, 6\n"
      "buffer_load_dword v248, v5, s[8:11], s66 offen\n"
      "s_lshl_b32 s66, s49, 6\n"
      "v_and_b32 v253, 1, v2\n"
      "v_lshl_add_u32 v5, v253, 13, v252\n"
      "v_lshrrev_b32_e64 v253, 1, v2\n"
      "v_lshl_add_u32 v5, v253, 8, v5\n"
      "v_lshl_add_u32 v5, v1, 2, v5\n"
      "v_add_u32 v5, s66, v5\n"
      ".Lcw_137:\n"
      "ds_read_b32 v113, v252 offset:24404\n"
      "v_mov_b32 v253, v254\n"
      "v_mov_b32 v253, v254\n"
      "v_mov_b32 v253, v254\n"
      "v_mov_b32 v253, v254\n"
      "v_mov_b32 v253, v254\n"
      "v_mov_b32 v253, v254\n"
      "v_mov_b32 v253, v254\n"
      "v_mov_b32 v253, v254\n"
      "s_waitcnt lgkmcnt(0)\n"
      "v_cmp_gt_i32 vcc, 8, v113\n"
      "v_mov_b32 v253, v254\n"
      "v_mov_b32 v253, v254\n"
      "v_mov_b32 v253, v254\n"
      "v_mov_b32_e64 v253, v254\n"
      "s_cbranch_vccnz .Lcw_137\n"
      "ds_read_b32 v48, v5\n"
      "ds_read_b32 v49, v5 offset:512\n"
      "ds_read_b32 v50, v5 offset:1024\n"
      "ds_read_b32 v51, v5 offset:1536\n"
      "ds_read_b32 v52, v5 offset:4096\n"
      "ds_read_b32 v53, v5 offset:4608\n"
      "ds_read_b32 v54, v5 offset:5120\n"
      "ds_read_b32 v55, v5 offset:5632\n"
      "ds_read_b32 v56, v5 offset:2048\n"
      "ds_read_b32 v57, v5 offset:2560\n"
      "ds_read_b32 v58, v5 offset:3072\n"
      "ds_read_b32 v59, v5 offset:3584\n"
      "ds_read_b32 v60, v5 offset:6144\n"
      "ds_read_b32 v61, v5 offset:6656\n"
      "ds_read_b32 v62, v5 offset:7168\n"
      "ds_read_b32 v63, v5 offset:7680\n"
      "s_mov_b64 exec, 0x1\n"
      "ds_add_u32 v252, v254 offset:24416\n"
      "s_mov_b64 exec, -1\n"
      "s_waitcnt lgkmcnt(0)\n"
      "v_perm_b32 v16, v49, v48, s72\n"
      "v_perm_b32 v17, v51, v50, s72\n"
      "v_perm_b32 v18, v53, v52, s72\n"
      "v_perm_b32 v19, v55, v54, s72\n"
      "v_perm_b32 v20, v49, v48, s73\n"
      "v_perm_b32 v21, v51, v50, s73\n"
      "v_perm_b32 v22, v53, v52, s73\n"
      "v_perm_b32 v23, v55, v54, s73\n"
      "v_perm_b32 v24, v57, v56, s72\n"
      "v_perm_b32 v25, v59, v58, s72\n"
      "v_perm_b32 v26, v61, v60, s72\n"
      "v_perm_b32 v27, v63, v62, s72\n"
      "v_perm_b32 v28, v57, v56, s73\n"
      "v_perm_b32 v29, v59, v58, s73\n"
      "v_perm_b32 v30, v61, v60, s73\n"
      "v_perm_b32 v31, v63, v62, s73\n"
      "v_lshlrev_b32 v164, 8, v2\n"
      "v_lshrrev_b32 v253, 3, v1\n"
      "v_lshl_add_u32 v164, v253, 7, v164\n"
      "v_and_b32 v253, 3, v1\n"
      "v_lshl_add_u32 v164, v253, 5, v164\n"
      "s_lshl_b32 s66, s49, 8\n"
      "v_lshl_add_u32 v165, v2, 6, v252\n"
      "v_and_b32_e64 v253, 7, v1\n"
      "v_lshl_add_u32 v165, v253, 3, v165\n"
      "v_add_u32 v165, s66, v165\n"
      "v_lshrrev_b32 v253, 2, v1\n"
      "v_lshl_add_u32 v166, v253, 8, v252\n"
      "v_and_b32_e64 v253, 1, v1\n"
      "v_lshl_add_u32 v166, v253, 7, v166\n"
      "v_lshl_add_u32 v166, v2, 4, v166\n"
      "v_lshlrev_b32 v167, 2, v0\n"
      "v_lshl_add_u32 v169, v0, 3, v252\n"
      "s_lshl_b32 s66, s49, 9\n"
      "v_add_u32 v168, s66, v169\n"
      "v_lshlrev_b32 v170, 15, v2\n"
      "v_lshrrev_b32 v253, 3, v1\n"
      "v_lshl_add_u32 v170, v253, 7, v170\n"
      "v_and_b32 v253, 7, v1\n"
      "v_lshl_add_u32 v170, v253, 2, v170\n"
      "v_and_b32 v253, 3, v1\n"
      "v_lshlrev_b32 v162, 0, v253\n"
      "v_and_b32 v163, 2, v2\n"
      "v_sub_u32 v163, v162, v163\n"
      "v_cmp_eq_u32 vcc, 0, v163\n"
      "s_nop 1\n"
      "v_cndmask_b32 v162, 0, -1, vcc\n"
      "v_cmp_eq_u32 vcc, 1, v163\n"
      "s_nop 1\n"
      "v_cndmask_b32 v163, 0, -1, vcc\n"
      "s_mov_b32 s99, 286\n"
      ".Lcw_138:\n"
      "ds_read_b32 v112, v252 offset:24432\n"
      "ds_read_b32 v113, v252 offset:24408\n"
      "v_mov_b32 v253, v254\n"
      "v_mov_b32 v253, v254\n"
      "v_mov_b32 v253, v254\n"
      "v_mov_b32 v253, v254\n"
      "v_mov_b32 v253, v254\n"
      "v_mov_b32 v253, v254\n"
      "s_waitcnt lgkmcnt(0)\n"
      "v_cmp_gt_i32 vcc, 1, v112\n"
      "v_readfirstlane_b32 s61, v113\n"
      "s_cbranch_vccnz .Lcw_138\n"
      ".Lwe_139:\n"
      "s_memrealtime s[64:65]\n"
      "v_mov_b32 v253, v254\n"
      "v_mov_b32 v253, v254\n"
      "v_mov_b32 v253, v254\n"
      "v_mov_b32 v253, v254\n"
      "v_mov_b32 v253, v254\n"
      "v_mov_b32 v253, v254\n"
      "v_mov_b32 v253, v254\n"
      "v_mov_b32 v253, v254\n"
      "s_waitcnt lgkmcnt(0)\n"
      "s_sub_i32 s64, s64, s99\n"
      "s_sub_i32 s65, s61, s64\n"
      "s_sub_i32 s65, s3, s65\n"
      "s_max_i32 s65, s65, 0\n"
      "s_add_i32 s99, s99, s65\n"
      "s_add_i32 s62, s61, s99\n"
      ".Lwf_142:\n"
      "s_memrealtime s[64:65]\n"
      "v_mov_b32 v253, v254\n"
      "v_mov_b32 v253, v254\n"
      "v_mov_b32 v253, v254\n"
      "v_mov_b32 v253, v254\n"
      "v_mov_b32 v253, v254\n"
      "v_mov_b32 v253, v254\n"
      "v_mov_b32 v253, v254\n"
      "v_mov_b32 v253, v254\n"
      "s_waitcnt lgkmcnt(0)\n"
      "s_sub_i32 s65, s62, s64\n"
      "s_cmp_gt_i32 s65, 3\n"
      "s_cbranch_scc1 .Lwf_142\n"
      "s_max_i32 s65, s65, 0\n"
      ".Lwd_143:\n"
      "s_sub_u32 s65, s65, 1\n"
      "s_cbranch_scc0 .Lwd_143\n"
      "buffer_load_dwordx2 v[112:113], v164, s[4:7], s91 offen sc1\n"
      "s_waitcnt vmcnt(0)\n"
      "v_max_u32_e64 v114, v112, v113\n"
      "v_cmp_eq_u32_e64 vcc, -1, v114\n"
      "s_cbranch_vccnz .Lsl_144\n"
      ".Lok_145:\n"
      "buffer_load_dwordx4 v[176:179], v3, s[4:7], s90 offen sc1\n"
      "buffer_load_dwordx4 v[180:183], v3, s[4:7], s90 offen offset:1024 sc1\n"
      "buffer_load_dwordx4 v[184:187], v3, s[4:7], s90 offen offset:2048 sc1\n"
      "buffer_load_dwordx4 v[188:191], v3, s[4:7], s90 offen offset:3072 sc1\n"
      "v_max_f32_dpp v114, v112, v112 quad_perm:[1,0,3,2] row_mask:0xf bank_mask:0xf bound_ctrl:1\n"
      "s_nop 1\n"
      "v_max_f32_dpp v114, v114, v114 quad_perm:[2,3,0,1] row_mask:0xf bank_mask:0xf\n"
      "s_nop 1\n"
      "v_max_f32_dpp v114, v114, v114 row_mirror row_mask:0xf bank_mask:0xf\n"
      "v_mov_b32 v115, v114\n"
      "s_nop 1\n"
      "v_permlane16_swap_b32 v114, v115\n"
      "v_max_f32 v114, v114, v115\n"
      "v_mov_b32 v115, v114\n"
      "s_nop 1\n"
      "v_permlane32_swap_b32 v114, v115\n"
      "v_max_f32 v114, v114, v115\n"
      "v_sub_f32 v112, v112, v114\n"
      "v_exp_f32 v112, v112\n"
      "s_nop 0\n"
      "v_mul_f32 v113, v112, v113\n"
      "s_nop 1\n"
      "v_add_f32_dpp v113, v113, v113 quad_perm:[1,0,3,2] row_mask:0xf bank_mask:0xf\n"
      "s_nop 1\n"
      "v_add_f32_dpp v113, v113, v113 quad_perm:[2,3,0,1] row_mask:0xf bank_mask:0xf\n"
      "s_nop 1\n"
      "v_add_f32_dpp v113, v113, v113 row_mirror row_mask:0xf bank_mask:0xf\n"
      "v_mov_b32 v115, v113\n"
      "s_nop 1\n"
      "v_permlane16_swap_b32 v113, v115\n"
      "v_add_f32_e64 v113, v113, v115\n"
      "v_mov_b32 v115, v113\n"
      "s_nop 1\n"
      "v_permlane32_swap_b32_e64 v113, v115\n"
      "v_add_f32_e64 v113, v113, v115\n"
      "v_rcp_f32_e64 v113, v113\n"
      "s_nop 0\n"
      "v_mul_f32_e64 v112, v112, v113\n"
      "v_cvt_pk_bf16_f32 v112, v112, v112\n"
      "s_nop 1\n"
      "v_and_b32_dpp v116, v112, v12 quad_perm:[0,0,0,0] row_mask:0xf bank_mask:0xf bound_ctrl:1\n"
      "v_and_b32_dpp v117, v112, v13 quad_perm:[0,0,0,0] row_mask:0xf bank_mask:0xf bound_ctrl:1\n"
      "v_and_b32_dpp v118, v112, v14 quad_perm:[0,0,0,0] row_mask:0xf bank_mask:0xf bound_ctrl:1\n"
      "v_and_b32_dpp v119, v112, v15 quad_perm:[0,0,0,0] row_mask:0xf bank_mask:0xf bound_ctrl:1\n"
      "v_and_b32_dpp v120, v112, v12 quad_perm:[1,1,1,1] row_mask:0xf bank_mask:0xf bound_ctrl:1\n"
      "v_and_b32_dpp v121, v112, v13 quad_perm:[1,1,1,1] row_mask:0xf bank_mask:0xf bound_ctrl:1\n"
      "v_and_b32_dpp v122, v112, v14 quad_perm:[1,1,1,1] row_mask:0xf bank_mask:0xf bound_ctrl:1\n"
      "v_and_b32_dpp v123, v112, v15 quad_perm:[1,1,1,1] row_mask:0xf bank_mask:0xf bound_ctrl:1\n"
      "v_and_b32_dpp v124, v112, v12 quad_perm:[2,2,2,2] row_mask:0xf bank_mask:0xf bound_ctrl:1\n"
      "v_and_b32_dpp v125, v112, v13 quad_perm:[2,2,2,2] row_mask:0xf bank_mask:0xf bound_ctrl:1\n"
      "v_and_b32_dpp v126, v112, v14 quad_perm:[2,2,2,2] row_mask:0xf bank_mask:0xf bound_ctrl:1\n"
      "v_and_b32_dpp v127, v112, v15 quad_perm:[2,2,2,2] row_mask:0xf bank_mask:0xf bound_ctrl:1\n"
      "v_and_b32_dpp v128, v112, v12 quad_perm:[3,3,3,3] row_mask:0xf bank_mask:0xf bound_ctrl:1\n"
      "v_and_b32_dpp v129, v112, v13 quad_perm:[3,3,3,3] row_mask:0xf bank_mask:0xf bound_ctrl:1\n"
      "v_and_b32_dpp v130, v112, v14 quad_perm:[3,3,3,3] row_mask:0xf bank_mask:0xf bound_ctrl:1\n"
      "v_and_b32_dpp v131, v112, v15 quad_perm:[3,3,3,3] row_mask:0xf bank_mask:0xf bound_ctrl:1\n"
      "s_waitcnt vmcnt(3)\n"
      "v_mfma_f32_16x16x32_bf16 v[136:139], v[176:179], v[116:119], 0\n"
      "s_waitcnt vmcnt(2)\n"
      "v_mfma_f32_16x16x32_bf16 v[136:139], v[180:183], v[120:123], v[136:139]\n"
      "s_waitcnt vmcnt(1)\n"
      "v_mfma_f32_16x16x32_bf16 v[136:139], v[184:187], v[124:127], v[136:139]\n"
      "s_waitcnt vmcnt(0)\n"
      "v_mfma_f32_16x16x32_bf16 v[136:139], v[188:191], v[128:131], v[136:139]\n"
      "s_mov_b32 exec_lo, 0xff00ff\n"
      "s_mov_b32 exec_hi, 0xff00ff00\n"
      "s_nop 5\n"
      "v_cvt_pk_bf16_f32 v132, v136, v137\n"
      "v_cvt_pk_bf16_f32 v133, v138, v139\n"
      "ds_write_b64 v165, v[132:133] offset:20992\n"
      "s_mov_b64 exec, 0x1\n"
      "ds_add_u32 v252, v254 offset:24376\n"
      "s_mov_b64 exec, -1\n"
      ".Lcw_146:\n"
      "ds_read_b32 v112, v252 offset:24376\n"
      "ds_read_b128 v[176:179], v166 offset:20992\n"
      "ds_read_b128 v[180:183], v166 offset:21056\n"
      "s_waitcnt lgkmcnt(0)\n"
      "v_cmp_gt_i32 vcc, 4, v112\n"
      "v_mfma_f32_16x16x32_bf16 v[136:139], v[176:179], v[16:19], 0\n"
      "v_mfma_f32_16x16x32_bf16 v[140:143], v[176:179], v[20:23], 0\n"
      "s_cbranch_vccnz .Lcw_146\n"
      "v_mfma_f32_16x16x32_bf16 v[136:139], v[180:183], v[24:27], v[136:139]\n"
      "v_mfma_f32_16x16x32_bf16 v[140:143], v[180:183], v[28:31], v[140:143]\n"
      "s_mov_b32 s99, 514\n"
      "s_nop 5\n"
      "v_add_f32 v136, v136, v137\n"
      "v_add_f32_e64 v140, v140, v141\n"
      "v_cvt_pk_bf16_f32 v132, v136, v140\n"
      "buffer_store_dword v132, v167, s[4:7], s92 offen sc1\n"
      ".Lwe_147:\n"
      "s_memrealtime s[64:65]\n"
      "v_mov_b32 v253, v254\n"
      "v_mov_b32 v253, v254\n"
      "v_mov_b32 v253, v254\n"
      "v_mov_b32 v253, v254\n"
      "v_mov_b32 v253, v254\n"
      "v_mov_b32 v253, v254\n"
      "v_mov_b32 v253, v254\n"
      "v_mov_b32 v253, v254\n"
      "s_waitcnt lgkmcnt(0)\n"
      "s_sub_i32 s64, s64, s99\n"
      "s_sub_i32 s65, s61, s64\n"
      "s_sub_i32 s65, s3, s65\n"
      "s_max_i32 s65, s65, 0\n"
      "s_add_i32 s99, s99, s65\n"
      "s_add_i32 s62, s61, s99\n"
      ".Lwf_150:\n"
      "s_memrealtime s[64:65]\n"
      "v_mov_b32 v253, v254\n"
      "v_mov_b32 v253, v254\n"
      "v_mov_b32 v253, v254\n"
      "v_mov_b32 v253, v254\n"
      "v_mov_b32 v253, v254\n"
      "v_mov_b32 v253, v254\n"
      "v_mov_b32 v253, v254\n"
      "v_mov_b32 v253, v254\n"
      "s_waitcnt lgkmcnt(0)\n"
      "s_sub_i32 s65, s62, s64\n"
      "s_cmp_gt_i32 s65, 3\n"
      "s_cbranch_scc1 .Lwf_150\n"
      "s_max_i32 s65, s65, 0\n"
      ".Lwd_151:\n"
      "s_sub_u32 s65, s65, 1\n"
      "s_cbranch_scc0 .Lwd_151\n"
      "buffer_load_dwordx4 v[176:179], v3, s[4:7], s93 offen sc1\n"
      "buffer_load_dwordx4 v[180:183], v3, s[4:7], s93 offen offset:1024 sc1\n"
      "buffer_load_dwordx4 v[184:187], v3, s[4:7], s94 offen sc1\n"
      "buffer_load_dwordx4 v[188:191], v3, s[4:7], s94 offen offset:1024 sc1\n"
      "v_mov_b32 v160, 0\n"
      "s_cmp_eq_u32 s54, s55\n"
      "s_cselect_b32 s66, -1, 0\n"
      "v_and_b32 v250, s66, v248\n"
      "v_and_b32 v251, 0xffff0000, v250\n"
      "v_lshlrev_b32_e64 v250, 16, v250\n"
      "s_xor_b32 s67, s95, 131072\n"
      "s_xor_b32 s68, s95, 8192\n"
      "s_xor_b32 s69, s95, 139264\n"
      "s_mul_i32 s70, s51, 1792\n"
      "s_lshl_b32 s71, s50, 2\n"
      "s_add_i32 s70, s70, s71\n"
      "s_add_i32 s70, s70, 0x70080\n"
      "s_lshl_b32 s66, s49, 11\n"
      "v_lshrrev_b32 v253, 1, v2\n"
      "v_lshl_add_u32 v5, v253, 10, v252\n"
      "v_and_b32_e64 v253, 1, v2\n"
      "v_lshl_add_u32 v5, v253, 8, v5\n"
      "v_lshl_add_u32 v5, v1, 2, v5\n"
      "v_add_u32 v5, s66, v5\n"
      ".Lcw_152:\n"
      "ds_read_b32 v112, v252 offset:24420\n"
      "v_mov_b32 v253, v254\n"
      "v_mov_b32 v253, v254\n"
      "v_mov_b32 v253, v254\n"
      "v_mov_b32 v253, v254\n"
      "v_mov_b32 v253, v254\n"
      "v_mov_b32 v253, v254\n"
      "v_mov_b32 v253, v254\n"
      "v_mov_b32 v253, v254\n"
      "s_waitcnt lgkmcnt(0)\n"
      "v_cmp_gt_i32 vcc, 4, v112\n"
      "v_mov_b32 v253, v254\n"
      "v_mov_b32_e64 v253, v254\n"
      "v_mov_b32_e64 v253, v254\n"
      "v_mov_b32_e64 v253, v254\n"
      "s_cbranch_vccnz .Lcw_152\n"
      "ds_read_b32 v64, v5\n"
      "ds_read_b32 v65, v5 offset:64\n"
      "ds_read_b32 v66, v5 offset:128\n"
      "ds_read_b32 v67, v5 offset:192\n"
      "ds_read_b32 v68, v5 offset:512\n"
      "ds_read_b32 v69, v5 offset:576\n"
      "ds_read_b32 v70, v5 offset:640\n"
      "ds_read_b32 v71, v5 offset:704\n"
      "ds_read_b32 v72, v5 offset:8192\n"
      "ds_read_b32 v73, v5 offset:8256\n"
      "ds_read_b32 v74, v5 offset:8320\n"
      "ds_read_b32 v75, v5 offset:8384\n"
      "ds_read_b32 v76, v5 offset:8704\n"
      "ds_read_b32 v77, v5 offset:8768\n"
      "ds_read_b32 v78, v5 offset:8832\n"
      "ds_read_b32 v79, v5 offset:8896\n"
      "s_waitcnt lgkmcnt(0)\n"
      "v_perm_b32 v32, v65, v64, s72\n"
      "v_perm_b32 v33, v67, v66, s72\n"
      "v_perm_b32 v34, v69, v68, s72\n"
      "v_perm_b32 v35, v71, v70, s72\n"
      "v_perm_b32 v36, v65, v64, s73\n"
      "v_perm_b32 v37, v67, v66, s73\n"
      "v_perm_b32 v38, v69, v68, s73\n"
      "v_perm_b32 v39, v71, v70, s73\n"
      "v_perm_b32 v40, v73, v72, s72\n"
      "v_perm_b32 v41, v75, v74, s72\n"
      "v_perm_b32 v42, v77, v76, s72\n"
      "v_perm_b32 v43, v79, v78, s72\n"
      "v_perm_b32 v44, v73, v72, s73\n"
      "v_perm_b32 v45, v75, v74, s73\n"
      "v_perm_b32 v46, v77, v76, s73\n"
      "v_perm_b32 v47, v79, v78, s73\n"
      "s_waitcnt vmcnt(3)\n"
      "v_mfma_f32_16x16x32_bf16 v[144:147], v[8:11], v[176:179], 0\n"
      "v_max3_u32 v160, v160, v176, v177\n"
      "v_max3_u32 v160, v160, v178, v179\n"
      "s_waitcnt vmcnt(2)\n"
      "v_mfma_f32_16x16x32_bf16 v[144:147], v[8:11], v[180:183], v[144:147]\n"
      "v_max3_u32 v160, v160, v180, v181\n"
      "v_max3_u32 v160, v160, v182, v183\n"
      "s_waitcnt vmcnt(1)\n"
      "v_mfma_f32_16x16x32_bf16 v[148:151], v[8:11], v[184:187], 0\n"
      "v_max3_u32 v160, v160, v184, v185\n"
      "v_max3_u32 v160, v160, v186, v187\n"
      "s_nop 1\n"
      "v_cvt_pk_bf16_f32 v154, v144, v145\n"
      "v_cvt_pk_bf16_f32 v155, v146, v147\n"
      "v_and_b32_e64 v152, v162, v154\n"
      "v_and_b32_e64 v153, v162, v155\n"
      "v_and_b32 v154, v163, v154\n"
      "v_and_b32_e64 v155, v163, v155\n"
      "s_waitcnt vmcnt(0)\n"
      "v_mfma_f32_16x16x32_bf16 v[148:151], v[8:11], v[188:191], v[148:151]\n"
      "v_max3_u32 v160, v160, v188, v189\n"
      "v_max3_u32 v160, v160, v190, v191\n"
      "v_mfma_f32_16x16x32_bf16 v[136:139], v[152:155], v[32:35], 0\n"
      "v_mfma_f32_16x16x32_bf16 v[140:143], v[152:155], v[36:39], 0\n"
      "v_cmp_eq_u32 vcc, -1, v160\n"
      "s_nop 2\n"
      "v_cvt_pk_bf16_f32 v158, v148, v149\n"
      "v_cvt_pk_bf16_f32 v159, v150, v151\n"
      "v_and_b32_e64 v156, v162, v158\n"
      "v_and_b32_e64 v157, v162, v159\n"
      "v_and_b32 v158, v163, v158\n"
      "v_and_b32_e64 v159, v163, v159\n"
      "s_nop 1\n"
      "v_mfma_f32_16x16x32_bf16 v[136:139], v[156:159], v[40:43], v[136:139]\n"
      "v_mfma_f32_16x16x32_bf16 v[140:143], v[156:159], v[44:47], v[140:143]\n"
      "s_cbranch_vccnz .Lsl_153\n"
      ".Lok_154:\n"
      "s_nop 5\n"
      "v_pk_add_f32 v[136:137], v[136:137], v[138:139]\n"
      "v_add_f32_e64 v132, v136, v137\n"
      "v_pk_add_f32 v[140:141], v[140:141], v[142:143]\n"
      "v_add_f32_e64 v133, v140, v141\n"
      "ds_write_b64 v168, v[132:133] offset:28544\n"
      "s_mov_b64 exec, 0x1\n"
      "ds_add_rtn_u32 v112, v252, v254 offset:24380\n"
      "s_mov_b64 exec, -1\n"
      "ds_read_b64 v[136:137], v169 offset:28544\n"
      "ds_read_b64 v[138:139], v169 offset:29056\n"
      "ds_read_b64 v[140:141], v169 offset:29568\n"
      "ds_read_b64 v[142:143], v169 offset:30080\n"
      "s_waitcnt lgkmcnt(4)\n"
      "v_readfirstlane_b32 s66, v112\n"
      "s_waitcnt lgkmcnt(2)\n"
      "v_pk_add_f32 v[136:137], v[136:137], v[138:139]\n"
      "v_pk_add_f32 v[136:137], v[136:137], v[250:251]\n"
      "s_waitcnt lgkmcnt(0)\n"
      "v_pk_add_f32 v[140:141], v[140:141], v[142:143]\n"
      "v_pk_add_f32 v[136:137], v[136:137], v[140:141]\n"
      "s_cmp_lg_u32 s66, 3\n"
      "v_cvt_pk_bf16_f32 v136, v136, v137\n"
      "s_cbranch_scc1 .Lv2d_155\n"
      "buffer_store_dword v136, v170, s[4:7], s95 offen sc1\n"
      "buffer_store_dword v136, v170, s[4:7], s68 offen sc1\n"
      "buffer_store_dword v136, v170, s[4:7], s67 offen sc1\n"
      "buffer_store_dword v136, v170, s[4:7], s69 offen sc1\n"
      "s_mov_b64 exec, 0x1\n"
      "ds_add_u32 v252, v254 offset:24384\n"
      "s_mov_b64 exec, -1\n"
      ".Lcw_156:\n"
      "ds_read_b32 v112, v252 offset:24392\n"
      "s_memrealtime s[64:65]\n"
      "s_waitcnt lgkmcnt(0)\n"
      "v_cmp_gt_i32 vcc, 8, v112\n"
      "s_bitset0_b32 s64, 0\n"
      "v_mov_b32 v113, s64\n"
      "s_cbranch_vccnz .Lcw_156\n"
      "s_mov_b64 exec, 0x1\n"
      "buffer_store_dword v113, off, s[4:7], s70 sc1\n"
      "s_mov_b64 exec, -1\n"
      ".Lv2d_155:\n"
      "s_setprio 0\n"
      "v_mov_b32 v176, -1\n"
      "v_mov_b32 v177, -1\n"
      "v_mov_b32 v178, -1\n"
      "v_mov_b32 v179, -1\n"
      "s_add_i32 s89, s89, 0x400000\n"
      "s_add_i32 s91, s91, 0x400000\n"
      "s_add_i32 s92, s92, 0x400000\n"
      "s_add_i32 s93, s93, 0x400000\n"
      "s_add_i32 s94, s94, 0x400000\n"
      "s_add_i32 s95, s95, 0x400000\n"
      "s_add_i32 s88, s88, 0x400000\n"
      "s_add_i32 s90, s90, 0x400000\n"
      "s_add_i32 s61, s61, 602\n"
      "s_mov_b32 s68, 6\n"
      "s_add_i32 s69, s68, 40\n"
      "s_mov_b32 s67, 0x70080\n"
      "v_lshrrev_b32 v185, 1, v0\n"
      "v_mul_u32_u24 v185, 1792, v185\n"
      "v_and_b32 v253, 1, v0\n"
      "v_lshl_add_u32 v185, v253, 4, v185\n"
      "v_mov_b32 v181, s61\n"
      ".Lcw_157:\n"
      "ds_read_b32 v180, v252 offset:24392\n"
      "v_mov_b32 v253, v254\n"
      "v_mov_b32 v253, v254\n"
      "v_mov_b32 v253, v254\n"
      "v_mov_b32 v253, v254\n"
      "v_mov_b32 v253, v254\n"
      "v_mov_b32 v253, v254\n"
      "v_mov_b32 v253, v254\n"
      "v_mov_b32 v253, v254\n"
      "s_waitcnt lgkmcnt(0)\n"
      "v_cmp_gt_i32 vcc, 8, v180\n"
      "v_mov_b32 v253, v254\n"
      "v_mov_b32 v253, v254\n"
      "v_mov_b32_e64 v253, v254\n"
      "v_mov_b32_e64 v253, v254\n"
      "s_cbranch_vccnz .Lcw_157\n"
      "s_cmp_lg_u32 s48, 6\n"
      "s_cbranch_scc1 .Lg6_158\n"
      "s_mov_b64 exec, 0x1\n"
      "ds_write_b32 v252, v181 offset:24408\n"
      "s_mov_b64 exec, 0x1\n"
      "ds_add_u32 v252, v254 offset:24432\n"
      "s_mov_b64 exec, -1\n"
      ".Lg6_158:\n"
      "s_add_i32 s71, s61, 18\n"
      ".Lpa_159:\n"
      "s_memrealtime s[64:65]\n"
      "v_mov_b32 v253, v254\n"
      "v_mov_b32 v253, v254\n"
      "v_mov_b32 v253, v254\n"
      "v_mov_b32 v253, v254\n"
      "v_mov_b32 v253, v254\n"
      "v_mov_b32 v253, v254\n"
      "v_mov_b32 v253, v254\n"
      "v_mov_b32_e64 v253, v254\n"
      "s_waitcnt lgkmcnt(0)\n"
      "s_sub_i32 s65, s64, s71\n"
      "s_cmp_lt_i32 s65, 0\n"
      "s_cbranch_scc1 .Lpa_159\n"
      "s_cmp_lg_u32 s48, 6\n"
      "s_cbranch_scc1 .Lg6_160\n"
      "s_setprio 1\n"
      "buffer_load_dwordx4 v[180:183], v185, s[4:7], s67 offen sc1\n"
      ".Lg6_160:\n"
      "buffer_store_dwordx4 v[176:179], v3, s[4:7], s90 offen sc1\n"
      "buffer_store_dwordx4 v[176:179], v3, s[4:7], s90 offen offset:1024 sc1\n"
      "buffer_store_dwordx4 v[176:179], v3, s[4:7], s90 offen offset:2048 sc1\n"
      "s_cmp_lg_u32 s48, 7\n"
      "s_cbranch_scc1 .Lcw_161\n"
      "s_getpc_b64 s[66:67]\n"
      ".Lch_162:\n"
      "s_sub_u32 s66, s66, .Lch_162-.Lpc_here\n"
      "s_subb_u32 s67, s67, 0\n"
      "s_lshl_b32 s71, s51, 7\n"
      "v_lshl_add_u32 v5, v0, 12, s71\n"
      "s_mov_b32 s71, .Lpc_end-.Lpc_here\n"
      "v_cmp_gt_u32 vcc, s71, v5\n"
      "s_and_b64 exec, exec, vcc\n"
      "s_cbranch_execz .Lcw_161\n"
      "global_load_dword v6, v5, s[66:67]\n"
      ".Lcw_161:\n"
      "s_mov_b64 exec, -1\n"
      "s_cmp_lg_u32 s48, 6\n"
      "s_cbranch_scc1 .Lgt_163\n"
      "s_mov_b32 s67, 0x70080\n"
      "v_lshrrev_b32 v185, 1, v0\n"
      "v_mul_u32_u24 v185, 1792, v185\n"
      "v_and_b32 v253, 1, v0\n"
      "v_lshl_add_u32 v185, v253, 4, v185\n"
      "s_waitcnt vmcnt(3)\n"
      "s_branch .Lgl_165\n"
      ".Lgr_164:\n"
      "v_mov_b32 v180, s64\n"
      "s_mov_b64 exec, 0x1\n"
      "ds_write_b32 v252, v180 offset:24408\n"
      "s_mov_b64 exec, -1\n"
      "buffer_load_dwordx4 v[180:183], v185, s[4:7], s67 offen sc1\n"
      "s_waitcnt vmcnt(0)\n"
      ".Lgl_165:\n"
      "v_mov_b32 v184, 0\n"
      "s_memrealtime s[64:65]\n"
      "v_max3_u32 v184, v184, v180, v181\n"
      "v_max3_u32 v184, v184, v182, v183\n"
      "v_cmp_eq_u32 vcc, -1, v184\n"
      "s_waitcnt lgkmcnt(0)\n"
      "s_cbranch_vccnz .Lsl_166\n"
      ".Lok_167:\n"
      "v_sub_u32 v180, s64, v180\n"
      "v_sub_u32 v181, s64, v181\n"
      "v_sub_u32 v182, s64, v182\n"
      "v_sub_u32_e64 v183, s64, v183\n"
      "v_min3_i32 v180, v180, v181, v182\n"
      "v_min_i32 v180, v180, v183\n"
      "s_nop 1\n"
      "v_min_i32_dpp v180, v180, v180 quad_perm:[1,0,3,2] row_mask:0xf bank_mask:0xf\n"
      "s_nop 1\n"
      "v_min_i32_dpp v180, v180, v180 quad_perm:[2,3,0,1] row_mask:0xf bank_mask:0xf\n"
      "s_nop 1\n"
      "v_min_i32_dpp v180, v180, v180 row_half_mirror row_mask:0xf bank_mask:0xf\n"
      "s_nop 1\n"
      "v_min_i32_dpp v180, v180, v180 row_mirror row_mask:0xf bank_mask:0xf\n"
      "v_mov_b32 v181, v180\n"
      "s_nop 1\n"
      "v_permlane16_swap_b32 v180, v181\n"
      "v_min_i32 v180, v180, v181\n"
      "v_mov_b32 v181, v180\n"
      "s_nop 1\n"
      "v_permlane32_swap_b32 v180, v181\n"
      "v_min_i32 v180, v180, v181\n"
      "s_nop 0\n"
      "v_readfirstlane_b32 s66, v180\n"
      "s_max_i32 s66, s66, 0\n"
      "s_min_i32 s66, s66, 0x1000\n"
      "s_sub_i32 s66, s64, s66\n"
      "s_add_i32 s69, s69, s66\n"
      "s_add_i32 s66, s66, s68\n"
      "s_sub_i32 s66, s66, s61\n"
      "s_cmp_gt_i32 s66, 0\n"
      "s_cselect_b32 s66, s69, s61\n"
      "v_mov_b32 v180, s66\n"
      "s_mov_b64 exec, 0x1\n"
      "ds_write_b32 v252, v180 offset:24408\n"
      "s_mov_b64 exec, 0x1\n"
      "ds_add_u32 v252, v254 offset:24432\n"
      "s_mov_b64 exec, -1\n"
      ".Lgt_163:\n"
      "s_setprio 0\n"
      ".Lcw_168:\n"
      "ds_read_b32 v180, v252 offset:24432\n"
      "ds_read_b32 v181, v252 offset:24408\n"
      "v_mov_b32 v253, v254\n"
      "v_mov_b32 v253, v254\n"
      "v_mov_b32 v253, v254\n"
      "v_mov_b32 v253, v254\n"
      "v_mov_b32 v253, v254\n"
      "v_mov_b32 v253, v254\n"
      "s_waitcnt lgkmcnt(0)\n"
      "v_cmp_gt_i32_e64 vcc, 3, v180\n"
      "v_readfirstlane_b32 s61, v181\n"
      "s_cbranch_vccnz .Lcw_168\n"
      "s_add_i32 s71, s61, 148\n"
      ".Lpa_169:\n"
      "s_memrealtime s[64:65]\n"
      "v_mov_b32 v253, v254\n"
      "v_mov_b32 v253, v254\n"
      "v_mov_b32_e64 v253, v254\n"
      "v_mov_b32_e64 v253, v254\n"
      "v_mov_b32_e64 v253, v254\n"
      "v_mov_b32_e64 v253, v254\n"
      "v_mov_b32_e64 v253, v254\n"
      "v_mov_b32_e64 v253, v254\n"
      "s_waitcnt lgkmcnt(0)\n"
      "s_sub_i32 s65, s64, s71\n"
      "s_cmp_lt_i32 s65, 0\n"
      "s_cbranch_scc1 .Lpa_169\n"
      "buffer_store_dwordx4 v[176:179], v3, s[4:7], s90 offen offset:3072 sc1\n"
      "s_cmp_lg_u32 s48, 4\n"
      "s_cbranch_scc1 .Lqs_170\n"
      "v_bfe_u32 v253, v1, 1, 2\n"
      "v_lshl_add_u32 v5, v253, 6, v252\n"
      "v_lshl_add_u32 v5, v2, 4, v5\n"
      "v_lshlrev_b32 v7, 2, v0\n"
      "s_bfe_u32 s71, s55, 0x10003\n"
      "s_lshl_b32 s70, s71, 13\n"
      "s_bfe_u32 s71, s55, 0x30000\n"
      "s_lshl_b32 s71, s71, 9\n"
      "s_add_i32 s70, s70, s71\n"
      "s_lshl_b32 s71, s54, 5\n"
      "s_add_i32 s70, s70, s71\n"
      "s_add_i32 s70, s70, 0x468000\n"
      "s_xor_b32 s70, s70, s75\n"
      "s_mov_b32 s68, 0xaaaaaaaa\n"
      "s_mov_b32 s69, s68\n"
      "v_mov_b32 v189, 0x3f803f80\n"
      "s_setprio 1\n"
      ".Lcw_171:\n"
      "ds_read_b32 v190, v252 offset:24352\n"
      "ds_read_b128 v[180:183], v5 offset:22016\n"
      "s_waitcnt lgkmcnt(0)\n"
      "v_cmp_gt_i32 vcc, 8, v190\n"
      "v_mov_b32 v188, 0\n"
      "v_cndmask_b32 v184, v189, v180, s[68:69]\n"
      "v_cndmask_b32 v185, v189, v181, s[68:69]\n"
      "v_cndmask_b32 v186, v189, v182, s[68:69]\n"
      "v_cndmask_b32 v187, v189, v183, s[68:69]\n"
      "s_cbranch_vccnz .Lcw_171\n"
      "v_dot2c_f32_bf16 v188, v180, v184\n"
      "v_dot2c_f32_bf16 v188, v181, v185\n"
      "v_dot2c_f32_bf16 v188, v182, v186\n"
      "v_dot2c_f32_bf16 v188, v183, v187\n"
      "s_nop 2\n"
      "v_mov_b32 v190, v188\n"
      "s_nop 1\n"
      "v_permlane16_swap_b32 v188, v190\n"
      "v_add_f32 v188, v188, v190\n"
      "v_mov_b32 v190, v188\n"
      "s_nop 1\n"
      "v_permlane32_swap_b32 v188, v190\n"
      "v_add_f32 v188, v188, v190\n"
      "s_mov_b64 exec, 0xff\n"
      "buffer_store_dword v188, v7, s[4:7], s70 offen sc1\n"
      "s_mov_b64 exec, -1\n"
      "s_setprio 0\n"
      "s_waitcnt vmcnt(1)\n"
      "s_branch .Lqs_170b\n"
      ".Lqs_170:\n"
      "s_waitcnt vmcnt(0)\n"
      ".Lqs_170b:\n"
      "s_mov_b64 exec, 0x1\n"
      "ds_add_u32 v252, v254 offset:24400\n"
      "s_mov_b64 exec, -1\n"
      "v_lshrrev_b32 v253, 3, v1\n"
      "v_lshlrev_b32 v248, 0, v253\n"
      "v_and_b32 v253, 7, v1\n"
      "v_lshl_add_u32 v248, v253, 1, v248\n"
      "v_mul_u32_u24 v248, 1040, v248\n"
      "s_mul_i32 s66, s49, 33280\n"
      "v_lshl_add_u32 v248, v2, 8, v248\n"
      "v_add_u32_e64 v248, s66, v248\n"
      "ds_read_b128 v[48:51], v248\n"
      "ds_read_b128 v[52:55], v248 offset:16\n"
      "ds_read_b128 v[56:59], v248 offset:32\n"
      "ds_read_b128 v[60:63], v248 offset:48\n"
      "ds_read_b128 v[64:67], v248 offset:64\n"
      "ds_read_b128 v[68:71], v248 offset:80\n"
      "ds_read_b128 v[72:75], v248 offset:96\n"
      "ds_read_b128 v[76:79], v248 offset:112\n"
      "ds_read_b128 v[80:83], v248 offset:128\n"
      "ds_read_b128 v[84:87], v248 offset:144\n"
      "ds_read_b128 v[88:91], v248 offset:160\n"
      "ds_read_b128 v[92:95], v248 offset:176\n"
      "ds_read_b128 v[96:99], v248 offset:192\n"
      "ds_read_b128 v[100:103], v248 offset:208\n"
      "ds_read_b128 v[104:107], v248 offset:224\n"
      "ds_read_b128 v[108:111], v248 offset:240\n"
      "v_lshrrev_b32 v253, 2, v1\n"
      "v_lshlrev_b32 v6, 0, v253\n"
      "v_lshl_add_u32 v6, v2, 2, v6\n"
      "v_lshrrev_b32 v253, 3, v6\n"
      "v_lshlrev_b32 v5, 0, v253\n"
      "v_and_b32 v253, 7, v6\n"
      "v_lshl_add_u32 v5, v253, 1, v5\n"
      "v_mul_u32_u24 v5, 1040, v5\n"
      "s_lshl_b32 s66, s48, 7\n"
      "v_and_b32_e64 v253, 3, v1\n"
      "v_lshl_add_u32 v5, v253, 3, v5\n"
      "v_add_u32_e64 v5, s66, v5\n"
      "v_add_u32 v6, 66560, v5\n"
      "v_mul_u32_u24 v7, 1040, v1\n"
      "v_lshl_add_u32 v7, v2, 8, v7\n"
      "v_add_u32 v7, v7, v252\n"
      "v_lshl_add_u32 v251, v1, 4, v252\n"
      "s_lshl_b32 s66, s49, 2\n"
      "v_add_u32 v250, s66, v251\n"
      "v_add_u32 v4, v3, v252\n"
      "s_lshl_b32 s66, s49, 10\n"
      "v_add_u32 v249, s66, v4\n"
      "ds_read_b128 v[112:115], v248 offset:16640\n"
      "ds_read_b128 v[116:119], v248 offset:16656\n"
      "ds_read_b128 v[120:123], v248 offset:16672\n"
      "ds_read_b128 v[124:127], v248 offset:16688\n"
      "ds_read_b128 v[128:131], v248 offset:16704\n"
      "ds_read_b128 v[132:135], v248 offset:16720\n"
      "ds_read_b128 v[136:139], v248 offset:16736\n"
      "ds_read_b128 v[140:143], v248 offset:16752\n"
      "ds_read_b128 v[144:147], v248 offset:16768\n"
      "ds_read_b128 v[148:151], v248 offset:16784\n"
      "ds_read_b128 v[152:155], v248 offset:16800\n"
      "ds_read_b128 v[156:159], v248 offset:16816\n"
      "ds_read_b128 v[160:163], v248 offset:16832\n"
      "ds_read_b128 v[164:167], v248 offset:16848\n"
      "ds_read_b128 v[168:171], v248 offset:16864\n"
      "ds_read_b128 v[172:175], v248 offset:16880\n"
      ".Lcw_172:\n"
      "ds_read_b32 v240, v252 offset:24356\n"
      "v_mov_b32 v253, v254\n"
      "v_mov_b32 v253, v254\n"
      "v_mov_b32 v253, v254\n"
      "v_mov_b32 v253, v254\n"
      "v_mov_b32 v253, v254\n"
      "v_mov_b32 v253, v254\n"
      "v_mov_b32 v253, v254\n"
      "v_mov_b32 v253, v254\n"
      "s_waitcnt lgkmcnt(0)\n"
      "v_cmp_gt_i32 vcc, 8, v240\n"
      "v_mov_b32 v253, v254\n"
      "v_mov_b32 v253, v254\n"
      "v_mov_b32 v253, v254\n"
      "v_mov_b32 v253, v254\n"
      "s_cbranch_vccnz .Lcw_172\n"
      "s_add_i32 s71, s61, 288\n"
      ".Lpa_173:\n"
      "s_memrealtime s[64:65]\n"
      "v_mov_b32 v253, v254\n"
      "v_mov_b32 v253, v254\n"
      "v_mov_b32 v253, v254\n"
      "v_mov_b32 v253, v254\n"
      "v_mov_b32 v253, v254\n"
      "v_mov_b32_e64 v253, v254\n"
      "v_mov_b32_e64 v253, v254\n"
      "v_mov_b32 v253, v254\n"
      "s_waitcnt lgkmcnt(0)\n"
      "s_sub_i32 s65, s64, s71\n"
      "s_cmp_lt_i32 s65, 0\n"
      "s_cbranch_scc1 .Lpa_173\n"
      "s_setprio 2\n"
      "s_waitcnt lgkmcnt(0)\n"
      "s_barrier\n"
      "ds_read_b128 v[176:179], v7\n"
      "ds_read_b128 v[180:183], v7 offset:16\n"
      "ds_read_b128 v[184:187], v7 offset:32\n"
      "ds_read_b128 v[188:191], v7 offset:48\n"
      "ds_read_b128 v[192:195], v7 offset:64\n"
      "ds_read_b128 v[196:199], v7 offset:80\n"
      "ds_read_b128 v[200:203], v7 offset:96\n"
      "ds_read_b128 v[204:207], v7 offset:112\n"
      "s_waitcnt lgkmcnt(7)\n"
      "v_mfma_f32_16x16x32_bf16 v[240:243], v[48:51], v[176:179], 0\n"
      "ds_read_b128 v[208:211], v7 offset:128\n"
      "v_mfma_f32_16x16x32_bf16 v[244:247], v[112:115], v[176:179], 0\n"
      "s_waitcnt lgkmcnt(7)\n"
      "v_mfma_f32_16x16x32_bf16 v[240:243], v[52:55], v[180:183], v[240:243]\n"
      "ds_read_b128 v[212:215], v7 offset:144\n"
      "v_mfma_f32_16x16x32_bf16 v[244:247], v[116:119], v[180:183], v[244:247]\n"
      "s_waitcnt lgkmcnt(7)\n"
      "v_mfma_f32_16x16x32_bf16 v[240:243], v[56:59], v[184:187], v[240:243]\n"
      "ds_read_b128 v[216:219], v7 offset:160\n"
      "v_mfma_f32_16x16x32_bf16 v[244:247], v[120:123], v[184:187], v[244:247]\n"
      "s_waitcnt lgkmcnt(7)\n"
      "v_mfma_f32_16x16x32_bf16 v[240:243], v[60:63], v[188:191], v[240:243]\n"
      "ds_read_b128 v[220:223], v7 offset:176\n"
      "v_mfma_f32_16x16x32_bf16 v[244:247], v[124:127], v[188:191], v[244:247]\n"
      "s_waitcnt lgkmcnt(7)\n"
      "v_mfma_f32_16x16x32_bf16 v[240:243], v[64:67], v[192:195], v[240:243]\n"
      "ds_read_b128 v[224:227], v7 offset:192\n"
      "v_mfma_f32_16x16x32_bf16 v[244:247], v[128:131], v[192:195], v[244:247]\n"
      "s_waitcnt lgkmcnt(7)\n"
      "v_mfma_f32_16x16x32_bf16 v[240:243], v[68:71], v[196:199], v[240:243]\n"
      "ds_read_b128 v[228:231], v7 offset:208\n"
      "v_mfma_f32_16x16x32_bf16 v[244:247], v[132:135], v[196:199], v[244:247]\n"
      "s_waitcnt lgkmcnt(7)\n"
      "v_mfma_f32_16x16x32_bf16 v[240:243], v[72:75], v[200:203], v[240:243]\n"
      "ds_read_b128 v[232:235], v7 offset:224\n"
      "v_mfma_f32_16x16x32_bf16 v[244:247], v[136:139], v[200:203], v[244:247]\n"
      "s_waitcnt lgkmcnt(7)\n"
      "v_mfma_f32_16x16x32_bf16 v[240:243], v[76:79], v[204:207], v[240:243]\n"
      "ds_read_b128 v[236:239], v7 offset:240\n"
      "v_mfma_f32_16x16x32_bf16 v[244:247], v[140:143], v[204:207], v[244:247]\n"
      "s_waitcnt lgkmcnt(7)\n"
      "v_mfma_f32_16x16x32_bf16 v[240:243], v[80:83], v[208:211], v[240:243]\n"
      "v_mfma_f32_16x16x32_bf16 v[244:247], v[144:147], v[208:211], v[244:247]\n"
      "s_waitcnt lgkmcnt(6)\n"
      "v_mfma_f32_16x16x32_bf16 v[240:243], v[84:87], v[212:215], v[240:243]\n"
      "v_mfma_f32_16x16x32_bf16 v[244:247], v[148:151], v[212:215], v[244:247]\n"
      "s_waitcnt lgkmcnt(5)\n"
      "v_mfma_f32_16x16x32_bf16 v[240:243], v[88:91], v[216:219], v[240:243]\n"
      "v_mfma_f32_16x16x32_bf16 v[244:247], v[152:155], v[216:219], v[244:247]\n"
      "s_waitcnt lgkmcnt(4)\n"
      "v_mfma_f32_16x16x32_bf16 v[240:243], v[92:95], v[220:223], v[240:243]\n"
      "v_mfma_f32_16x16x32_bf16 v[244:247], v[156:159], v[220:223], v[244:247]\n"
      "s_waitcnt lgkmcnt(3)\n"
      "v_mfma_f32_16x16x32_bf16 v[240:243], v[96:99], v[224:227], v[240:243]\n"
      "v_mfma_f32_16x16x32_bf16 v[244:247], v[160:163], v[224:227], v[244:247]\n"
      "s_waitcnt lgkmcnt(2)\n"
      "v_mfma_f32_16x16x32_bf16 v[240:243], v[100:103], v[228:231], v[240:243]\n"
      "v_mfma_f32_16x16x32_bf16 v[244:247], v[164:167], v[228:231], v[244:247]\n"
      "s_waitcnt lgkmcnt(1)\n"
      "v_mfma_f32_16x16x32_bf16 v[240:243], v[104:107], v[232:235], v[240:243]\n"
      "v_mfma_f32_16x16x32_bf16 v[244:247], v[168:171], v[232:235], v[244:247]\n"
      "s_waitcnt lgkmcnt(0)\n"
      "v_mfma_f32_16x16x32_bf16 v[240:243], v[108:111], v[236:239], v[240:243]\n"
      "v_mfma_f32_16x16x32_bf16 v[244:247], v[172:175], v[236:239], v[244:247]\n"
      "s_nop 6\n"
      "v_max3_f32 v248, v240, v241, v242\n"
      "v_max3_f32 v248, v248, v243, v244\n"
      "v_max3_f32 v248, v248, v245, v246\n"
      "v_max_f32 v248, v248, v247\n"
      "v_mov_b32 v253, v248\n"
      "s_nop 1\n"
      "v_permlane16_swap_b32 v248, v253\n"
      "v_max_f32 v248, v248, v253\n"
      "v_mov_b32 v253, v248\n"
      "s_nop 1\n"
      "v_permlane32_swap_b32 v248, v253\n"
      "v_max_f32 v248, v248, v253\n"
      "s_mov_b64 exec, 0xffff\n"
      "ds_write_b32 v250, v248 offset:20736\n"
      "s_mov_b64 exec, 0x1\n"
      "ds_add_u32 v252, v254 offset:24372\n"
      "s_mov_b64 exec, -1\n"
      "s_nop 8\n"
      ".Lcw_174:\n"
      "ds_read_b32 v208, v252 offset:24372\n"
      "ds_read_b128 v[216:219], v251 offset:20736\n"
      "s_waitcnt lgkmcnt(0)\n"
      "v_cmp_gt_i32_e64 vcc, 8, v208\n"
      "v_max3_f32 v212, v216, v217, v218\n"
      "v_max_f32_e64 v212, v212, v219\n"
      "v_pk_add_f32 v[220:221], v[240:241], v[212:213] op_sel_hi:[1,0] neg_lo:[0,1] neg_hi:[0,1]\n"
      "v_pk_add_f32 v[222:223], v[242:243], v[212:213] op_sel_hi:[1,0] neg_lo:[0,1] neg_hi:[0,1]\n"
      "v_pk_add_f32 v[224:225], v[244:245], v[212:213] op_sel_hi:[1,0] neg_lo:[0,1] neg_hi:[0,1]\n"
      "v_pk_add_f32 v[226:227], v[246:247], v[212:213] op_sel_hi:[1,0] neg_lo:[0,1] neg_hi:[0,1]\n"
      "s_cbranch_vccnz .Lcw_174\n"
      "v_exp_f32 v220, v220\n"
      "v_exp_f32 v221, v221\n"
      "v_exp_f32 v222, v222\n"
      "v_exp_f32_e64 v223, v223\n"
      "v_exp_f32_e64 v224, v224\n"
      "v_exp_f32_e64 v225, v225\n"
      "v_exp_f32_e64 v226, v226\n"
      "v_exp_f32 v227, v227\n"
      "v_cvt_pk_bf16_f32 v220, v220, v221\n"
      "v_cvt_pk_bf16_f32 v221, v222, v223\n"
      "v_cvt_pk_bf16_f32 v222, v224, v225\n"
      "v_cvt_pk_bf16_f32 v223, v226, v227\n"
      "ds_write_b128 v249, v[220:223] offset:16640\n"
      "v_mov_b32 v216, 0x3f803f80\n"
      "v_mov_b32 v217, 0x3f803f80\n"
      "v_mov_b32 v218, 0x3f803f80\n"
      "v_mov_b32 v219, 0x3f803f80\n"
      "s_setprio 1\n"
      "s_waitcnt lgkmcnt(0)\n"
      "s_barrier\n"
      "ds_read_b128 v[176:179], v4 offset:16640\n"
      "ds_read_b128 v[180:183], v4 offset:17664\n"
      "ds_read_b128 v[184:187], v4 offset:18688\n"
      "ds_read_b128 v[188:191], v4 offset:19712\n"
      "ds_read_b64_tr_b16 v[48:49], v5 offset:0\n"
      "ds_read_b64_tr_b16 v[52:53], v5 offset:32\n"
      "ds_read_b64_tr_b16 v[56:57], v5 offset:64\n"
      "ds_read_b64_tr_b16 v[60:61], v5 offset:96\n"
      "ds_read_b64_tr_b16 v[50:51], v5 offset:16640\n"
      "ds_read_b64_tr_b16 v[54:55], v5 offset:16672\n"
      "ds_read_b64_tr_b16 v[58:59], v5 offset:16704\n"
      "ds_read_b64_tr_b16 v[62:63], v5 offset:16736\n"
      "ds_read_b64_tr_b16 v[64:65], v5 offset:33280\n"
      "ds_read_b64_tr_b16 v[68:69], v5 offset:33312\n"
      "ds_read_b64_tr_b16 v[72:73], v5 offset:33344\n"
      "ds_read_b64_tr_b16 v[76:77], v5 offset:33376\n"
      "ds_read_b64_tr_b16 v[66:67], v5 offset:49920\n"
      "ds_read_b64_tr_b16 v[70:71], v5 offset:49952\n"
      "ds_read_b64_tr_b16 v[74:75], v5 offset:49984\n"
      "ds_read_b64_tr_b16 v[78:79], v5 offset:50016\n"
      "ds_read_b64_tr_b16 v[80:81], v6 offset:0\n"
      "ds_read_b64_tr_b16 v[84:85], v6 offset:32\n"
      "ds_read_b64_tr_b16 v[88:89], v6 offset:64\n"
      "ds_read_b64_tr_b16 v[92:93], v6 offset:96\n"
      "ds_read_b64_tr_b16 v[82:83], v6 offset:16640\n"
      "ds_read_b64_tr_b16 v[86:87], v6 offset:16672\n"
      "ds_read_b64_tr_b16 v[90:91], v6 offset:16704\n"
      "ds_read_b64_tr_b16 v[94:95], v6 offset:16736\n"
      "ds_read_b64_tr_b16 v[96:97], v6 offset:33280\n"
      "ds_read_b64_tr_b16 v[100:101], v6 offset:33312\n"
      "ds_read_b64_tr_b16 v[104:105], v6 offset:33344\n"
      "ds_read_b64_tr_b16 v[108:109], v6 offset:33376\n"
      "ds_read_b64_tr_b16 v[98:99], v6 offset:49920\n"
      "ds_read_b64_tr_b16 v[102:103], v6 offset:49952\n"
      "ds_read_b64_tr_b16 v[106:107], v6 offset:49984\n"
      "ds_read_b64_tr_b16 v[110:111], v6 offset:50016\n"
      "s_lshl_b32 s66, s58, 13\n"
      "s_xor_b32 s66, s66, s75\n"
      "v_lshlrev_b32 v5, 20, v2\n"
      "v_lshrrev_b32 v253, 1, v2\n"
      "v_lshl_add_u32 v5, v253, 13, v5\n"
      "v_lshl_add_u32 v5, v1, 3, v5\n"
      "v_xor_b32_e64 v5, s66, v5\n"
      "v_lshrrev_b32_e64 v253, 3, v1\n"
      "v_lshlrev_b32_e64 v6, 14, v253\n"
      "v_bfe_u32 v253, v1, 2, 1\n"
      "v_lshl_add_u32 v6, v253, 12, v6\n"
      "v_and_b32 v253, 3, v1\n"
      "v_lshl_add_u32 v6, v253, 3, v6\n"
      "s_cmp_lg_u32 s48, 7\n"
      "s_cbranch_scc1 .Lml_175\n"
      "s_waitcnt lgkmcnt(14)\n"
      "v_mfma_f32_16x16x32_bf16 v[192:195], v[216:219], v[176:179], 0\n"
      "v_mfma_f32_16x16x32_bf16 v[192:195], v[216:219], v[180:183], v[192:195]\n"
      "v_mfma_f32_16x16x32_bf16 v[192:195], v[216:219], v[184:187], v[192:195]\n"
      "v_mfma_f32_16x16x32_bf16 v[192:195], v[216:219], v[188:191], v[192:195]\n"
      "s_nop 7\n"
      "v_mov_b32 v213, v192\n"
      "s_mov_b64 exec, 0xffff\n"
      "buffer_store_dwordx2 v[212:213], v6, s[4:7], s89 offen sc1\n"
      "s_mov_b64 exec, -1\n"
      ".Lml_175:\n"
      "s_waitcnt lgkmcnt(14)\n"
      "v_mfma_f32_16x16x32_bf16 v[192:195], v[176:179], v[48:51], 0\n"
      "v_mfma_f32_16x16x32_bf16 v[196:199], v[176:179], v[52:55], 0\n"
      "v_mfma_f32_16x16x32_bf16 v[200:203], v[176:179], v[56:59], 0\n"
      "v_mfma_f32_16x16x32_bf16 v[204:207], v[176:179], v[60:63], 0\n"
      "s_waitcnt lgkmcnt(14)\n"
      "v_mfma_f32_16x16x32_bf16 v[192:195], v[180:183], v[64:67], v[192:195]\n"
      "v_mfma_f32_16x16x32_bf16 v[196:199], v[180:183], v[68:71], v[196:199]\n"
      "v_mfma_f32_16x16x32_bf16 v[200:203], v[180:183], v[72:75], v[200:203]\n"
      "v_mfma_f32_16x16x32_bf16 v[204:207], v[180:183], v[76:79], v[204:207]\n"
      "s_waitcnt lgkmcnt(8)\n"
      "v_mfma_f32_16x16x32_bf16 v[192:195], v[184:187], v[80:83], v[192:195]\n"
      "v_mfma_f32_16x16x32_bf16 v[196:199], v[184:187], v[84:87], v[196:199]\n"
      "v_mfma_f32_16x16x32_bf16 v[200:203], v[184:187], v[88:91], v[200:203]\n"
      "v_mfma_f32_16x16x32_bf16 v[204:207], v[184:187], v[92:95], v[204:207]\n"
      "s_waitcnt lgkmcnt(0)\n"
      "v_mfma_f32_16x16x32_bf16 v[192:195], v[188:191], v[96:99], v[192:195]\n"
      "v_mfma_f32_16x16x32_bf16 v[196:199], v[188:191], v[100:103], v[196:199]\n"
      "v_mfma_f32_16x16x32_bf16 v[200:203], v[188:191], v[104:107], v[200:203]\n"
      "v_mfma_f32_16x16x32_bf16 v[204:207], v[188:191], v[108:111], v[204:207]\n"
      "s_add_i32 s67, s88, 262144\n"
      "s_add_i32 s68, s88, 524288\n"
      "s_add_i32 s69, s88, 786432\n"
      "s_nop 2\n"
      "v_cvt_pk_bf16_f32 v48, v192, v196\n"
      "v_cvt_pk_bf16_f32 v50, v193, v197\n"
      "v_cvt_pk_bf16_f32 v49, v200, v204\n"
      "buffer_store_dwordx2 v[48:49], v5, s[4:7], s88 offen sc1\n"
      "v_cvt_pk_bf16_f32 v51, v201, v205\n"
      "buffer_store_dwordx2 v[50:51], v5, s[4:7], s67 offen sc1\n"
      "v_cvt_pk_bf16_f32 v52, v194, v198\n"
      "v_cvt_pk_bf16_f32 v53, v202, v206\n"
      "buffer_store_dwordx2 v[52:53], v5, s[4:7], s68 offen sc1\n"
      "v_cvt_pk_bf16_f32 v54, v195, v199\n"
      "v_cvt_pk_bf16_f32 v55, v203, v207\n"
      "buffer_store_dwordx2 v[54:55], v5, s[4:7], s69 offen sc1\n"
      "s_setprio 2\n"
      "v_lshl_add_u32 v5, v0, 2, v252\n"
      "ds_read_b32 v248, v5 offset:22016\n"
      "v_lshlrev_b32 v164, 8, v2\n"
      "v_lshrrev_b32_e64 v253, 3, v1\n"
      "v_lshl_add_u32 v164, v253, 7, v164\n"
      "v_and_b32 v253, 3, v1\n"
      "v_lshl_add_u32 v164, v253, 5, v164\n"
      "s_lshl_b32 s66, s49, 8\n"
      "v_lshl_add_u32 v165, v2, 6, v252\n"
      "v_and_b32 v253, 7, v1\n"
      "v_lshl_add_u32 v165, v253, 3, v165\n"
      "v_add_u32 v165, s66, v165\n"
      "v_lshrrev_b32 v253, 2, v1\n"
      "v_lshl_add_u32 v166, v253, 8, v252\n"
      "v_and_b32 v253, 1, v1\n"
      "v_lshl_add_u32 v166, v253, 7, v166\n"
      "v_lshl_add_u32 v166, v2, 4, v166\n"
      "v_lshlrev_b32 v167, 2, v0\n"
      "v_lshl_add_u32 v169, v0, 3, v252\n"
      "s_lshl_b32 s66, s49, 9\n"
      "v_add_u32 v168, s66, v169\n"
      "v_lshlrev_b32 v170, 15, v2\n"
      "v_lshrrev_b32 v253, 3, v1\n"
      "v_lshl_add_u32 v170, v253, 7, v170\n"
      "v_and_b32 v253, 7, v1\n"
      "v_lshl_add_u32 v170, v253, 2, v170\n"
      "v_and_b32 v253, 3, v1\n"
      "v_lshlrev_b32 v162, 0, v253\n"
      "v_and_b32 v163, 2, v2\n"
      "v_sub_u32 v163, v162, v163\n"
      "v_cmp_eq_u32 vcc, 0, v163\n"
      "s_nop 1\n"
      "v_cndmask_b32 v162, 0, -1, vcc\n"
      "v_cmp_eq_u32 vcc, 1, v163\n"
      "s_nop 1\n"
      "v_cndmask_b32 v163, 0, -1, vcc\n"
      "s_mov_b32 s99, 470\n"
      ".Lwe_176:\n"
      ".Lwt_177:\n"
      "s_memrealtime s[64:65]\n"
      "ds_read_b32 v253, v252 offset:24408\n"
      "v_mov_b32 v254, 1\n"
      "v_mov_b32 v254, 1\n"
      "v_mov_b32 v254, 1\n"
      "v_mov_b32 v254, 1\n"
      "v_mov_b32 v254, 1\n"
      "v_mov_b32 v254, 1\n"
      "v_mov_b32 v254, 1\n"
      "v_mov_b32 v254, 1\n"
      "s_waitcnt lgkmcnt(0)\n"
      "v_readfirstlane_b32 s61, v253\n"
      "s_sub_i32 s64, s64, s99\n"
      "s_sub_i32 s65, s61, s64\n"
      "s_cmp_gt_i32 s65, 16\n"
      "s_cbranch_scc1 .Lwt_177\n"
      "s_add_i32 s62, s61, s99\n"
      "s_cmp_lt_i32 s65, s78\n"
      "s_cbranch_scc1 .Lws_178\n"
      ".Lwf_179:\n"
      "s_memrealtime s[64:65]\n"
      "v_mov_b32 v253, v254\n"
      "v_mov_b32 v253, v254\n"
      "v_mov_b32 v253, v254\n"
      "v_mov_b32 v253, v254\n"
      "v_mov_b32 v253, v254\n"
      "v_mov_b32 v253, v254\n"
      "v_mov_b32 v253, v254\n"
      "v_mov_b32 v253, v254\n"
      "s_waitcnt lgkmcnt(0)\n"
      "s_sub_i32 s65, s62, s64\n"
      "s_cmp_gt_i32 s65, 3\n"
      "s_cbranch_scc1 .Lwf_179\n"
      "s_max_i32 s65, s65, 0\n"
      ".Lwd_180:\n"
      "s_sub_u32 s65, s65, 1\n"
      "s_cbranch_scc0 .Lwd_180\n"
      "buffer_load_dwordx2 v[112:113], v164, s[4:7], s91 offen sc1\n"
      "v_mov_b32 v253, v254\n"
      "v_mov_b32 v253, v254\n"
      "v_mov_b32 v253, v254\n"
      "v_mov_b32 v253, v254\n"
      "v_mov_b32 v253, v254\n"
      "v_mov_b32 v253, v254\n"
      "v_mov_b32 v253, v254\n"
      "v_mov_b32 v253, v254\n"
      "v_mov_b32 v253, v254\n"
      "v_mov_b32 v253, v254\n"
      "v_mov_b32 v253, v254\n"
      "v_mov_b32 v253, v254\n"
      "buffer_load_dwordx4 v[176:179], v3, s[4:7], s90 offen sc1\n"
      "buffer_load_dwordx4 v[180:183], v3, s[4:7], s90 offen offset:1024 sc1\n"
      "buffer_load_dwordx4 v[184:187], v3, s[4:7], s90 offen offset:2048 sc1\n"
      "buffer_load_dwordx4 v[188:191], v3, s[4:7], s90 offen offset:3072 sc1\n"
      "s_waitcnt vmcnt(4)\n"
      "v_max_u32_e64 v160, v112, v113\n"
      "v_max_f32_dpp v114, v112, v112 quad_perm:[1,0,3,2] row_mask:0xf bank_mask:0xf bound_ctrl:1\n"
      "s_nop 1\n"
      "v_max_f32_dpp v114, v114, v114 quad_perm:[2,3,0,1] row_mask:0xf bank_mask:0xf\n"
      "s_nop 1\n"
      "v_max_f32_dpp v114, v114, v114 row_mirror row_mask:0xf bank_mask:0xf\n"
      "v_mov_b32 v115, v114\n"
      "s_nop 1\n"
      "v_permlane16_swap_b32 v114, v115\n"
      "v_max_f32 v114, v114, v115\n"
      "v_mov_b32 v115, v114\n"
      "s_nop 1\n"
      "v_permlane32_swap_b32 v114, v115\n"
      "v_max_f32 v114, v114, v115\n"
      "v_sub_f32 v112, v112, v114\n"
      "v_exp_f32 v112, v112\n"
      "s_nop 0\n"
      "v_mul_f32 v113, v112, v113\n"
      "s_nop 1\n"
      "v_add_f32_dpp v113, v113, v113 quad_perm:[1,0,3,2] row_mask:0xf bank_mask:0xf\n"
      "s_nop 1\n"
      "v_add_f32_dpp v113, v113, v113 quad_perm:[2,3,0,1] row_mask:0xf bank_mask:0xf\n"
      "s_nop 1\n"
      "v_add_f32_dpp v113, v113, v113 row_mirror row_mask:0xf bank_mask:0xf\n"
      "v_mov_b32 v115, v113\n"
      "s_nop 1\n"
      "v_permlane16_swap_b32 v113, v115\n"
      "v_add_f32 v113, v113, v115\n"
      "v_mov_b32 v115, v113\n"
      "s_nop 1\n"
      "v_permlane32_swap_b32 v113, v115\n"
      "v_add_f32 v113, v113, v115\n"
      "v_rcp_f32 v113, v113\n"
      "s_nop 0\n"
      "v_mul_f32 v112, v112, v113\n"
      "v_cvt_pk_bf16_f32 v112, v112, v112\n"
      "s_nop 1\n"
      "v_and_b32_dpp v116, v112, v12 quad_perm:[0,0,0,0] row_mask:0xf bank_mask:0xf bound_ctrl:1\n"
      "v_and_b32_dpp v117, v112, v13 quad_perm:[0,0,0,0] row_mask:0xf bank_mask:0xf bound_ctrl:1\n"
      "v_and_b32_dpp v118, v112, v14 quad_perm:[0,0,0,0] row_mask:0xf bank_mask:0xf bound_ctrl:1\n"
      "v_and_b32_dpp v119, v112, v15 quad_perm:[0,0,0,0] row_mask:0xf bank_mask:0xf bound_ctrl:1\n"
      "v_and_b32_dpp v120, v112, v12 quad_perm:[1,1,1,1] row_mask:0xf bank_mask:0xf bound_ctrl:1\n"
      "v_and_b32_dpp v121, v112, v13 quad_perm:[1,1,1,1] row_mask:0xf bank_mask:0xf bound_ctrl:1\n"
      "v_and_b32_dpp v122, v112, v14 quad_perm:[1,1,1,1] row_mask:0xf bank_mask:0xf bound_ctrl:1\n"
      "v_and_b32_dpp v123, v112, v15 quad_perm:[1,1,1,1] row_mask:0xf bank_mask:0xf bound_ctrl:1\n"
      "v_and_b32_dpp v124, v112, v12 quad_perm:[2,2,2,2] row_mask:0xf bank_mask:0xf bound_ctrl:1\n"
      "v_and_b32_dpp v125, v112, v13 quad_perm:[2,2,2,2] row_mask:0xf bank_mask:0xf bound_ctrl:1\n"
      "v_and_b32_dpp v126, v112, v14 quad_perm:[2,2,2,2] row_mask:0xf bank_mask:0xf bound_ctrl:1\n"
      "v_and_b32_dpp v127, v112, v15 quad_perm:[2,2,2,2] row_mask:0xf bank_mask:0xf bound_ctrl:1\n"
      "v_and_b32_dpp v128, v112, v12 quad_perm:[3,3,3,3] row_mask:0xf bank_mask:0xf bound_ctrl:1\n"
      "v_and_b32_dpp v129, v112, v13 quad_perm:[3,3,3,3] row_mask:0xf bank_mask:0xf bound_ctrl:1\n"
      "v_and_b32_dpp v130, v112, v14 quad_perm:[3,3,3,3] row_mask:0xf bank_mask:0xf bound_ctrl:1\n"
      "v_and_b32_dpp v131, v112, v15 quad_perm:[3,3,3,3] row_mask:0xf bank_mask:0xf bound_ctrl:1\n"
      "s_waitcnt vmcnt(3)\n"
      "v_mfma_f32_16x16x32_bf16 v[136:139], v[176:179], v[116:119], 0\n"
      "v_max3_u32 v160, v160, v176, v177\n"
      "v_max3_u32 v160, v160, v178, v179\n"
      "s_waitcnt vmcnt(2)\n"
      "v_mfma_f32_16x16x32_bf16 v[136:139], v[180:183], v[120:123], v[136:139]\n"
      "v_max3_u32 v160, v160, v180, v181\n"
      "v_max3_u32 v160, v160, v182, v183\n"
      "s_waitcnt vmcnt(1)\n"
      "v_mfma_f32_16x16x32_bf16 v[136:139], v[184:187], v[124:127], v[136:139]\n"
      "v_max3_u32 v160, v160, v184, v185\n"
      "v_max3_u32 v160, v160, v186, v187\n"
      "s_waitcnt vmcnt(0)\n"
      "v_mfma_f32_16x16x32_bf16 v[136:139], v[188:191], v[128:131], v[136:139]\n"
      "v_max3_u32 v160, v160, v188, v189\n"
      "v_max3_u32 v160, v160, v190, v191\n"
      "v_cmp_eq_u32_e64 vcc, -1, v160\n"
      "s_cbranch_vccnz .Lsl_181\n"
      ".Lok_182:\n"
      "s_mov_b32 exec_lo, 0xff00ff\n"
      "s_mov_b32 exec_hi, 0xff00ff00\n"
      "s_nop 1\n"
      "v_cvt_pk_bf16_f32 v132, v136, v137\n"
      "v_cvt_pk_bf16_f32 v133, v138, v139\n"
      "ds_write_b64 v165, v[132:133] offset:20992\n"
      "s_mov_b64 exec, 0x1\n"
      "ds_add_u32 v252, v254 offset:24376\n"
      "s_mov_b64 exec, -1\n"
      ".Lcw_183:\n"
      "ds_read_b32 v112, v252 offset:24376\n"
      "ds_read_b128 v[176:179], v166 offset:20992\n"
      "ds_read_b128 v[180:183], v166 offset:21056\n"
      "s_waitcnt lgkmcnt(0)\n"
      "v_cmp_gt_i32 vcc, 8, v112\n"
      "v_mfma_f32_16x16x32_bf16 v[136:139], v[176:179], v[16:19], 0\n"
      "v_mfma_f32_16x16x32_bf16 v[140:143], v[176:179], v[20:23], 0\n"
      "s_cbranch_vccnz .Lcw_183\n"
      "v_mfma_f32_16x16x32_bf16 v[136:139], v[180:183], v[24:27], v[136:139]\n"
      "v_mfma_f32_16x16x32_bf16 v[140:143], v[180:183], v[28:31], v[140:143]\n"
      "s_mov_b32 s99, 560\n"
      "s_nop 5\n"
      "v_add_f32 v136, v136, v137\n"
      "v_add_f32 v140, v140, v141\n"
      "v_cvt_pk_bf16_f32 v132, v136, v140\n"
      "buffer_store_dword v132, v167, s[4:7], s92 offen sc1\n"
      ".Lwe_184:\n"
      ".Lwt_185:\n"
      "s_memrealtime s[64:65]\n"
      "ds_read_b32 v253, v252 offset:24408\n"
      "v_mov_b32 v254, 1\n"
      "v_mov_b32 v254, 1\n"
      "v_mov_b32 v254, 1\n"
      "v_mov_b32 v254, 1\n"
      "v_mov_b32 v254, 1\n"
      "v_mov_b32 v254, 1\n"
      "v_mov_b32 v254, 1\n"
      "v_mov_b32 v254, 1\n"
      "s_waitcnt lgkmcnt(0)\n"
      "v_readfirstlane_b32 s61, v253\n"
      "s_sub_i32 s64, s64, s99\n"
      "s_sub_i32 s65, s61, s64\n"
      "s_cmp_gt_i32 s65, 16\n"
      "s_cbranch_scc1 .Lwt_185\n"
      "s_add_i32 s62, s61, s99\n"
      "s_cmp_lt_i32 s65, s78\n"
      "s_cbranch_scc1 .Lws_186\n"
      ".Lwf_187:\n"
      "s_memrealtime s[64:65]\n"
      "v_mov_b32 v253, v254\n"
      "v_mov_b32 v253, v254\n"
      "v_mov_b32 v253, v254\n"
      "v_mov_b32 v253, v254\n"
      "v_mov_b32 v253, v254\n"
      "v_mov_b32_e64 v253, v254\n"
      "v_mov_b32_e64 v253, v254\n"
      "v_mov_b32 v253, v254\n"
      "s_waitcnt lgkmcnt(0)\n"
      "s_sub_i32 s65, s62, s64\n"
      "s_cmp_gt_i32 s65, 3\n"
      "s_cbranch_scc1 .Lwf_187\n"
      "s_max_i32 s65, s65, 0\n"
      ".Lwd_188:\n"
      "s_sub_u32 s65, s65, 1\n"
      "s_cbranch_scc0 .Lwd_188\n"
      "buffer_load_dwordx4 v[176:179], v3, s[4:7], s93 offen sc1\n"
      "buffer_load_dwordx4 v[180:183], v3, s[4:7], s93 offen offset:1024 sc1\n"
      "buffer_load_dwordx4 v[184:187], v3, s[4:7], s94 offen sc1\n"
      "buffer_load_dwordx4 v[188:191], v3, s[4:7], s94 offen offset:1024 sc1\n"
      "v_mov_b32_e64 v160, 0\n"
      "s_cmp_eq_u32 s54, s55\n"
      "s_cselect_b32 s66, -1, 0\n"
      "v_and_b32_e64 v250, s66, v248\n"
      "v_and_b32 v251, 0xffff0000, v250\n"
      "v_lshlrev_b32_e64 v250, 16, v250\n"
      "s_xor_b32 s67, s95, 131072\n"
      "s_xor_b32 s68, s95, 8192\n"
      "s_xor_b32 s69, s95, 139264\n"
      "s_waitcnt vmcnt(3)\n"
      "v_mfma_f32_16x16x32_bf16 v[144:147], v[8:11], v[176:179], 0\n"
      "v_max3_u32 v160, v160, v176, v177\n"
      "v_max3_u32 v160, v160, v178, v179\n"
      "s_waitcnt vmcnt(2)\n"
      "v_mfma_f32_16x16x32_bf16 v[144:147], v[8:11], v[180:183], v[144:147]\n"
      "v_max3_u32 v160, v160, v180, v181\n"
      "v_max3_u32 v160, v160, v182, v183\n"
      "s_waitcnt vmcnt(1)\n"
      "v_mfma_f32_16x16x32_bf16 v[148:151], v[8:11], v[184:187], 0\n"
      "v_max3_u32 v160, v160, v184, v185\n"
      "v_max3_u32 v160, v160, v186, v187\n"
      "s_nop 1\n"
      "v_cvt_pk_bf16_f32 v154, v144, v145\n"
      "v_cvt_pk_bf16_f32 v155, v146, v147\n"
      "v_and_b32 v152, v162, v154\n"
      "v_and_b32 v153, v162, v155\n"
      "v_and_b32 v154, v163, v154\n"
      "v_and_b32_e64 v155, v163, v155\n"
      "s_waitcnt vmcnt(0)\n"
      "v_mfma_f32_16x16x32_bf16 v[148:151], v[8:11], v[188:191], v[148:151]\n"
      "v_max3_u32 v160, v160, v188, v189\n"
      "v_max3_u32 v160, v160, v190, v191\n"
      "v_mfma_f32_16x16x32_bf16 v[136:139], v[152:155], v[32:35], 0\n"
      "v_mfma_f32_16x16x32_bf16 v[140:143], v[152:155], v[36:39], 0\n"
      "v_cmp_eq_u32 vcc, -1, v160\n"
      "s_nop 2\n"
      "v_cvt_pk_bf16_f32 v158, v148, v149\n"
      "v_cvt_pk_bf16_f32 v159, v150, v151\n"
      "v_and_b32 v156, v162, v158\n"
      "v_and_b32_e64 v157, v162, v159\n"
      "v_and_b32_e64 v158, v163, v158\n"
      "v_and_b32_e64 v159, v163, v159\n"
      "s_nop 1\n"
      "v_mfma_f32_16x16x32_bf16 v[136:139], v[156:159], v[40:43], v[136:139]\n"
      "v_mfma_f32_16x16x32_bf16 v[140:143], v[156:159], v[44:47], v[140:143]\n"
      "s_cbranch_vccnz .Lsl_189\n"
      ".Lok_190:\n"
      "s_nop 5\n"
      "v_pk_add_f32 v[136:137], v[136:137], v[138:139]\n"
      "v_add_f32_e64 v132, v136, v137\n"
      "v_pk_add_f32 v[140:141], v[140:141], v[142:143]\n"
      "v_add_f32_e64 v133, v140, v141\n"
      "ds_write_b64 v168, v[132:133] offset:28544\n"
      "s_mov_b64 exec, 0x1\n"
      "ds_add_rtn_u32 v112, v252, v254 offset:24380\n"
      "s_mov_b64 exec, -1\n"
      "ds_read_b64 v[136:137], v169 offset:28544\n"
      "ds_read_b64 v[138:139], v169 offset:29056\n"
      "ds_read_b64 v[140:141], v169 offset:29568\n"
      "ds_read_b64 v[142:143], v169 offset:30080\n"
      "s_waitcnt lgkmcnt(4)\n"
      "v_readfirstlane_b32 s66, v112\n"
      "s_waitcnt lgkmcnt(2)\n"
      "v_pk_add_f32 v[136:137], v[136:137], v[138:139]\n"
      "v_pk_add_f32 v[136:137], v[136:137], v[250:251]\n"
      "s_waitcnt lgkmcnt(0)\n"
      "v_pk_add_f32 v[140:141], v[140:141], v[142:143]\n"
      "v_pk_add_f32 v[136:137], v[136:137], v[140:141]\n"
      "s_cmp_lg_u32 s66, 7\n"
      "v_cvt_pk_bf16_f32 v136, v136, v137\n"
      "s_cbranch_scc1 .Lv2d_191\n"
      "buffer_store_dword v136, v170, s[4:7], s95 offen sc1\n"
      "buffer_store_dword v136, v170, s[4:7], s68 offen sc1\n"
      "buffer_store_dword v136, v170, s[4:7], s67 offen sc1\n"
      "buffer_store_dword v136, v170, s[4:7], s69 offen sc1\n"
      "s_mov_b64 exec, 0x1\n"
      "ds_add_u32 v252, v254 offset:24384\n"
      "s_mov_b64 exec, -1\n"
      ".Lv2d_191:\n"
      "s_setprio 0\n"
      "s_add_i32 s71, s61, 768\n"
      ".Lpa_192:\n"
      "s_memrealtime s[64:65]\n"
      "v_mov_b32 v253, v254\n"
      "v_mov_b32 v253, v254\n"
      "v_mov_b32 v253, v254\n"
      "v_mov_b32 v253, v254\n"
      "v_mov_b32 v253, v254\n"
      "v_mov_b32 v253, v254\n"
      "v_mov_b32 v253, v254\n"
      "v_mov_b32 v253, v254\n"
      "s_waitcnt lgkmcnt(0)\n"
      "s_sub_i32 s65, s64, s71\n"
      "s_cmp_lt_i32 s65, 0\n"
      "s_cbranch_scc1 .Lpa_192\n"
      "s_branch .Lend\n"
      ".Lsl_135:\n"
      "s_add_u32 s74, s74, 1\n"
      "s_cmp_gt_u32 s74, 0x186a0\n"
      "s_cbranch_scc1 .Lok_136\n"
      "s_sleep 6\n"
      "s_nop 9\n"
      "s_branch .Lgr_133\n"
      ".Lsl_144:\n"
      "s_add_u32 s74, s74, 1\n"
      "s_cmp_gt_u32 s74, 0x186a0\n"
      "s_cbranch_scc1 .Lok_145\n"
      "s_sleep 2\n"
      "s_nop 9\n"
      "s_branch .Lwe_139\n"
      ".Lsl_153:\n"
      "s_add_u32 s74, s74, 1\n"
      "s_cmp_gt_u32 s74, 0x186a0\n"
      "s_cbranch_scc1 .Lok_154\n"
      "s_sleep 2\n"
      "s_nop 9\n"
      "s_branch .Lwe_147\n"
      ".Lsl_166:\n"
      "s_add_u32 s74, s74, 1\n"
      "s_cmp_gt_u32 s74, 0x186a0\n"
      "s_cbranch_scc1 .Lok_167\n"
      "s_sleep 6\n"
      "s_nop 9\n"
      "s_branch .Lgr_164\n"
      ".Lws_178:\n"
      ".Lwg_193:\n"
      "s_add_i32 s61, s61, s79\n"
      "s_add_i32 s65, s65, s79\n"
      "s_cmp_lt_i32 s65, s78\n"
      "s_cbranch_scc1 .Lwg_193\n"
      "v_mov_b32 v253, s61\n"
      "s_mov_b64 exec, 0x1\n"
      "ds_max_u32 v252, v253 offset:24408\n"
      "s_mov_b64 exec, -1\n"
      "s_branch .Lwt_177\n"
      ".Lsl_181:\n"
      "s_add_u32 s74, s74, 1\n"
      "s_cmp_gt_u32 s74, 0x186a0\n"
      "s_cbranch_scc1 .Lok_182\n"
      "s_sleep 2\n"
      "s_nop 9\n"
      "s_branch .Lwe_176\n"
      ".Lws_186:\n"
      ".Lwg_194:\n"
      "s_add_i32 s61, s61, s79\n"
      "s_add_i32 s65, s65, s79\n"
      "s_cmp_lt_i32 s65, s78\n"
      "s_cbranch_scc1 .Lwg_194\n"
      "v_mov_b32 v253, s61\n"
      "s_mov_b64 exec, 0x1\n"
      "ds_max_u32 v252, v253 offset:24408\n"
      "s_mov_b64 exec, -1\n"
      "s_branch .Lwt_185\n"
      ".Lsl_189:\n"
      "s_add_u32 s74, s74, 1\n"
      "s_cmp_gt_u32 s74, 0x186a0\n"
      "s_cbranch_scc1 .Lok_190\n"
      "s_sleep 2\n"
      "s_nop 9\n"
      "s_branch .Lwe_184\n"
      ".Lend:\n"
      "s_endpgm\n"
      ".fill 384,4,0xbf800000\n"
      ".Lpc_end:\n"
      : "+{v0}"(tid)
      : "{s[0:1]}"(__builtin_amdgcn_kernarg_segment_ptr()), "{s2}"(blockIdx.x), "{s3}"((unsigned)(size_t)(__attribute__((address_space(3))) unsigned char*)smem)
      : "memory", "vcc", "s4", "s5", "s6", "s7", "s8", "s9", "s10", "s11", "s12", "s13", "s14", "s15", "s16", "s17", "s18", "s19", "s20", "s21", "s22", "s23", "s24", "s25", "s26", "s27", "s28", "s29", "s30", "s31", "s33", "s34", "s35", "s36", "s37", "s38", "s39", "s40", "s41", "s42", "s43", "s44", "s45", "s46", "s47", "s48", "s49", "s50", "s51", "s52", "s53", "s54", "s55", "s56", "s57", "s58", "s59", "s60", "s61", "s62", "s63", "s64", "s65", "s66", "s67", "s68", "s69", "s70", "s71", "s72", "s73", "s74", "s75", "s76", "s77", "s78", "s79", "s80", "s81", "s82", "s83", "s84", "s85", "s86", "s87", "s88", "s89", "s90", "s91", "s92", "s93", "s94", "s95", "s96", "s97", "s98", "s99", "v1", "v2", "v3", "v4", "v5", "v6", "v7", "v8", "v9", "v10", "v11", "v12", "v13", "v14", "v15", "v16", "v17", "v18", "v19", "v20", "v21", "v22", "v23", "v24", "v25", "v26", "v27", "v28", "v29", "v30", "v31", "v32", "v33", "v34", "v35", "v36", "v37", "v38", "v39", "v40", "v41", "v42", "v43", "v44", "v45", "v46", "v47", "v48", "v49", "v50", "v51", "v52", "v53", "v54", "v55", "v56", "v57", "v58", "v59", "v60", "v61", "v62", "v63", "v64", "v65", "v66", "v67", "v68", "v69", "v70", "v71", "v72", "v73", "v74", "v75", "v76", "v77", "v78", "v79", "v80", "v81", "v82", "v83", "v84", "v85", "v86", "v87", "v88", "v89", "v90", "v91", "v92", "v93", "v94", "v95", "v96", "v97", "v98", "v99", "v100", "v101", "v102", "v103", "v104", "v105", "v106", "v107", "v108", "v109", "v110", "v111", "v112", "v113", "v114", "v115", "v116", "v117", "v118", "v119", "v120", "v121", "v122", "v123", "v124", "v125", "v126", "v127", "v128", "v129", "v130", "v131", "v132", "v133", "v134", "v135", "v136", "v137", "v138", "v139", "v140", "v141", "v142", "v143", "v144", "v145", "v146", "v147", "v148", "v149", "v150", "v151", "v152", "v153", "v154", "v155", "v156", "v157", "v158", "v159", "v160", "v161", "v162", "v163", "v164", "v165", "v166", "v167", "v168", "v169", "v170", "v171", "v172", "v173", "v174", "v175", "v176", "v177", "v178", "v179", "v180", "v181", "v182", "v183", "v184", "v185", "v186", "v187", "v188", "v189", "v190", "v191", "v192", "v193", "v194", "v195", "v196", "v197", "v198", "v199", "v200", "v201", "v202", "v203", "v204", "v205", "v206", "v207", "v208", "v209", "v210", "v211", "v212", "v213", "v214", "v215", "v216", "v217", "v218", "v219", "v220", "v221", "v222", "v223", "v224", "v225", "v226", "v227", "v228", "v229", "v230", "v231", "v232", "v233", "v234", "v235", "v236", "v237", "v238", "v239", "v240", "v241", "v242", "v243", "v244", "v245", "v246", "v247", "v248", "v249", "v250", "v251", "v252", "v253", "v254", "v255");
}

extern "C" void kernel_launch(void* const* d_in, const int* in_sizes, int n_in, void* d_out, int out_size, void* d_ws, size_t ws_size,
                              hipStream_t stream) {
  Args a;
  const unsigned F = 0x00020000u;
  a.ws = {d_ws, 20971520u, F}; a.x = {d_in[0], 4096u, F}; a.wq = {d_in[2], 4194304u, F}; a.wuk = {d_in[3], 4194304u, F};
  a.ckv = {d_in[1], 16777216u, F}; a.wuv = {d_in[4], 4194304u, F}; a.wo = {d_in[5], 4194304u, F};
  a.wg = {d_in[7], 1835008u, F}; a.wu = {d_in[8], 1835008u, F}; a.wd = {d_in[9], 1835008u, F}; a.out = {d_out, 4096u, F};
  decode_block<<<256, 512, 0, stream>>>(a);
}
```
